```python
import torch

def _softmax(x, axis=-1):
    return torch.softmax(x, dim=axis)
def _gelu(x):
    return torch.nn.functional.gelu(x, approximate="tanh")
def _silu(x):
    return torch.nn.functional.silu(x)
def _sigmoid(x):
    return torch.sigmoid(x)
def _layernorm(x, axis=-1, eps=1e-5):
    m = x.mean(dim=axis, keepdim=True)
    v = x.var(dim=axis, keepdim=True, unbiased=False)
    return (x - m) / torch.sqrt(v + eps)
def _scatter_add(tgt, idx, src, axis=0):
    return tgt.clone().index_add_(axis, idx, src)
def _segment_sum(src, seg, n):
    out = torch.zeros((n,) + src.shape[1:], dtype=src.dtype)
    return out.index_add_(0, seg, src)
def _pad(x, pw):
    flat = tuple(v for pair in reversed(pw) for v in pair)
    return torch.nn.functional.pad(x, flat)

kernel_name = 'parallel_sum__ssm_conv_s5__glu_gated_swiglu__prompt'

def setup_inputs():

    _w = (lambda a: (lambda t: t.to(torch.bfloat16) if t.is_floating_point() else t)(torch.as_tensor(a)))
    def _A_setup():
        B, S, D = 1, 2048, 576
        P = D
        Lambda_re = -0.5 * (lambda _x: torch.exp(torch.as_tensor(_x)))((lambda *s: torch.randn(*s, dtype=torch.bfloat16) * 0.2)(P))
        Lambda_im = (lambda *s: torch.randn(*s, dtype=torch.bfloat16) * 0.8)(P) * 1.0
        B_re = (lambda *s: torch.randn(*s, dtype=torch.bfloat16) * 0.2)(P, D) * (D ** -0.5)
        B_im = (lambda *s: torch.randn(*s, dtype=torch.bfloat16) * 0.2)(P, D) * (D ** -0.5)
        C_re = (lambda *s: torch.randn(*s, dtype=torch.bfloat16) * 0.2)(D, P) * (P ** -0.5)
        C_im = (lambda *s: torch.randn(*s, dtype=torch.bfloat16) * 0.2)(D, P) * (P ** -0.5)
        log_dt = (lambda *s: torch.randn(*s, dtype=torch.bfloat16) * 0.2)(P) * 0.3 - 2.0
        Dskip = (lambda *s: torch.randn(*s, dtype=torch.bfloat16) * 0.05)(D)
        return {
            "x": _w((lambda *s: torch.randn(*s, dtype=torch.bfloat16) * 0.05)(B, S, D)),
            "Lambda_re": _w(Lambda_re),
            "Lambda_im": _w(Lambda_im),
            "B_re": _w(B_re),
            "B_im": _w(B_im),
            "C_re": _w(C_re),
            "C_im": _w(C_im),
            "log_dt": _w(log_dt),
            "Dskip": _w(Dskip),
        }
    _A_in = _A_setup()

    def _B_setup():
        B, S, D, F = 1, 2048, 576, 4608
        return {
            "x": _w((lambda *s: torch.randn(*s, dtype=torch.bfloat16) * 0.05)(B, S, D)),
            "w1": _w((lambda *s: torch.randn(*s, dtype=torch.bfloat16) * 0.4)(D, F) * (2.0 / D) ** 0.5),
            "w3": _w((lambda *s: torch.randn(*s, dtype=torch.bfloat16) * 0.4)(D, F) * (2.0 / D) ** 0.5),
            "w2": _w((lambda *s: torch.randn(*s, dtype=torch.bfloat16) * 0.4)(F, D) * (2.0 / F) ** 0.5),
        }
    _B_in = _B_setup()

    return {
        "A_x": _A_in["x"],
        "A_Lambda_re": _A_in["Lambda_re"],
        "A_Lambda_im": _A_in["Lambda_im"],
        "A_B_re": _A_in["B_re"],
        "A_B_im": _A_in["B_im"],
        "A_C_re": _A_in["C_re"],
        "A_C_im": _A_in["C_im"],
        "A_log_dt": _A_in["log_dt"],
        "A_Dskip": _A_in["Dskip"],
        "B_x": _B_in["x"],
        "B_w1": _B_in["w1"],
        "B_w3": _B_in["w3"],
        "B_w2": _B_in["w2"],
    }

def reference(A_x, A_Lambda_re, A_Lambda_im, A_B_re, A_B_im, A_C_re, A_C_im, A_log_dt, A_Dskip, B_x, B_w1, B_w3, B_w2):
    def _A_ref(x, Lambda_re, Lambda_im, B_re, B_im, C_re, C_im, log_dt, Dskip):
        Bsz, L, H = x.shape
        Lam = Lambda_re + 1j * Lambda_im
        Bmat = B_re + 1j * B_im
        Cmat = C_re + 1j * C_im
        dt = torch.exp(log_dt)
        Lam_bar = torch.exp(dt * Lam)
        B_bar = ((Lam_bar - 1.0) / Lam)[:, None] * Bmat
        Bu = torch.einsum("bld,pd->blp", x + 0j, B_bar)
        h = torch.zeros_like(Bu[:, 0, :])
        hs = []
        for t in range(L):
            h = Lam_bar[None, :] * h + Bu[:, t, :]
            hs.append(h)
        hseq = torch.stack(hs, 1)
        y = 2.0 * torch.einsum("blp,dp->bld", hseq, Cmat).real + x * Dskip[None, None, :]
        return y

    def _B_ref(x, w1, w3, w2):
        gate = torch.einsum("bsd,df->bsf", x, w1)
        up = torch.einsum("bsd,df->bsf", x, w3)
        h = _silu(gate) * up
        out = torch.einsum("bsf,fd->bsd", h, w2)
        return out

    x = A_x
    xn = _layernorm(x, axis=-1)
    return x + _A_ref(xn, A_Lambda_re, A_Lambda_im, A_B_re, A_B_im, A_C_re, A_C_im, A_log_dt, A_Dskip) + _B_ref(xn, B_w1, B_w3, B_w2)
```

```cpp
#include <hip/hip_runtime.h>
#include <hip/hip_bf16.h>

constexpr int L = 2048, D = 576, P = 576, F = 4608;
typedef unsigned short u16;

__device__ __forceinline__ float bf2f(u16 v) { return __uint_as_float(((unsigned)v) << 16); }
__device__ __forceinline__ u16 f2bf(float f) { __bf16 b = (__bf16)f; return __builtin_bit_cast(u16, b); }
__device__ __forceinline__ float rbf(float f) { return bf2f(f2bf(f)); }

__global__ void ln_kernel(const u16* x, u16* xn) {
  int t = blockIdx.x; int lane = threadIdx.x;
  float v[9]; float s = 0;
  for (int i = 0; i < 9; ++i) { v[i] = bf2f(x[t * D + i * 64 + lane]); s += v[i]; }
  for (int o = 32; o; o >>= 1) s += __shfl_xor(s, o);
  float m = s / D; float q = 0;
  for (int i = 0; i < 9; ++i) { float d = v[i] - m; q += d * d; }
  for (int o = 32; o; o >>= 1) q += __shfl_xor(q, o);
  float mb = rbf(m), vb = rbf(q / D);
  float sd = rbf(sqrtf(rbf(vb + 1e-5f)));
  for (int i = 0; i < 9; ++i) xn[t * D + i * 64 + lane] = f2bf(rbf(v[i] - mb) / sd);
}

__global__ void gemm1_kernel(const u16* xn, const u16* w1, const u16* w3, u16* h) {
  int f = blockIdx.x * 64 + threadIdx.x; int t0 = blockIdx.y * 8;
  float g[8] = {}, u[8] = {};
  for (int k = 0; k < D; ++k) {
    float a = bf2f(w1[k * F + f]), b = bf2f(w3[k * F + f]);
    for (int i = 0; i < 8; ++i) { float xv = bf2f(xn[(t0 + i) * D + k]); g[i] += xv * a; u[i] += xv * b; }
  }
  for (int i = 0; i < 8; ++i) { float s = g[i] / (1.f + __expf(-g[i])); h[(t0 + i) * F + f] = f2bf(s * u[i]); }
}

__global__ void bu_kernel(const u16* xn, const u16* bre, const u16* bim, float2* z) {
  int p = blockIdx.x * 64 + threadIdx.x; int t0 = blockIdx.y * 8;
  float g[8] = {}, u[8] = {};
  for (int k = 0; k < D; ++k) {
    float a = bf2f(bre[p * D + k]), b = bf2f(bim[p * D + k]);
    for (int i = 0; i < 8; ++i) { float xv = bf2f(xn[(t0 + i) * D + k]); g[i] += xv * a; u[i] += xv * b; }
  }
  for (int i = 0; i < 8; ++i) z[(t0 + i) * P + p] = make_float2(g[i], u[i]);
}

__global__ void scan_kernel(const u16* lre, const u16* lim, const u16* logdt, float2* z) {
  int p = blockIdx.x * 64 + threadIdx.x;
  float a = bf2f(lre[p]), b = bf2f(lim[p]);
  float dt = rbf(expf(bf2f(logdt[p])));
  float e = expf(dt * a); float lr = e * cosf(dt * b), li = e * sinf(dt * b);
  float nr = lr - 1.f, ni = li; float den = a * a + b * b;
  float cr = (nr * a + ni * b) / den, ci = (ni * a - nr * b) / den;
  float hr = 0, hi = 0;
  for (int t = 0; t < L; ++t) {
    float2 v = z[t * P + p];
    float br = cr * v.x - ci * v.y, bi = cr * v.y + ci * v.x;
    float nhr = lr * hr - li * hi + br, nhi = lr * hi + li * hr + bi;
    hr = nhr; hi = nhi; z[t * P + p] = make_float2(hr, hi);
  }
}

__global__ void out_kernel(const u16* x, const u16* xn, const u16* h, const u16* w2, const float2* z,
                           const u16* cre, const u16* cim, const u16* dskip, float* out) {
  int d = blockIdx.x * 64 + threadIdx.x; int t0 = blockIdx.y * 8;
  float o[8] = {}, y[8] = {};
  for (int k = 0; k < F; ++k) { float a = bf2f(w2[k * D + d]);
    for (int i = 0; i < 8; ++i) o[i] += bf2f(h[(t0 + i) * F + k]) * a; }
  for (int k = 0; k < P; ++k) { float a = bf2f(cre[d * P + k]), b = bf2f(cim[d * P + k]);
    for (int i = 0; i < 8; ++i) { float2 v = z[(t0 + i) * P + k]; y[i] += v.x * a - v.y * b; } }
  float ds = bf2f(dskip[d]);
  for (int i = 0; i < 8; ++i) { int idx = (t0 + i) * D + d;
    out[idx] = bf2f(x[idx]) + 2.f * y[i] + bf2f(xn[idx]) * ds + o[i]; }
}

extern "C" void kernel_launch(void* const* d_in, const int* in_sizes, int n_in, void* d_out, int out_size,
                              void* d_ws, size_t ws_size, hipStream_t stream) {
  const u16* x = (const u16*)d_in[0];
  const u16* lre = (const u16*)d_in[1]; const u16* lim = (const u16*)d_in[2];
  const u16* bre = (const u16*)d_in[3]; const u16* bim = (const u16*)d_in[4];
  const u16* cre = (const u16*)d_in[5]; const u16* cim = (const u16*)d_in[6];
  const u16* logdt = (const u16*)d_in[7]; const u16* dskip = (const u16*)d_in[8];
  const u16* w1 = (const u16*)d_in[10]; const u16* w3 = (const u16*)d_in[11]; const u16* w2 = (const u16*)d_in[12];
  char* ws = (char*)d_ws;
  u16* xn = (u16*)ws; u16* h = (u16*)(ws + (4 << 20)); float2* z = (float2*)(ws + (32 << 20));
  ln_kernel<<<L, 64, 0, stream>>>(x, xn);
  gemm1_kernel<<<dim3(F / 64, L / 8), 64, 0, stream>>>(xn, w1, w3, h);
  bu_kernel<<<dim3(P / 64, L / 8), 64, 0, stream>>>(xn, bre, bim, z);
  scan_kernel<<<P / 64, 64, 0, stream>>>(lre, lim, logdt, z);
  out_kernel<<<dim3(D / 64, L / 8), 64, 0, stream>>>(x, xn, h, w2, z, cre, cim, dskip, (float*)d_out);
}
```

```cpp
#include <hip/hip_runtime.h>
#include <hip/hip_bf16.h>
#include <cstdio>

constexpr int L = 2048, D = 576, P = 576, F = 4608;
typedef unsigned short u16;

__device__ __forceinline__ float bf2f(u16 v) { return __uint_as_float(((unsigned)v) << 16); }
__device__ __forceinline__ u16 f2bf(float f) { __bf16 b = (__bf16)f; return __builtin_bit_cast(u16, b); }
__device__ __forceinline__ float rbf(float f) { return bf2f(f2bf(f)); }

__global__ void ln_kernel(const u16* x, u16* xn) {
  int t = blockIdx.x; int lane = threadIdx.x;
  float v[9]; float s = 0;
  for (int i = 0; i < 9; ++i) { v[i] = bf2f(x[t * D + i * 64 + lane]); s += v[i]; }
  for (int o = 32; o; o >>= 1) s += __shfl_xor(s, o);
  float m = s / D; float q = 0;
  for (int i = 0; i < 9; ++i) { float d = v[i] - m; q += d * d; }
  for (int o = 32; o; o >>= 1) q += __shfl_xor(q, o);
  float mb = rbf(m), vb = rbf(q / D);
  float sd = rbf(sqrtf(rbf(vb + 1e-5f)));
  for (int i = 0; i < 9; ++i) xn[t * D + i * 64 + lane] = f2bf(rbf(v[i] - mb) / sd);
}

__global__ void gemm1_kernel(const u16* xn, const u16* w1, const u16* w3, u16* h) {
  int f = blockIdx.x * 64 + threadIdx.x; int t0 = blockIdx.y * 8;
  float g[8] = {}, u[8] = {};
  for (int k = 0; k < D; ++k) {
    float a = bf2f(w1[k * F + f]), b = bf2f(w3[k * F + f]);
    for (int i = 0; i < 8; ++i) { float xv = bf2f(xn[(t0 + i) * D + k]); g[i] += xv * a; u[i] += xv * b; }
  }
  for (int i = 0; i < 8; ++i) { float s = g[i] / (1.f + __expf(-g[i])); h[(t0 + i) * F + f] = f2bf(s * u[i]); }
}

__global__ void bu_kernel(const u16* xn, const u16* bre, const u16* bim, float2* z) {
  int p = blockIdx.x * 64 + threadIdx.x; int t0 = blockIdx.y * 8;
  float g[8] = {}, u[8] = {};
  for (int k = 0; k < D; ++k) {
    float a = bf2f(bre[p * D + k]), b = bf2f(bim[p * D + k]);
    for (int i = 0; i < 8; ++i) { float xv = bf2f(xn[(t0 + i) * D + k]); g[i] += xv * a; u[i] += xv * b; }
  }
  for (int i = 0; i < 8; ++i) z[(t0 + i) * P + p] = make_float2(g[i], u[i]);
}

__global__ void scan_kernel(const u16* lre, const u16* lim, const u16* logdt, float2* z) {
  int p = blockIdx.x * 64 + threadIdx.x;
  float a = bf2f(lre[p]), b = bf2f(lim[p]);
  float dt = rbf(expf(bf2f(logdt[p])));
  float e = expf(dt * a); float lr = e * cosf(dt * b), li = e * sinf(dt * b);
  float nr = lr - 1.f, ni = li; float den = a * a + b * b;
  float cr = (nr * a + ni * b) / den, ci = (ni * a - nr * b) / den;
  float hr = 0, hi = 0;
  for (int t = 0; t < L; ++t) {
    float2 v = z[t * P + p];
    float br = cr * v.x - ci * v.y, bi = cr * v.y + ci * v.x;
    float nhr = lr * hr - li * hi + br, nhi = lr * hi + li * hr + bi;
    hr = nhr; hi = nhi; z[t * P + p] = make_float2(hr, hi);
  }
}

__global__ void out_kernel(const u16* x, const u16* xn, const u16* h, const u16* w2, const float2* z,
                           const u16* cre, const u16* cim, const u16* dskip, float* out) {
  int d = blockIdx.x * 64 + threadIdx.x; int t0 = blockIdx.y * 8;
  float o[8] = {}, y[8] = {};
  for (int k = 0; k < F; ++k) { float a = bf2f(w2[k * D + d]);
    for (int i = 0; i < 8; ++i) o[i] += bf2f(h[(t0 + i) * F + k]) * a; }
  for (int k = 0; k < P; ++k) { float a = bf2f(cre[d * P + k]), b = bf2f(cim[d * P + k]);
    for (int i = 0; i < 8; ++i) { float2 v = z[(t0 + i) * P + k]; y[i] += v.x * a - v.y * b; } }
  float ds = bf2f(dskip[d]);
  for (int i = 0; i < 8; ++i) { int idx = (t0 + i) * D + d;
    out[idx] = bf2f(x[idx]) + 2.f * y[i] + bf2f(xn[idx]) * ds + o[i]; }
}


__global__ __launch_bounds__(256) void probe_kernel(const uint4* w1, const uint4* w3, const uint4* w2, const uint4* x, int shift, int id, uint4* sink) {
  int b = (blockIdx.x + shift) & 255;
  unsigned long long t0 = __builtin_amdgcn_s_memrealtime();
  uint4 f = x[b * 576 + threadIdx.x];
  asm volatile("s_waitcnt vmcnt(0)" ::: "memory");
  unsigned acc = f.x;
  asm volatile("" : "+v"(acc));
  unsigned long long t1 = __builtin_amdgcn_s_memrealtime();
  uint4 v[15];
  for (int i = 0; i < 5; ++i) { v[i] = w1[b * 1296 + i * 256 + threadIdx.x]; v[5 + i] = w3[b * 1296 + i * 256 + threadIdx.x]; v[10 + i] = w2[b * 1296 + i * 256 + threadIdx.x]; }
  for (int i = 0; i < 15; ++i) acc ^= v[i].x ^ v[i].y ^ v[i].z ^ v[i].w;
  asm volatile("" : "+v"(acc));
  unsigned long long t2 = __builtin_amdgcn_s_memrealtime();
  if (acc == 0x12345678) sink[0].x = acc;
  if (threadIdx.x == 0 && (blockIdx.x == 5 || blockIdx.x == 130)) printf("probe %d blk %d first %d bulk %d\n", id, blockIdx.x, (int)(t1 - t0), (int)(t2 - t1));
}
extern "C" void kernel_launch(void* const* d_in, const int* in_sizes, int n_in, void* d_out, int out_size,
                              void* d_ws, size_t ws_size, hipStream_t stream) {
  const u16* x = (const u16*)d_in[0];
  const u16* lre = (const u16*)d_in[1]; const u16* lim = (const u16*)d_in[2];
  const u16* bre = (const u16*)d_in[3]; const u16* bim = (const u16*)d_in[4];
  const u16* cre = (const u16*)d_in[5]; const u16* cim = (const u16*)d_in[6];
  const u16* logdt = (const u16*)d_in[7]; const u16* dskip = (const u16*)d_in[8];
  const u16* w1 = (const u16*)d_in[10]; const u16* w3 = (const u16*)d_in[11]; const u16* w2 = (const u16*)d_in[12];
  char* ws = (char*)d_ws;
  u16* xn = (u16*)ws; u16* h = (u16*)(ws + (4 << 20)); float2* z = (float2*)(ws + (32 << 20));
  probe_kernel<<<256, 256, 0, stream>>>((const uint4*)w1, (const uint4*)w3, (const uint4*)w2, (const uint4*)x, 0, 0, (uint4*)(ws + (64 << 20)));
  probe_kernel<<<256, 256, 0, stream>>>((const uint4*)w1, (const uint4*)w3, (const uint4*)w2, (const uint4*)x, 1, 1, (uint4*)(ws + (64 << 20)));
  probe_kernel<<<256, 256, 0, stream>>>((const uint4*)w1, (const uint4*)w3, (const uint4*)w2, (const uint4*)x, 1, 2, (uint4*)(ws + (64 << 20)));
  ln_kernel<<<L, 64, 0, stream>>>(x, xn);
  gemm1_kernel<<<dim3(F / 64, L / 8), 64, 0, stream>>>(xn, w1, w3, h);
  bu_kernel<<<dim3(P / 64, L / 8), 64, 0, stream>>>(xn, bre, bim, z);
  scan_kernel<<<P / 64, 64, 0, stream>>>(lre, lim, logdt, z);
  out_kernel<<<dim3(D / 64, L / 8), 64, 0, stream>>>(x, xn, h, w2, z, cre, cim, dskip, (float*)d_out);
}
```

```cpp
#include <hip/hip_runtime.h>

typedef unsigned short u16;

typedef int i32x4 __attribute__((ext_vector_type(4)));

__device__ __forceinline__ void touch(const void* p, int bytes, int voff, int soff) {
  i32x4 d = {(int)(size_t)p, (int)((size_t)p >> 32), bytes, 0x00020000};
  asm volatile("buffer_load_dword v255, %0, %1, %2 offen" :: "v"(voff), "s"(d), "s"(soff) : "v255", "memory");
}

__global__ __launch_bounds__(256, 1) void fused_kernel(const u16* x, const u16* lre, const u16* lim, const u16* bre, const u16* bim,
                                                       const u16* cre, const u16* cim, const u16* ldt, const u16* dsk,
                                                       const u16* w1, const u16* w3, const u16* w2, float* out, char* ws) {
  __shared__ char lds[163840];
  int j = blockIdx.x & 7, idx = blockIdx.x >> 3;
  int q = j >> 1, lo = (117 * q + 3) / 4, hi = (117 * (q + 1) + 3) / 4;
  if (__builtin_expect(idx >= hi - lo, 0)) {
    const int l = threadIdx.x & 63, nw = (32 - (hi - lo)) * 4;
    const int gw = (idx - (hi - lo)) * 4 + __builtin_amdgcn_readfirstlane(threadIdx.x >> 6);
    const int c0 = (int)(24859717636160ull >> (5 * j)) & 31, c1 = (int)(24859717636160ull >> (5 * j + 5)) & 31, oob = 0x40000000;
    {
      unsigned long long pc; int len;
      asm volatile("s_getpc_b64 %0\n.Lpf_pc:\ns_mov_b32 %1, .Lfz_start-.Lpf_pc" : "=s"(pc), "=s"(len));
      pc = (pc + len) & ~127ull;
      asm volatile("s_mov_b32 %0, .Lfz_end-.Lfz_start" : "=s"(len));
      for (int i = gw * 8192; i < len; i += nw * 8192) touch((const void*)pc, len, l * 128, i);
    }
    for (int i = 0; i < 2; i++) __builtin_amdgcn_s_sleep(16);
    {
      const int v = l < 60 ? l / 5 * 1152 + l % 5 * 128 + 512 : oob;
      for (int r = 576 * c0 / 22 + gw * 12; r < 576 * c1 / 22; r += nw * 12) { touch(bre, 663552, v, r * 1152); touch(bim, 663552, v, r * 1152); }
    }
    {
      const int v = l * 9216 + ((4 - (int)((size_t)w1 >> 7)) & 7) * 128;
      for (int r = 81 * c0 / 22 + gw; r < 81 * c1 / 22; r += nw) { touch(w1, 5308416, v, r % 9 * 589824 + r / 9 * 1024); touch(w3, 5308416, v, r % 9 * 589824 + r / 9 * 1024); }
    }
    {
      const int v = l * 9216 + ((5 - (int)((size_t)w1 >> 7)) & 7) * 128;
      for (int r = 81 * c0 / 22 + gw; r < 81 * c1 / 22; r += nw) { touch(w1, 5308416, v, r % 9 * 589824 + r / 9 * 1024); touch(w3, 5308416, v, r % 9 * 589824 + r / 9 * 1024); }
    }
    {
      const int v = l * 9216 + ((6 - (int)((size_t)w1 >> 7)) & 7) * 128;
      for (int r = 81 * c0 / 22 + gw; r < 81 * c1 / 22; r += nw) { touch(w1, 5308416, v, r % 9 * 589824 + r / 9 * 1024); touch(w3, 5308416, v, r % 9 * 589824 + r / 9 * 1024); }
    }
    {
      const int v = l * 9216 + ((7 - (int)((size_t)w1 >> 7)) & 7) * 128;
      for (int r = 81 * c0 / 22 + gw; r < 81 * c1 / 22; r += nw) { touch(w1, 5308416, v, r % 9 * 589824 + r / 9 * 1024); touch(w3, 5308416, v, r % 9 * 589824 + r / 9 * 1024); }
    }
    {
      const int v = l * 9216 + ((0 - (int)((size_t)w1 >> 7)) & 7) * 128;
      for (int r = 81 * c0 / 22 + gw; r < 81 * c1 / 22; r += nw) { touch(w1, 5308416, v, r % 9 * 589824 + r / 9 * 1024); touch(w3, 5308416, v, r % 9 * 589824 + r / 9 * 1024); }
    }
    {
      const int v = l * 9216 + ((1 - (int)((size_t)w1 >> 7)) & 7) * 128;
      for (int r = 81 * c0 / 22 + gw; r < 81 * c1 / 22; r += nw) { touch(w1, 5308416, v, r % 9 * 589824 + r / 9 * 1024); touch(w3, 5308416, v, r % 9 * 589824 + r / 9 * 1024); }
    }
    {
      const int v = l * 9216 + ((2 - (int)((size_t)w1 >> 7)) & 7) * 128;
      for (int r = 81 * c0 / 22 + gw; r < 81 * c1 / 22; r += nw) { touch(w1, 5308416, v, r % 9 * 589824 + r / 9 * 1024); touch(w3, 5308416, v, r % 9 * 589824 + r / 9 * 1024); }
    }
    {
      const int v = l * 9216 + ((3 - (int)((size_t)w1 >> 7)) & 7) * 128;
      for (int r = 81 * c0 / 22 + gw; r < 81 * c1 / 22; r += nw) { touch(w1, 5308416, v, r % 9 * 589824 + r / 9 * 1024); touch(w3, 5308416, v, r % 9 * 589824 + r / 9 * 1024); }
    }
    if (gw < 4) touch(ws, 22614016, gw * 64 + l < 234 ? (gw * 64 + l) * 92160 + 1135616 : oob, 0);
    for (int r = 81 * c0 / 22 + gw; r < 81 * c1 / 22; r += nw) { touch(cre, 663552, l * 128, r * 8192); touch(cim, 663552, l * 128, r * 8192); }
    for (int r = 648 * c0 / 22 + gw; r < 648 * c1 / 22; r += nw) touch(w2, 5308416, l * 128, r * 8192);
    asm volatile("s_waitcnt vmcnt(0)" :: "s"(out), "s"(ws), "s"(x), "s"(dsk) : "memory");
    return;
  }
  int lw = lo + idx, sl = lw / 13, ti = 13 * (j & 1) + lw % 13, cs = (sl + 27 - ti) % 9;
  int tid = threadIdx.x;
  asm volatile(
".Lfz_start:\n"
"s_mov_b32 s38, 65536\n"
"v_lshrrev_b32 v1, 6, v0\n"
"s_nop 0\n"
"v_readfirstlane_b32 s95, v1\n"
"v_and_b32 v0, 63, v0\n"
"s_mul_i32 s98, %[ti], 92160\n"
"s_sub_u32 s99, 2359296, s98\n"
"s_mov_b64 s[60:61], %[x]\n"
"s_add_u32 s60, s60, s98\n"
"s_addc_u32 s61, s61, 0\n"
"s_mov_b32 s62, s99\n"
"s_mov_b32 s63, 0x00020000\n"
"v_lshrrev_b32 v20, 3, v0\n"
"v_and_b32 v21, 7, v0\n"
"s_mul_i32 s98, s95, 23040\n"
"v_mul_u32_u24 v24, 1152, v20\n"
"v_lshl_add_u32 v24, v21, 4, v24\n"
"v_add_u32 v24, s98, v24\n"
"buffer_load_dwordx4 v[56:59], v24, s[60:63], 0 offen offset:0\n"
"buffer_load_dwordx4 v[60:63], v24, s[60:63], 0 offen offset:128\n"
"buffer_load_dwordx4 v[64:67], v24, s[60:63], 0 offen offset:256\n"
"buffer_load_dwordx4 v[68:71], v24, s[60:63], 0 offen offset:384\n"
"buffer_load_dwordx4 v[72:75], v24, s[60:63], 0 offen offset:512\n"
"buffer_load_dwordx4 v[76:79], v24, s[60:63], 0 offen offset:640\n"
"buffer_load_dwordx4 v[80:83], v24, s[60:63], 0 offen offset:768\n"
"buffer_load_dwordx4 v[84:87], v24, s[60:63], 0 offen offset:896\n"
"buffer_load_dwordx4 v[88:91], v24, s[60:63], 0 offen offset:1024\n"
"v_lshrrev_b32 v22, 4, v0\n"
"v_and_b32 v23, 15, v0\n"
"s_mov_b64 s[64:65], %[ws]\n"
"s_mov_b32 s66, 22614016\n"
"s_mov_b32 s67, 0x00020000\n"
"s_mov_b64 s[40:41], %[bre]\n"
"s_mov_b32 s42, 663552\n"
"s_mov_b32 s43, 0x00020000\n"
"s_mov_b64 s[44:45], %[bim]\n"
"s_sub_u32 s44, s44, 1024\n"
"s_subb_u32 s45, s45, 0\n"
"s_mov_b32 s46, 664576\n"
"s_mov_b32 s47, 0x00020000\n"
"s_lshl_b32 s97, s95, 12\n"
"s_mul_i32 s98, s95, 18432\n"
"v_xor_b32 v30, v21, v22\n"
"v_mul_u32_u24 v3, 1152, v20\n"
"v_add_u32 v3, s98, v3\n"
"v_add_u32 v4, 7168, v3\n"
"v_add_u32 v25, 9216, v24\n"
"v_lshl_add_u32 v3, v30, 4, v3\n"
"v_xor_b32 v30, 4, v30\n"
"v_lshl_add_u32 v4, v30, 4, v4\n"
"s_lshl_b32 s98, %[sl], 6\n"
"s_lshl_b32 s99, s95, 4\n"
"s_add_u32 s98, s98, s99\n"
"v_add_u32 v19, s98, v23\n"
"v_lshlrev_b32 v19, 1, v19\n"
"s_mov_b64 s[48:49], %[w1]\n"
"s_mov_b32 s50, 5308416\n"
"s_mov_b32 s51, 0x00020000\n"
"s_mov_b64 s[52:53], %[w3]\n"
"s_sub_u32 s52, s52, 1024\n"
"s_subb_u32 s53, s53, 0\n"
"s_mov_b32 s54, 5309440\n"
"s_mov_b32 s55, 0x00020000\n"
"s_mov_b64 s[56:57], %[w2]\n"
"s_sub_u32 s56, s56, 2048\n"
"s_subb_u32 s57, s57, 0\n"
"buffer_load_dwordx4 v[92:95], v25, s[60:63], 0 offen offset:0\n"
"s_mov_b32 s58, 5310464\n"
"s_mov_b32 s59, 0x00020000\n"
"s_mul_i32 s98, %[ti], 184320\n"
"s_sub_u32 s99, 4718592, s98\n"
"s_mov_b64 s[68:69], %[out]\n"
"s_add_u32 s68, s68, s98\n"
"s_addc_u32 s69, s69, 0\n"
"s_mov_b32 s70, s99\n"
"s_mov_b32 s71, 0x00020000\n"
"s_and_b32 s39, %[ti], 0\n"
"s_lshl_b32 s39, s39, 0\n"
"s_add_u32 s39, s39, 4\n"
"s_lshr_b32 s98, s48, 7\n"
"s_sub_u32 s39, s39, s98\n"
"s_add_u32 s98, s39, 0\n"
"s_and_b32 s98, s98, 7\n"
"s_lshl_b32 s98, s98, 7\n"
"s_lshl_b32 s99, %[sl], 10\n"
"s_add_u32 s72, s98, s99\n"
"buffer_load_dwordx4 v[96:99], v25, s[60:63], 0 offen offset:128\n"
"s_mul_i32 s80, s72, 576\n"
"s_add_u32 s80, s80, 2048\n"
"s_add_u32 s98, s39, 1\n"
"s_and_b32 s98, s98, 7\n"
"s_lshl_b32 s98, s98, 7\n"
"s_lshl_b32 s99, %[sl], 10\n"
"s_add_u32 s73, s98, s99\n"
"s_mul_i32 s81, s73, 576\n"
"s_add_u32 s98, s39, 2\n"
"s_and_b32 s98, s98, 7\n"
"s_lshl_b32 s98, s98, 7\n"
"s_lshl_b32 s99, %[sl], 10\n"
"s_add_u32 s74, s98, s99\n"
"s_mul_i32 s82, s74, 576\n"
"s_add_u32 s82, s82, 2048\n"
"s_add_u32 s98, s39, 3\n"
"s_and_b32 s98, s98, 7\n"
"s_lshl_b32 s98, s98, 7\n"
"s_lshl_b32 s99, %[sl], 10\n"
"buffer_load_dwordx4 v[100:103], v25, s[60:63], 0 offen offset:256\n"
"s_add_u32 s75, s98, s99\n"
"s_mul_i32 s83, s75, 576\n"
"s_add_u32 s98, s39, 4\n"
"s_and_b32 s98, s98, 7\n"
"s_lshl_b32 s98, s98, 7\n"
"s_lshl_b32 s99, %[sl], 10\n"
"s_add_u32 s76, s98, s99\n"
"s_mul_i32 s84, s76, 576\n"
"s_add_u32 s84, s84, 2048\n"
"s_add_u32 s98, s39, 5\n"
"s_and_b32 s98, s98, 7\n"
"s_lshl_b32 s98, s98, 7\n"
"s_lshl_b32 s99, %[sl], 10\n"
"s_add_u32 s77, s98, s99\n"
"s_mul_i32 s85, s77, 576\n"
"s_add_u32 s98, s39, 6\n"
"s_and_b32 s98, s98, 7\n"
"s_lshl_b32 s98, s98, 7\n"
"s_lshl_b32 s99, %[sl], 10\n"
"buffer_load_dwordx4 v[104:107], v25, s[60:63], 0 offen offset:384\n"
"s_add_u32 s78, s98, s99\n"
"s_mul_i32 s86, s78, 576\n"
"s_add_u32 s86, s86, 2048\n"
"s_add_u32 s98, s39, 7\n"
"s_and_b32 s98, s98, 7\n"
"s_lshl_b32 s98, s98, 7\n"
"s_lshl_b32 s99, %[sl], 10\n"
"s_add_u32 s79, s98, s99\n"
"s_mul_i32 s87, s79, 576\n"
"s_lshl_b32 s93, %[cs], 7\n"
"s_lshl_b32 s92, %[sl], 7\n"
"s_mov_b32 s94, 200000\n"
"v_and_b32 v28, 3, v20\n"
"v_lshrrev_b32 v29, 5, v0\n"
"v_mul_u32_u24 v26, 1152, v28\n"
"v_lshl_add_u32 v26, v21, 4, v26\n"
"v_lshl_add_u32 v26, v29, 7, v26\n"
"s_mul_i32 s98, s95, 23040\n"
"s_add_u32 s98, s98, 18432\n"
"buffer_load_dwordx4 v[108:111], v25, s[60:63], 0 offen offset:512\n"
"v_add_u32 v26, s98, v26\n"
"v_cmp_gt_u32 vcc, 32, v0\n"
"v_mov_b32 v27, 0x40000000\n"
"s_nop 0\n"
"v_cndmask_b32 v27, v27, v26, vcc\n"
"s_and_b32 s98, s95, 1\n"
"s_lshl_b32 s98, s98, 2\n"
"v_and_b32 v30, 1, v22\n"
"v_lshlrev_b32 v30, 1, v30\n"
"v_or_b32 v30, s98, v30\n"
"v_xor_b32 v30, v30, v21\n"
"s_mul_i32 s99, s95, 73728\n"
"v_mul_u32_u24 v1, 9216, v20\n"
"v_lshl_add_u32 v1, v30, 4, v1\n"
"v_add_u32 v1, s99, v1\n"
"v_add_u32 v2, 292864, v1\n"
"v_lshlrev_b32 v30, 1, v22\n"
"v_xor_b32 v30, v30, v21\n"
"s_mul_i32 s99, s95, 9216\n"
"v_mul_u32_u24 v5, 1152, v20\n"
"buffer_load_dwordx4 v[112:115], v25, s[60:63], 0 offen offset:640\n"
"v_lshl_add_u32 v5, v30, 4, v5\n"
"v_add_u32 v5, s99, v5\n"
"v_add_u32 v6, 35840, v5\n"
"v_bfe_u32 v31, v0, 2, 2\n"
"v_and_b32 v32, 3, v0\n"
"v_lshrrev_b32 v33, 1, v32\n"
"s_lshl_b32 s98, s95, 1\n"
"v_add_u32 v33, s98, v33\n"
"v_and_b32 v32, 1, v32\n"
"v_lshlrev_b32 v32, 3, v32\n"
"v_lshl_add_u32 v32, v31, 7, v32\n"
"v_lshrrev_b32 v34, 1, v31\n"
"v_and_b32 v35, 1, v22\n"
"v_lshl_add_u32 v34, v35, 1, v34\n"
"v_lshlrev_b32 v34, 1, v34\n"
"v_xor_b32 v34, v34, v33\n"
"v_lshl_add_u32 v32, v34, 4, v32\n"
"v_lshl_add_u32 v7, v22, 12, v32\n"
"v_lshrrev_b32 v36, 1, v22\n"
"buffer_load_dwordx4 v[116:119], v25, s[60:63], 0 offen offset:768\n"
"v_lshl_add_u32 v10, v36, 12, v32\n"
"v_lshl_add_u32 v10, v35, 9, v10\n"
"v_lshrrev_b32 v30, 1, v23\n"
"v_xor_b32 v30, v30, v22\n"
"v_lshlrev_b32 v8, 4, v30\n"
"v_and_b32 v30, 7, v23\n"
"v_lshl_add_u32 v8, v30, 7, v8\n"
"v_lshrrev_b32 v30, 3, v23\n"
"v_lshl_add_u32 v8, v30, 11, v8\n"
"v_add_u32 v8, s97, v8\n"
"v_xor_b32 v9, 64, v8\n"
"v_lshlrev_b32 v12, 4, v0\n"
"v_add_u32 v12, 65536, v12\n"
"s_lshr_b32 s98, s95, 1\n"
"s_mul_i32 s98, s98, 5120\n"
"s_and_b32 s99, s95, 1\n"
"s_lshl_b32 s99, s99, 3\n"
"s_add_u32 s98, s98, s99\n"
"v_add_u32 v11, s98, v12\n"
"buffer_load_dwordx4 v[120:123], v25, s[60:63], 0 offen offset:896\n"
"v_and_b32 v17, 3, v20\n"
"v_lshrrev_b32 v18, 5, v0\n"
"v_cmp_gt_u32 vcc, 32, v0\n"
"s_lshl_b32 s98, s95, 2\n"
"v_add_u32 v30, s98, v17\n"
"v_lshlrev_b32 v31, 1, v21\n"
"v_xor_b32 v30, v30, v31\n"
"v_lshrrev_b32 v31, 2, v21\n"
"v_xor_b32 v30, v30, v31\n"
"v_and_b32 v31, 3, v21\n"
"v_lshl_add_u32 v30, v31, 4, v30\n"
"v_lshlrev_b32 v30, 4, v30\n"
"v_lshrrev_b32 v31, 2, v21\n"
"v_mul_u32_u24 v31, 5120, v31\n"
"v_add3_u32 v30, v30, v31, s38\n"
"v_lshl_add_u32 v48, v18, 10, v30\n"
"v_add_u32 v49, 61440, v48\n"
"v_mul_u32_u24 v31, 10240, v18\n"
"v_add_u32 v50, 4096, v30\n"
"buffer_load_dwordx4 v[124:127], v25, s[60:63], 0 offen offset:1024\n"
"v_add_u32 v50, v50, v31\n"
"v_add_u32 v51, 81920, v50\n"
"v_lshlrev_b32 v31, 4, v0\n"
"v_add_u32 v31, 158720, v31\n"
"v_cndmask_b32 v51, v31, v51, vcc\n"
"s_mul_i32 s98, s95, 160\n"
"s_add_u32 s98, s98, 157696\n"
"v_lshlrev_b32 v52, 3, v20\n"
"v_add_u32 v52, s98, v52\n"
"v_lshlrev_b32 v53, 3, v17\n"
"v_add_u32 v53, s98, v53\n"
"v_lshlrev_b32 v30, 1, v22\n"
"v_xor_b32 v30, v30, v23\n"
"v_lshl_add_u32 v30, v22, 4, v30\n"
"v_lshlrev_b32 v54, 4, v30\n"
"v_add_u32 v54, 65536, v54\n"
"v_xor_b32 v55, 144, v54\n"
"s_mov_b32 s39, 0x3f803f80\n"
"s_waitcnt vmcnt(17)\n"
"v_dot2_f32_bf16 v28, v56, s39, 0\n"
"v_dot2_f32_bf16 v29, v56, v56, 0\n"
"v_dot2_f32_bf16 v28, v57, s39, v28\n"
"v_dot2_f32_bf16 v29, v57, v57, v29\n"
"v_dot2_f32_bf16 v28, v58, s39, v28\n"
"v_dot2_f32_bf16 v29, v58, v58, v29\n"
"v_dot2_f32_bf16 v28, v59, s39, v28\n"
"v_dot2_f32_bf16 v29, v59, v59, v29\n"
"s_waitcnt vmcnt(16)\n"
"v_dot2_f32_bf16 v28, v60, s39, v28\n"
"v_dot2_f32_bf16 v29, v60, v60, v29\n"
"v_dot2_f32_bf16 v28, v61, s39, v28\n"
"v_dot2_f32_bf16 v29, v61, v61, v29\n"
"v_dot2_f32_bf16 v28, v62, s39, v28\n"
"v_dot2_f32_bf16 v29, v62, v62, v29\n"
"v_dot2_f32_bf16 v28, v63, s39, v28\n"
"buffer_load_dwordx4 v[128:131], v26, s[60:63], 0 offen offset:0\n"
"v_dot2_f32_bf16 v29, v63, v63, v29\n"
"s_waitcnt vmcnt(16)\n"
"v_dot2_f32_bf16 v28, v64, s39, v28\n"
"v_dot2_f32_bf16 v29, v64, v64, v29\n"
"v_dot2_f32_bf16 v28, v65, s39, v28\n"
"v_dot2_f32_bf16 v29, v65, v65, v29\n"
"v_dot2_f32_bf16 v28, v66, s39, v28\n"
"v_dot2_f32_bf16 v29, v66, v66, v29\n"
"v_dot2_f32_bf16 v28, v67, s39, v28\n"
"v_dot2_f32_bf16 v29, v67, v67, v29\n"
"s_waitcnt vmcnt(15)\n"
"v_dot2_f32_bf16 v28, v68, s39, v28\n"
"v_dot2_f32_bf16 v29, v68, v68, v29\n"
"v_dot2_f32_bf16 v28, v69, s39, v28\n"
"v_dot2_f32_bf16 v29, v69, v69, v29\n"
"v_dot2_f32_bf16 v28, v70, s39, v28\n"
"v_dot2_f32_bf16 v29, v70, v70, v29\n"
"buffer_load_dwordx4 v[132:135], v26, s[60:63], 0 offen offset:256\n"
"v_dot2_f32_bf16 v28, v71, s39, v28\n"
"v_dot2_f32_bf16 v29, v71, v71, v29\n"
"s_waitcnt vmcnt(15)\n"
"v_dot2_f32_bf16 v28, v72, s39, v28\n"
"v_dot2_f32_bf16 v29, v72, v72, v29\n"
"v_dot2_f32_bf16 v28, v73, s39, v28\n"
"v_dot2_f32_bf16 v29, v73, v73, v29\n"
"v_dot2_f32_bf16 v28, v74, s39, v28\n"
"v_dot2_f32_bf16 v29, v74, v74, v29\n"
"v_dot2_f32_bf16 v28, v75, s39, v28\n"
"v_dot2_f32_bf16 v29, v75, v75, v29\n"
"s_waitcnt vmcnt(14)\n"
"v_dot2_f32_bf16 v28, v76, s39, v28\n"
"v_dot2_f32_bf16 v29, v76, v76, v29\n"
"v_dot2_f32_bf16 v28, v77, s39, v28\n"
"v_dot2_f32_bf16 v29, v77, v77, v29\n"
"buffer_load_dwordx4 v[136:139], v26, s[60:63], 0 offen offset:512\n"
"v_dot2_f32_bf16 v28, v78, s39, v28\n"
"v_dot2_f32_bf16 v29, v78, v78, v29\n"
"v_dot2_f32_bf16 v28, v79, s39, v28\n"
"v_dot2_f32_bf16 v29, v79, v79, v29\n"
"s_waitcnt vmcnt(14)\n"
"v_dot2_f32_bf16 v28, v80, s39, v28\n"
"v_dot2_f32_bf16 v29, v80, v80, v29\n"
"v_dot2_f32_bf16 v28, v81, s39, v28\n"
"v_dot2_f32_bf16 v29, v81, v81, v29\n"
"v_dot2_f32_bf16 v28, v82, s39, v28\n"
"v_dot2_f32_bf16 v29, v82, v82, v29\n"
"v_dot2_f32_bf16 v28, v83, s39, v28\n"
"v_dot2_f32_bf16 v29, v83, v83, v29\n"
"s_waitcnt vmcnt(13)\n"
"v_dot2_f32_bf16 v28, v84, s39, v28\n"
"v_dot2_f32_bf16 v29, v84, v84, v29\n"
"v_dot2_f32_bf16 v28, v85, s39, v28\n"
"buffer_load_dwordx4 v[140:143], v26, s[60:63], 0 offen offset:768\n"
"v_dot2_f32_bf16 v29, v85, v85, v29\n"
"v_dot2_f32_bf16 v28, v86, s39, v28\n"
"v_dot2_f32_bf16 v29, v86, v86, v29\n"
"v_dot2_f32_bf16 v28, v87, s39, v28\n"
"v_dot2_f32_bf16 v29, v87, v87, v29\n"
"s_waitcnt vmcnt(13)\n"
"v_dot2_f32_bf16 v28, v88, s39, v28\n"
"v_dot2_f32_bf16 v29, v88, v88, v29\n"
"v_dot2_f32_bf16 v28, v89, s39, v28\n"
"v_dot2_f32_bf16 v29, v89, v89, v29\n"
"v_dot2_f32_bf16 v28, v90, s39, v28\n"
"v_dot2_f32_bf16 v29, v90, v90, v29\n"
"v_dot2_f32_bf16 v28, v91, s39, v28\n"
"v_dot2_f32_bf16 v29, v91, v91, v29\n"
"s_nop 1\n"
"v_add_f32_dpp v28, v28, v28 quad_perm:[1,0,3,2] row_mask:0xf bank_mask:0xf\n"
"v_add_f32_dpp v29, v29, v29 quad_perm:[1,0,3,2] row_mask:0xf bank_mask:0xf\n"
"buffer_load_dwordx4 v[144:147], v27, s[60:63], 0 offen offset:1024\n"
"v_add_f32_dpp v28, v28, v28 quad_perm:[2,3,0,1] row_mask:0xf bank_mask:0xf\n"
"v_add_f32_dpp v29, v29, v29 quad_perm:[2,3,0,1] row_mask:0xf bank_mask:0xf\n"
"s_nop 0\n"
"v_add_f32_dpp v28, v28, v28 row_half_mirror row_mask:0xf bank_mask:0xf\n"
"v_add_f32_dpp v29, v29, v29 row_half_mirror row_mask:0xf bank_mask:0xf\n"
"v_mul_f32 v28, 0x3ae38e39, v28\n"
"v_mul_f32 v29, 0x3ae38e39, v29\n"
"v_fma_f32 v29, -v28, v28, v29\n"
"v_max_f32 v29, 0, v29\n"
"v_add_f32 v29, 0x3727c5ac, v29\n"
"v_rsq_f32 v32, v29\n"
"s_nop 0\n"
"v_mul_f32 v33, v28, v32\n"
"ds_write_b64 v52, v[32:33] offset:0\n"
"v_mul_f32 v30, 0x3fb8aa3b, v32\n"
"v_mul_f32 v31, 0xbfb8aa3b, v33\n"
"v_lshlrev_b32 v32, 16, v56\n"
"v_and_b32 v33, 0xffff0000, v56\n"
"v_lshlrev_b32 v34, 16, v57\n"
"v_and_b32 v35, 0xffff0000, v57\n"
"v_lshlrev_b32 v36, 16, v58\n"
"v_and_b32 v37, 0xffff0000, v58\n"
"v_lshlrev_b32 v38, 16, v59\n"
"v_and_b32 v39, 0xffff0000, v59\n"
"v_pk_fma_f32 v[32:33], v[32:33], v[30:31], v[30:31] op_sel:[0,0,1] op_sel_hi:[1,0,1]\n"
"v_pk_fma_f32 v[34:35], v[34:35], v[30:31], v[30:31] op_sel:[0,0,1] op_sel_hi:[1,0,1]\n"
"v_pk_fma_f32 v[36:37], v[36:37], v[30:31], v[30:31] op_sel:[0,0,1] op_sel_hi:[1,0,1]\n"
"v_pk_fma_f32 v[38:39], v[38:39], v[30:31], v[30:31] op_sel:[0,0,1] op_sel_hi:[1,0,1]\n"
"v_cvt_pk_bf16_f32 v56, v32, v33\n"
"v_cvt_pk_bf16_f32 v57, v34, v35\n"
"v_cvt_pk_bf16_f32 v58, v36, v37\n"
"v_cvt_pk_bf16_f32 v59, v38, v39\n"
"ds_write_b128 v48, v[56:59] offset:0\n"
"v_lshlrev_b32 v40, 16, v60\n"
"v_and_b32 v41, 0xffff0000, v60\n"
"v_lshlrev_b32 v42, 16, v61\n"
"v_and_b32 v43, 0xffff0000, v61\n"
"v_lshlrev_b32 v44, 16, v62\n"
"s_add_u32 m0, s97, 0\n"
"v_and_b32 v45, 0xffff0000, v62\n"
"v_lshlrev_b32 v46, 16, v63\n"
"v_and_b32 v47, 0xffff0000, v63\n"
"v_pk_fma_f32 v[40:41], v[40:41], v[30:31], v[30:31] op_sel:[0,0,1] op_sel_hi:[1,0,1]\n"
"v_pk_fma_f32 v[42:43], v[42:43], v[30:31], v[30:31] op_sel:[0,0,1] op_sel_hi:[1,0,1]\n"
"v_pk_fma_f32 v[44:45], v[44:45], v[30:31], v[30:31] op_sel:[0,0,1] op_sel_hi:[1,0,1]\n"
"v_pk_fma_f32 v[46:47], v[46:47], v[30:31], v[30:31] op_sel:[0,0,1] op_sel_hi:[1,0,1]\n"
"v_cvt_pk_bf16_f32 v60, v40, v41\n"
"v_cvt_pk_bf16_f32 v61, v42, v43\n"
"v_cvt_pk_bf16_f32 v62, v44, v45\n"
"v_cvt_pk_bf16_f32 v63, v46, v47\n"
"ds_write_b128 v48, v[60:63] offset:10240\n"
"v_lshlrev_b32 v32, 16, v64\n"
"v_and_b32 v33, 0xffff0000, v64\n"
"v_lshlrev_b32 v34, 16, v65\n"
"v_and_b32 v35, 0xffff0000, v65\n"
"v_lshlrev_b32 v36, 16, v66\n"
"v_and_b32 v37, 0xffff0000, v66\n"
"v_lshlrev_b32 v38, 16, v67\n"
"v_and_b32 v39, 0xffff0000, v67\n"
"v_pk_fma_f32 v[32:33], v[32:33], v[30:31], v[30:31] op_sel:[0,0,1] op_sel_hi:[1,0,1]\n"
"v_pk_fma_f32 v[34:35], v[34:35], v[30:31], v[30:31] op_sel:[0,0,1] op_sel_hi:[1,0,1]\n"
"s_mul_i32 s90, %[sl], 73728\n"
"v_pk_fma_f32 v[36:37], v[36:37], v[30:31], v[30:31] op_sel:[0,0,1] op_sel_hi:[1,0,1]\n"
"v_pk_fma_f32 v[38:39], v[38:39], v[30:31], v[30:31] op_sel:[0,0,1] op_sel_hi:[1,0,1]\n"
"v_cvt_pk_bf16_f32 v64, v32, v33\n"
"v_cvt_pk_bf16_f32 v65, v34, v35\n"
"v_cvt_pk_bf16_f32 v66, v36, v37\n"
"v_cvt_pk_bf16_f32 v67, v38, v39\n"
"ds_write_b128 v48, v[64:67] offset:20480\n"
"v_lshlrev_b32 v40, 16, v68\n"
"v_and_b32 v41, 0xffff0000, v68\n"
"v_lshlrev_b32 v42, 16, v69\n"
"v_and_b32 v43, 0xffff0000, v69\n"
"v_lshlrev_b32 v44, 16, v70\n"
"v_and_b32 v45, 0xffff0000, v70\n"
"v_lshlrev_b32 v46, 16, v71\n"
"v_and_b32 v47, 0xffff0000, v71\n"
"v_pk_fma_f32 v[40:41], v[40:41], v[30:31], v[30:31] op_sel:[0,0,1] op_sel_hi:[1,0,1]\n"
"v_pk_fma_f32 v[42:43], v[42:43], v[30:31], v[30:31] op_sel:[0,0,1] op_sel_hi:[1,0,1]\n"
"v_pk_fma_f32 v[44:45], v[44:45], v[30:31], v[30:31] op_sel:[0,0,1] op_sel_hi:[1,0,1]\n"
"v_pk_fma_f32 v[46:47], v[46:47], v[30:31], v[30:31] op_sel:[0,0,1] op_sel_hi:[1,0,1]\n"
"v_cvt_pk_bf16_f32 v68, v40, v41\n"
"v_cvt_pk_bf16_f32 v69, v42, v43\n"
"v_cvt_pk_bf16_f32 v70, v44, v45\n"
"buffer_load_dwordx4 v3, s[40:43], s90 offen lds\n"
"v_cvt_pk_bf16_f32 v71, v46, v47\n"
"ds_write_b128 v48, v[68:71] offset:30720\n"
"v_lshlrev_b32 v32, 16, v72\n"
"v_and_b32 v33, 0xffff0000, v72\n"
"v_lshlrev_b32 v34, 16, v73\n"
"v_and_b32 v35, 0xffff0000, v73\n"
"v_lshlrev_b32 v36, 16, v74\n"
"v_and_b32 v37, 0xffff0000, v74\n"
"v_lshlrev_b32 v38, 16, v75\n"
"v_and_b32 v39, 0xffff0000, v75\n"
"v_pk_fma_f32 v[32:33], v[32:33], v[30:31], v[30:31] op_sel:[0,0,1] op_sel_hi:[1,0,1]\n"
"v_pk_fma_f32 v[34:35], v[34:35], v[30:31], v[30:31] op_sel:[0,0,1] op_sel_hi:[1,0,1]\n"
"v_pk_fma_f32 v[36:37], v[36:37], v[30:31], v[30:31] op_sel:[0,0,1] op_sel_hi:[1,0,1]\n"
"v_pk_fma_f32 v[38:39], v[38:39], v[30:31], v[30:31] op_sel:[0,0,1] op_sel_hi:[1,0,1]\n"
"v_cvt_pk_bf16_f32 v72, v32, v33\n"
"v_cvt_pk_bf16_f32 v73, v34, v35\n"
"v_cvt_pk_bf16_f32 v74, v36, v37\n"
"v_cvt_pk_bf16_f32 v75, v38, v39\n"
"ds_write_b128 v48, v[72:75] offset:40960\n"
"v_lshlrev_b32 v40, 16, v76\n"
"v_and_b32 v41, 0xffff0000, v76\n"
"v_lshlrev_b32 v42, 16, v77\n"
"buffer_load_dwordx4 v3, s[44:47], s90 offen offset:1024 lds\n"
"v_and_b32 v43, 0xffff0000, v77\n"
"v_lshlrev_b32 v44, 16, v78\n"
"v_and_b32 v45, 0xffff0000, v78\n"
"v_lshlrev_b32 v46, 16, v79\n"
"v_and_b32 v47, 0xffff0000, v79\n"
"v_pk_fma_f32 v[40:41], v[40:41], v[30:31], v[30:31] op_sel:[0,0,1] op_sel_hi:[1,0,1]\n"
"v_pk_fma_f32 v[42:43], v[42:43], v[30:31], v[30:31] op_sel:[0,0,1] op_sel_hi:[1,0,1]\n"
"v_pk_fma_f32 v[44:45], v[44:45], v[30:31], v[30:31] op_sel:[0,0,1] op_sel_hi:[1,0,1]\n"
"v_pk_fma_f32 v[46:47], v[46:47], v[30:31], v[30:31] op_sel:[0,0,1] op_sel_hi:[1,0,1]\n"
"v_cvt_pk_bf16_f32 v76, v40, v41\n"
"v_cvt_pk_bf16_f32 v77, v42, v43\n"
"v_cvt_pk_bf16_f32 v78, v44, v45\n"
"v_cvt_pk_bf16_f32 v79, v46, v47\n"
"ds_write_b128 v48, v[76:79] offset:51200\n"
"v_lshlrev_b32 v32, 16, v80\n"
"v_and_b32 v33, 0xffff0000, v80\n"
"v_lshlrev_b32 v34, 16, v81\n"
"v_and_b32 v35, 0xffff0000, v81\n"
"v_lshlrev_b32 v36, 16, v82\n"
"v_and_b32 v37, 0xffff0000, v82\n"
"v_lshlrev_b32 v38, 16, v83\n"
"v_and_b32 v39, 0xffff0000, v83\n"
"buffer_load_dwordx4 v4, s[40:43], s90 offen offset:2048 lds\n"
"v_pk_fma_f32 v[32:33], v[32:33], v[30:31], v[30:31] op_sel:[0,0,1] op_sel_hi:[1,0,1]\n"
"v_pk_fma_f32 v[34:35], v[34:35], v[30:31], v[30:31] op_sel:[0,0,1] op_sel_hi:[1,0,1]\n"
"v_pk_fma_f32 v[36:37], v[36:37], v[30:31], v[30:31] op_sel:[0,0,1] op_sel_hi:[1,0,1]\n"
"v_pk_fma_f32 v[38:39], v[38:39], v[30:31], v[30:31] op_sel:[0,0,1] op_sel_hi:[1,0,1]\n"
"v_cvt_pk_bf16_f32 v80, v32, v33\n"
"v_cvt_pk_bf16_f32 v81, v34, v35\n"
"v_cvt_pk_bf16_f32 v82, v36, v37\n"
"v_cvt_pk_bf16_f32 v83, v38, v39\n"
"ds_write_b128 v49, v[80:83] offset:0\n"
"v_lshlrev_b32 v40, 16, v84\n"
"v_and_b32 v41, 0xffff0000, v84\n"
"v_lshlrev_b32 v42, 16, v85\n"
"v_and_b32 v43, 0xffff0000, v85\n"
"v_lshlrev_b32 v44, 16, v86\n"
"v_and_b32 v45, 0xffff0000, v86\n"
"v_lshlrev_b32 v46, 16, v87\n"
"v_and_b32 v47, 0xffff0000, v87\n"
"v_pk_fma_f32 v[40:41], v[40:41], v[30:31], v[30:31] op_sel:[0,0,1] op_sel_hi:[1,0,1]\n"
"v_pk_fma_f32 v[42:43], v[42:43], v[30:31], v[30:31] op_sel:[0,0,1] op_sel_hi:[1,0,1]\n"
"v_pk_fma_f32 v[44:45], v[44:45], v[30:31], v[30:31] op_sel:[0,0,1] op_sel_hi:[1,0,1]\n"
"v_pk_fma_f32 v[46:47], v[46:47], v[30:31], v[30:31] op_sel:[0,0,1] op_sel_hi:[1,0,1]\n"
"v_cvt_pk_bf16_f32 v84, v40, v41\n"
"buffer_load_dwordx4 v4, s[44:47], s90 offen offset:3072 lds\n"
"v_cvt_pk_bf16_f32 v85, v42, v43\n"
"v_cvt_pk_bf16_f32 v86, v44, v45\n"
"v_cvt_pk_bf16_f32 v87, v46, v47\n"
"ds_write_b128 v49, v[84:87] offset:10240\n"
"v_lshlrev_b32 v32, 16, v88\n"
"v_and_b32 v33, 0xffff0000, v88\n"
"v_lshlrev_b32 v34, 16, v89\n"
"v_and_b32 v35, 0xffff0000, v89\n"
"v_lshlrev_b32 v36, 16, v90\n"
"v_and_b32 v37, 0xffff0000, v90\n"
"v_lshlrev_b32 v38, 16, v91\n"
"v_and_b32 v39, 0xffff0000, v91\n"
"v_pk_fma_f32 v[32:33], v[32:33], v[30:31], v[30:31] op_sel:[0,0,1] op_sel_hi:[1,0,1]\n"
"v_pk_fma_f32 v[34:35], v[34:35], v[30:31], v[30:31] op_sel:[0,0,1] op_sel_hi:[1,0,1]\n"
"v_pk_fma_f32 v[36:37], v[36:37], v[30:31], v[30:31] op_sel:[0,0,1] op_sel_hi:[1,0,1]\n"
"v_pk_fma_f32 v[38:39], v[38:39], v[30:31], v[30:31] op_sel:[0,0,1] op_sel_hi:[1,0,1]\n"
"v_cvt_pk_bf16_f32 v88, v32, v33\n"
"v_cvt_pk_bf16_f32 v89, v34, v35\n"
"v_cvt_pk_bf16_f32 v90, v36, v37\n"
"v_cvt_pk_bf16_f32 v91, v38, v39\n"
"ds_write_b128 v49, v[88:91] offset:20480\n"
"s_waitcnt vmcnt(17)\n"
"v_dot2_f32_bf16 v28, v92, s39, 0\n"
"v_dot2_f32_bf16 v29, v92, v92, 0\n"
"v_dot2_f32_bf16 v28, v93, s39, v28\n"
"v_dot2_f32_bf16 v29, v93, v93, v29\n"
"v_dot2_f32_bf16 v28, v94, s39, v28\n"
"v_dot2_f32_bf16 v29, v94, v94, v29\n"
"v_dot2_f32_bf16 v28, v95, s39, v28\n"
"v_dot2_f32_bf16 v29, v95, v95, v29\n"
"s_waitcnt vmcnt(16)\n"
"v_dot2_f32_bf16 v28, v96, s39, v28\n"
"v_dot2_f32_bf16 v29, v96, v96, v29\n"
"v_dot2_f32_bf16 v28, v97, s39, v28\n"
"v_dot2_f32_bf16 v29, v97, v97, v29\n"
"v_dot2_f32_bf16 v28, v98, s39, v28\n"
"v_dot2_f32_bf16 v29, v98, v98, v29\n"
"v_dot2_f32_bf16 v28, v99, s39, v28\n"
"v_dot2_f32_bf16 v29, v99, v99, v29\n"
"s_waitcnt vmcnt(15)\n"
"v_dot2_f32_bf16 v28, v100, s39, v28\n"
"v_dot2_f32_bf16 v29, v100, v100, v29\n"
"v_dot2_f32_bf16 v28, v101, s39, v28\n"
"v_dot2_f32_bf16 v29, v101, v101, v29\n"
"v_dot2_f32_bf16 v28, v102, s39, v28\n"
"v_dot2_f32_bf16 v29, v102, v102, v29\n"
"v_dot2_f32_bf16 v28, v103, s39, v28\n"
"v_dot2_f32_bf16 v29, v103, v103, v29\n"
"s_waitcnt vmcnt(14)\n"
"v_dot2_f32_bf16 v28, v104, s39, v28\n"
"v_dot2_f32_bf16 v29, v104, v104, v29\n"
"v_dot2_f32_bf16 v28, v105, s39, v28\n"
"v_dot2_f32_bf16 v29, v105, v105, v29\n"
"v_dot2_f32_bf16 v28, v106, s39, v28\n"
"v_dot2_f32_bf16 v29, v106, v106, v29\n"
"v_dot2_f32_bf16 v28, v107, s39, v28\n"
"v_dot2_f32_bf16 v29, v107, v107, v29\n"
"s_waitcnt vmcnt(13)\n"
"v_dot2_f32_bf16 v28, v108, s39, v28\n"
"v_dot2_f32_bf16 v29, v108, v108, v29\n"
"v_dot2_f32_bf16 v28, v109, s39, v28\n"
"v_dot2_f32_bf16 v29, v109, v109, v29\n"
"v_dot2_f32_bf16 v28, v110, s39, v28\n"
"v_dot2_f32_bf16 v29, v110, v110, v29\n"
"v_dot2_f32_bf16 v28, v111, s39, v28\n"
"v_dot2_f32_bf16 v29, v111, v111, v29\n"
"s_waitcnt vmcnt(12)\n"
"v_dot2_f32_bf16 v28, v112, s39, v28\n"
"v_dot2_f32_bf16 v29, v112, v112, v29\n"
"v_dot2_f32_bf16 v28, v113, s39, v28\n"
"v_dot2_f32_bf16 v29, v113, v113, v29\n"
"v_dot2_f32_bf16 v28, v114, s39, v28\n"
"v_dot2_f32_bf16 v29, v114, v114, v29\n"
"v_dot2_f32_bf16 v28, v115, s39, v28\n"
"v_dot2_f32_bf16 v29, v115, v115, v29\n"
"s_waitcnt vmcnt(11)\n"
"v_dot2_f32_bf16 v28, v116, s39, v28\n"
"v_dot2_f32_bf16 v29, v116, v116, v29\n"
"v_dot2_f32_bf16 v28, v117, s39, v28\n"
"v_dot2_f32_bf16 v29, v117, v117, v29\n"
"v_dot2_f32_bf16 v28, v118, s39, v28\n"
"v_dot2_f32_bf16 v29, v118, v118, v29\n"
"v_dot2_f32_bf16 v28, v119, s39, v28\n"
"v_dot2_f32_bf16 v29, v119, v119, v29\n"
"s_waitcnt vmcnt(10)\n"
"v_dot2_f32_bf16 v28, v120, s39, v28\n"
"v_dot2_f32_bf16 v29, v120, v120, v29\n"
"v_dot2_f32_bf16 v28, v121, s39, v28\n"
"v_dot2_f32_bf16 v29, v121, v121, v29\n"
"v_dot2_f32_bf16 v28, v122, s39, v28\n"
"v_dot2_f32_bf16 v29, v122, v122, v29\n"
"v_dot2_f32_bf16 v28, v123, s39, v28\n"
"v_dot2_f32_bf16 v29, v123, v123, v29\n"
"s_waitcnt vmcnt(9)\n"
"v_dot2_f32_bf16 v28, v124, s39, v28\n"
"v_dot2_f32_bf16 v29, v124, v124, v29\n"
"v_dot2_f32_bf16 v28, v125, s39, v28\n"
"v_dot2_f32_bf16 v29, v125, v125, v29\n"
"v_dot2_f32_bf16 v28, v126, s39, v28\n"
"v_dot2_f32_bf16 v29, v126, v126, v29\n"
"v_dot2_f32_bf16 v28, v127, s39, v28\n"
"v_dot2_f32_bf16 v29, v127, v127, v29\n"
"s_nop 1\n"
"v_add_f32_dpp v28, v28, v28 quad_perm:[1,0,3,2] row_mask:0xf bank_mask:0xf\n"
"v_add_f32_dpp v29, v29, v29 quad_perm:[1,0,3,2] row_mask:0xf bank_mask:0xf\n"
"s_nop 0\n"
"v_add_f32_dpp v28, v28, v28 quad_perm:[2,3,0,1] row_mask:0xf bank_mask:0xf\n"
"v_add_f32_dpp v29, v29, v29 quad_perm:[2,3,0,1] row_mask:0xf bank_mask:0xf\n"
"s_nop 0\n"
"v_add_f32_dpp v28, v28, v28 row_half_mirror row_mask:0xf bank_mask:0xf\n"
"v_add_f32_dpp v29, v29, v29 row_half_mirror row_mask:0xf bank_mask:0xf\n"
"v_mul_f32 v28, 0x3ae38e39, v28\n"
"v_mul_f32 v29, 0x3ae38e39, v29\n"
"v_fma_f32 v29, -v28, v28, v29\n"
"v_max_f32 v29, 0, v29\n"
"v_add_f32 v29, 0x3727c5ac, v29\n"
"v_rsq_f32 v32, v29\n"
"s_nop 0\n"
"v_mul_f32 v33, v28, v32\n"
"ds_write_b64 v52, v[32:33] offset:64\n"
"v_mul_f32 v30, 0x3fb8aa3b, v32\n"
"v_mul_f32 v31, 0xbfb8aa3b, v33\n"
"v_lshlrev_b32 v32, 16, v92\n"
"v_and_b32 v33, 0xffff0000, v92\n"
"v_lshlrev_b32 v34, 16, v93\n"
"v_and_b32 v35, 0xffff0000, v93\n"
"v_lshlrev_b32 v36, 16, v94\n"
"v_and_b32 v37, 0xffff0000, v94\n"
"v_lshlrev_b32 v38, 16, v95\n"
"v_and_b32 v39, 0xffff0000, v95\n"
"v_pk_fma_f32 v[32:33], v[32:33], v[30:31], v[30:31] op_sel:[0,0,1] op_sel_hi:[1,0,1]\n"
"v_pk_fma_f32 v[34:35], v[34:35], v[30:31], v[30:31] op_sel:[0,0,1] op_sel_hi:[1,0,1]\n"
"v_pk_fma_f32 v[36:37], v[36:37], v[30:31], v[30:31] op_sel:[0,0,1] op_sel_hi:[1,0,1]\n"
"v_pk_fma_f32 v[38:39], v[38:39], v[30:31], v[30:31] op_sel:[0,0,1] op_sel_hi:[1,0,1]\n"
"s_add_u32 m0, s97, 16384\n"
"v_cvt_pk_bf16_f32 v92, v32, v33\n"
"v_cvt_pk_bf16_f32 v93, v34, v35\n"
"v_cvt_pk_bf16_f32 v94, v36, v37\n"
"v_cvt_pk_bf16_f32 v95, v38, v39\n"
"ds_write_b128 v48, v[92:95] offset:2048\n"
"v_lshlrev_b32 v40, 16, v96\n"
"v_and_b32 v41, 0xffff0000, v96\n"
"v_lshlrev_b32 v42, 16, v97\n"
"v_and_b32 v43, 0xffff0000, v97\n"
"v_lshlrev_b32 v44, 16, v98\n"
"v_and_b32 v45, 0xffff0000, v98\n"
"v_lshlrev_b32 v46, 16, v99\n"
"s_add_u32 s90, s90, 128\n"
"v_and_b32 v47, 0xffff0000, v99\n"
"v_pk_fma_f32 v[40:41], v[40:41], v[30:31], v[30:31] op_sel:[0,0,1] op_sel_hi:[1,0,1]\n"
"v_pk_fma_f32 v[42:43], v[42:43], v[30:31], v[30:31] op_sel:[0,0,1] op_sel_hi:[1,0,1]\n"
"v_pk_fma_f32 v[44:45], v[44:45], v[30:31], v[30:31] op_sel:[0,0,1] op_sel_hi:[1,0,1]\n"
"v_pk_fma_f32 v[46:47], v[46:47], v[30:31], v[30:31] op_sel:[0,0,1] op_sel_hi:[1,0,1]\n"
"v_cvt_pk_bf16_f32 v96, v40, v41\n"
"v_cvt_pk_bf16_f32 v97, v42, v43\n"
"v_cvt_pk_bf16_f32 v98, v44, v45\n"
"v_cvt_pk_bf16_f32 v99, v46, v47\n"
"ds_write_b128 v48, v[96:99] offset:12288\n"
"v_lshlrev_b32 v32, 16, v100\n"
"v_and_b32 v33, 0xffff0000, v100\n"
"buffer_load_dwordx4 v3, s[40:43], s90 offen lds\n"
"v_lshlrev_b32 v34, 16, v101\n"
"v_and_b32 v35, 0xffff0000, v101\n"
"v_lshlrev_b32 v36, 16, v102\n"
"v_and_b32 v37, 0xffff0000, v102\n"
"v_lshlrev_b32 v38, 16, v103\n"
"v_and_b32 v39, 0xffff0000, v103\n"
"v_pk_fma_f32 v[32:33], v[32:33], v[30:31], v[30:31] op_sel:[0,0,1] op_sel_hi:[1,0,1]\n"
"v_pk_fma_f32 v[34:35], v[34:35], v[30:31], v[30:31] op_sel:[0,0,1] op_sel_hi:[1,0,1]\n"
"v_pk_fma_f32 v[36:37], v[36:37], v[30:31], v[30:31] op_sel:[0,0,1] op_sel_hi:[1,0,1]\n"
"v_pk_fma_f32 v[38:39], v[38:39], v[30:31], v[30:31] op_sel:[0,0,1] op_sel_hi:[1,0,1]\n"
"v_cvt_pk_bf16_f32 v100, v32, v33\n"
"v_cvt_pk_bf16_f32 v101, v34, v35\n"
"buffer_load_dwordx4 v3, s[44:47], s90 offen offset:1024 lds\n"
"v_cvt_pk_bf16_f32 v102, v36, v37\n"
"v_cvt_pk_bf16_f32 v103, v38, v39\n"
"ds_write_b128 v48, v[100:103] offset:22528\n"
"v_lshlrev_b32 v40, 16, v104\n"
"v_and_b32 v41, 0xffff0000, v104\n"
"v_lshlrev_b32 v42, 16, v105\n"
"v_and_b32 v43, 0xffff0000, v105\n"
"v_lshlrev_b32 v44, 16, v106\n"
"v_and_b32 v45, 0xffff0000, v106\n"
"v_lshlrev_b32 v46, 16, v107\n"
"v_and_b32 v47, 0xffff0000, v107\n"
"buffer_load_dwordx4 v4, s[40:43], s90 offen offset:2048 lds\n"
"v_pk_fma_f32 v[40:41], v[40:41], v[30:31], v[30:31] op_sel:[0,0,1] op_sel_hi:[1,0,1]\n"
"v_pk_fma_f32 v[42:43], v[42:43], v[30:31], v[30:31] op_sel:[0,0,1] op_sel_hi:[1,0,1]\n"
"v_pk_fma_f32 v[44:45], v[44:45], v[30:31], v[30:31] op_sel:[0,0,1] op_sel_hi:[1,0,1]\n"
"v_pk_fma_f32 v[46:47], v[46:47], v[30:31], v[30:31] op_sel:[0,0,1] op_sel_hi:[1,0,1]\n"
"v_cvt_pk_bf16_f32 v104, v40, v41\n"
"v_cvt_pk_bf16_f32 v105, v42, v43\n"
"v_cvt_pk_bf16_f32 v106, v44, v45\n"
"v_cvt_pk_bf16_f32 v107, v46, v47\n"
"ds_write_b128 v48, v[104:107] offset:32768\n"
"v_lshlrev_b32 v32, 16, v108\n"
"v_and_b32 v33, 0xffff0000, v108\n"
"v_lshlrev_b32 v34, 16, v109\n"
"buffer_load_dwordx4 v4, s[44:47], s90 offen offset:3072 lds\n"
"v_and_b32 v35, 0xffff0000, v109\n"
"v_lshlrev_b32 v36, 16, v110\n"
"v_and_b32 v37, 0xffff0000, v110\n"
"v_lshlrev_b32 v38, 16, v111\n"
"v_and_b32 v39, 0xffff0000, v111\n"
"v_pk_fma_f32 v[32:33], v[32:33], v[30:31], v[30:31] op_sel:[0,0,1] op_sel_hi:[1,0,1]\n"
"v_pk_fma_f32 v[34:35], v[34:35], v[30:31], v[30:31] op_sel:[0,0,1] op_sel_hi:[1,0,1]\n"
"v_pk_fma_f32 v[36:37], v[36:37], v[30:31], v[30:31] op_sel:[0,0,1] op_sel_hi:[1,0,1]\n"
"v_pk_fma_f32 v[38:39], v[38:39], v[30:31], v[30:31] op_sel:[0,0,1] op_sel_hi:[1,0,1]\n"
"v_cvt_pk_bf16_f32 v108, v32, v33\n"
"v_cvt_pk_bf16_f32 v109, v34, v35\n"
"v_cvt_pk_bf16_f32 v110, v36, v37\n"
"s_add_u32 m0, s97, 32768\n"
"v_cvt_pk_bf16_f32 v111, v38, v39\n"
"ds_write_b128 v48, v[108:111] offset:43008\n"
"v_lshlrev_b32 v40, 16, v112\n"
"v_and_b32 v41, 0xffff0000, v112\n"
"v_lshlrev_b32 v42, 16, v113\n"
"v_and_b32 v43, 0xffff0000, v113\n"
"v_lshlrev_b32 v44, 16, v114\n"
"v_and_b32 v45, 0xffff0000, v114\n"
"v_lshlrev_b32 v46, 16, v115\n"
"v_and_b32 v47, 0xffff0000, v115\n"
"v_pk_fma_f32 v[40:41], v[40:41], v[30:31], v[30:31] op_sel:[0,0,1] op_sel_hi:[1,0,1]\n"
"v_pk_fma_f32 v[42:43], v[42:43], v[30:31], v[30:31] op_sel:[0,0,1] op_sel_hi:[1,0,1]\n"
"s_add_u32 s90, s90, 128\n"
"v_pk_fma_f32 v[44:45], v[44:45], v[30:31], v[30:31] op_sel:[0,0,1] op_sel_hi:[1,0,1]\n"
"v_pk_fma_f32 v[46:47], v[46:47], v[30:31], v[30:31] op_sel:[0,0,1] op_sel_hi:[1,0,1]\n"
"v_cvt_pk_bf16_f32 v112, v40, v41\n"
"v_cvt_pk_bf16_f32 v113, v42, v43\n"
"v_cvt_pk_bf16_f32 v114, v44, v45\n"
"v_cvt_pk_bf16_f32 v115, v46, v47\n"
"ds_write_b128 v48, v[112:115] offset:53248\n"
"v_lshlrev_b32 v32, 16, v116\n"
"v_and_b32 v33, 0xffff0000, v116\n"
"v_lshlrev_b32 v34, 16, v117\n"
"v_and_b32 v35, 0xffff0000, v117\n"
"buffer_load_dwordx4 v3, s[40:43], s90 offen lds\n"
"v_lshlrev_b32 v36, 16, v118\n"
"v_and_b32 v37, 0xffff0000, v118\n"
"v_lshlrev_b32 v38, 16, v119\n"
"v_and_b32 v39, 0xffff0000, v119\n"
"v_pk_fma_f32 v[32:33], v[32:33], v[30:31], v[30:31] op_sel:[0,0,1] op_sel_hi:[1,0,1]\n"
"v_pk_fma_f32 v[34:35], v[34:35], v[30:31], v[30:31] op_sel:[0,0,1] op_sel_hi:[1,0,1]\n"
"v_pk_fma_f32 v[36:37], v[36:37], v[30:31], v[30:31] op_sel:[0,0,1] op_sel_hi:[1,0,1]\n"
"v_pk_fma_f32 v[38:39], v[38:39], v[30:31], v[30:31] op_sel:[0,0,1] op_sel_hi:[1,0,1]\n"
"v_cvt_pk_bf16_f32 v116, v32, v33\n"
"v_cvt_pk_bf16_f32 v117, v34, v35\n"
"v_cvt_pk_bf16_f32 v118, v36, v37\n"
"v_cvt_pk_bf16_f32 v119, v38, v39\n"
"buffer_load_dwordx4 v3, s[44:47], s90 offen offset:1024 lds\n"
"ds_write_b128 v49, v[116:119] offset:2048\n"
"v_lshlrev_b32 v40, 16, v120\n"
"v_and_b32 v41, 0xffff0000, v120\n"
"v_lshlrev_b32 v42, 16, v121\n"
"v_and_b32 v43, 0xffff0000, v121\n"
"v_lshlrev_b32 v44, 16, v122\n"
"v_and_b32 v45, 0xffff0000, v122\n"
"v_lshlrev_b32 v46, 16, v123\n"
"v_and_b32 v47, 0xffff0000, v123\n"
"v_pk_fma_f32 v[40:41], v[40:41], v[30:31], v[30:31] op_sel:[0,0,1] op_sel_hi:[1,0,1]\n"
"v_pk_fma_f32 v[42:43], v[42:43], v[30:31], v[30:31] op_sel:[0,0,1] op_sel_hi:[1,0,1]\n"
"v_pk_fma_f32 v[44:45], v[44:45], v[30:31], v[30:31] op_sel:[0,0,1] op_sel_hi:[1,0,1]\n"
"buffer_load_dwordx4 v4, s[40:43], s90 offen offset:2048 lds\n"
"v_pk_fma_f32 v[46:47], v[46:47], v[30:31], v[30:31] op_sel:[0,0,1] op_sel_hi:[1,0,1]\n"
"v_cvt_pk_bf16_f32 v120, v40, v41\n"
"v_cvt_pk_bf16_f32 v121, v42, v43\n"
"v_cvt_pk_bf16_f32 v122, v44, v45\n"
"v_cvt_pk_bf16_f32 v123, v46, v47\n"
"ds_write_b128 v49, v[120:123] offset:12288\n"
"v_lshlrev_b32 v32, 16, v124\n"
"v_and_b32 v33, 0xffff0000, v124\n"
"v_lshlrev_b32 v34, 16, v125\n"
"v_and_b32 v35, 0xffff0000, v125\n"
"v_lshlrev_b32 v36, 16, v126\n"
"v_and_b32 v37, 0xffff0000, v126\n"
"buffer_load_dwordx4 v4, s[44:47], s90 offen offset:3072 lds\n"
"v_lshlrev_b32 v38, 16, v127\n"
"v_and_b32 v39, 0xffff0000, v127\n"
"v_pk_fma_f32 v[32:33], v[32:33], v[30:31], v[30:31] op_sel:[0,0,1] op_sel_hi:[1,0,1]\n"
"v_pk_fma_f32 v[34:35], v[34:35], v[30:31], v[30:31] op_sel:[0,0,1] op_sel_hi:[1,0,1]\n"
"v_pk_fma_f32 v[36:37], v[36:37], v[30:31], v[30:31] op_sel:[0,0,1] op_sel_hi:[1,0,1]\n"
"v_pk_fma_f32 v[38:39], v[38:39], v[30:31], v[30:31] op_sel:[0,0,1] op_sel_hi:[1,0,1]\n"
"v_cvt_pk_bf16_f32 v124, v32, v33\n"
"v_cvt_pk_bf16_f32 v125, v34, v35\n"
"v_cvt_pk_bf16_f32 v126, v36, v37\n"
"v_cvt_pk_bf16_f32 v127, v38, v39\n"
"ds_write_b128 v49, v[124:127] offset:22528\n"
"s_waitcnt vmcnt(16)\n"
"v_dot2_f32_bf16 v28, v128, s39, 0\n"
"v_dot2_f32_bf16 v29, v128, v128, 0\n"
"v_dot2_f32_bf16 v28, v129, s39, v28\n"
"v_dot2_f32_bf16 v29, v129, v129, v29\n"
"v_dot2_f32_bf16 v28, v130, s39, v28\n"
"v_dot2_f32_bf16 v29, v130, v130, v29\n"
"v_dot2_f32_bf16 v28, v131, s39, v28\n"
"v_dot2_f32_bf16 v29, v131, v131, v29\n"
"s_waitcnt vmcnt(15)\n"
"v_dot2_f32_bf16 v28, v132, s39, v28\n"
"v_dot2_f32_bf16 v29, v132, v132, v29\n"
"v_dot2_f32_bf16 v28, v133, s39, v28\n"
"v_dot2_f32_bf16 v29, v133, v133, v29\n"
"v_dot2_f32_bf16 v28, v134, s39, v28\n"
"global_load_ushort v13, v19, %[lre]\n"
"v_dot2_f32_bf16 v29, v134, v134, v29\n"
"v_dot2_f32_bf16 v28, v135, s39, v28\n"
"v_dot2_f32_bf16 v29, v135, v135, v29\n"
"s_waitcnt vmcnt(15)\n"
"v_dot2_f32_bf16 v28, v136, s39, v28\n"
"v_dot2_f32_bf16 v29, v136, v136, v29\n"
"v_dot2_f32_bf16 v28, v137, s39, v28\n"
"v_dot2_f32_bf16 v29, v137, v137, v29\n"
"v_dot2_f32_bf16 v28, v138, s39, v28\n"
"v_dot2_f32_bf16 v29, v138, v138, v29\n"
"v_dot2_f32_bf16 v28, v139, s39, v28\n"
"v_dot2_f32_bf16 v29, v139, v139, v29\n"
"s_waitcnt vmcnt(14)\n"
"v_dot2_f32_bf16 v28, v140, s39, v28\n"
"global_load_ushort v14, v19, %[lim]\n"
"v_dot2_f32_bf16 v29, v140, v140, v29\n"
"v_dot2_f32_bf16 v28, v141, s39, v28\n"
"v_dot2_f32_bf16 v29, v141, v141, v29\n"
"v_dot2_f32_bf16 v28, v142, s39, v28\n"
"v_dot2_f32_bf16 v29, v142, v142, v29\n"
"v_dot2_f32_bf16 v28, v143, s39, v28\n"
"v_dot2_f32_bf16 v29, v143, v143, v29\n"
"s_waitcnt vmcnt(14)\n"
"v_dot2_f32_bf16 v28, v144, s39, v28\n"
"v_dot2_f32_bf16 v29, v144, v144, v29\n"
"v_dot2_f32_bf16 v28, v145, s39, v28\n"
"v_dot2_f32_bf16 v29, v145, v145, v29\n"
"v_dot2_f32_bf16 v28, v146, s39, v28\n"
"v_dot2_f32_bf16 v29, v146, v146, v29\n"
"global_load_ushort v15, v19, %[ldt]\n"
"v_dot2_f32_bf16 v28, v147, s39, v28\n"
"v_dot2_f32_bf16 v29, v147, v147, v29\n"
"s_nop 1\n"
"v_add_f32_dpp v28, v28, v28 quad_perm:[1,0,3,2] row_mask:0xf bank_mask:0xf\n"
"v_add_f32_dpp v29, v29, v29 quad_perm:[1,0,3,2] row_mask:0xf bank_mask:0xf\n"
"s_nop 0\n"
"v_add_f32_dpp v28, v28, v28 quad_perm:[2,3,0,1] row_mask:0xf bank_mask:0xf\n"
"v_add_f32_dpp v29, v29, v29 quad_perm:[2,3,0,1] row_mask:0xf bank_mask:0xf\n"
"s_nop 0\n"
"v_add_f32_dpp v28, v28, v28 row_half_mirror row_mask:0xf bank_mask:0xf\n"
"v_add_f32_dpp v29, v29, v29 row_half_mirror row_mask:0xf bank_mask:0xf\n"
"v_mov_b32 v30, v28\n"
"v_mov_b32 v31, v29\n"
"s_nop 0\n"
"v_permlane32_swap_b32 v28, v30\n"
"v_permlane32_swap_b32 v29, v31\n"
"buffer_load_dword v16, off, s[64:67], 0 sc1\n"
"v_add_f32 v28, v28, v30\n"
"v_add_f32 v29, v29, v31\n"
"v_mul_f32 v28, 0x3ae38e39, v28\n"
"v_mul_f32 v29, 0x3ae38e39, v29\n"
"v_fma_f32 v29, -v28, v28, v29\n"
"v_max_f32 v29, 0, v29\n"
"v_add_f32 v29, 0x3727c5ac, v29\n"
"v_rsq_f32 v32, v29\n"
"s_nop 0\n"
"v_mul_f32 v33, v28, v32\n"
"ds_write_b64 v53, v[32:33] offset:128\n"
"v_mul_f32 v30, 0x3fb8aa3b, v32\n"
"v_mul_f32 v31, 0xbfb8aa3b, v33\n"
"v_lshlrev_b32 v32, 16, v128\n"
"v_and_b32 v33, 0xffff0000, v128\n"
"v_lshlrev_b32 v34, 16, v129\n"
"v_and_b32 v35, 0xffff0000, v129\n"
"v_lshlrev_b32 v36, 16, v130\n"
"v_and_b32 v37, 0xffff0000, v130\n"
"v_lshlrev_b32 v38, 16, v131\n"
"v_and_b32 v39, 0xffff0000, v131\n"
"v_pk_fma_f32 v[32:33], v[32:33], v[30:31], v[30:31] op_sel:[0,0,1] op_sel_hi:[1,0,1]\n"
"v_pk_fma_f32 v[34:35], v[34:35], v[30:31], v[30:31] op_sel:[0,0,1] op_sel_hi:[1,0,1]\n"
"v_pk_fma_f32 v[36:37], v[36:37], v[30:31], v[30:31] op_sel:[0,0,1] op_sel_hi:[1,0,1]\n"
"v_pk_fma_f32 v[38:39], v[38:39], v[30:31], v[30:31] op_sel:[0,0,1] op_sel_hi:[1,0,1]\n"
"v_cvt_pk_bf16_f32 v128, v32, v33\n"
"s_add_u32 m0, s97, 49152\n"
"v_cvt_pk_bf16_f32 v129, v34, v35\n"
"v_cvt_pk_bf16_f32 v130, v36, v37\n"
"v_cvt_pk_bf16_f32 v131, v38, v39\n"
"ds_write_b128 v50, v[128:131] offset:0\n"
"v_lshlrev_b32 v40, 16, v132\n"
"v_and_b32 v41, 0xffff0000, v132\n"
"v_lshlrev_b32 v42, 16, v133\n"
"v_and_b32 v43, 0xffff0000, v133\n"
"v_lshlrev_b32 v44, 16, v134\n"
"v_and_b32 v45, 0xffff0000, v134\n"
"v_lshlrev_b32 v46, 16, v135\n"
"v_and_b32 v47, 0xffff0000, v135\n"
"s_add_u32 s90, s90, 128\n"
"v_pk_fma_f32 v[40:41], v[40:41], v[30:31], v[30:31] op_sel:[0,0,1] op_sel_hi:[1,0,1]\n"
"v_pk_fma_f32 v[42:43], v[42:43], v[30:31], v[30:31] op_sel:[0,0,1] op_sel_hi:[1,0,1]\n"
"v_pk_fma_f32 v[44:45], v[44:45], v[30:31], v[30:31] op_sel:[0,0,1] op_sel_hi:[1,0,1]\n"
"v_pk_fma_f32 v[46:47], v[46:47], v[30:31], v[30:31] op_sel:[0,0,1] op_sel_hi:[1,0,1]\n"
"v_cvt_pk_bf16_f32 v132, v40, v41\n"
"v_cvt_pk_bf16_f32 v133, v42, v43\n"
"v_cvt_pk_bf16_f32 v134, v44, v45\n"
"v_cvt_pk_bf16_f32 v135, v46, v47\n"
"ds_write_b128 v50, v[132:135] offset:20480\n"
"v_lshlrev_b32 v32, 16, v136\n"
"v_and_b32 v33, 0xffff0000, v136\n"
"v_lshlrev_b32 v34, 16, v137\n"
"buffer_load_dwordx4 v3, s[40:43], s90 offen lds\n"
"v_and_b32 v35, 0xffff0000, v137\n"
"v_lshlrev_b32 v36, 16, v138\n"
"v_and_b32 v37, 0xffff0000, v138\n"
"v_lshlrev_b32 v38, 16, v139\n"
"v_and_b32 v39, 0xffff0000, v139\n"
"v_pk_fma_f32 v[32:33], v[32:33], v[30:31], v[30:31] op_sel:[0,0,1] op_sel_hi:[1,0,1]\n"
"v_pk_fma_f32 v[34:35], v[34:35], v[30:31], v[30:31] op_sel:[0,0,1] op_sel_hi:[1,0,1]\n"
"v_pk_fma_f32 v[36:37], v[36:37], v[30:31], v[30:31] op_sel:[0,0,1] op_sel_hi:[1,0,1]\n"
"v_pk_fma_f32 v[38:39], v[38:39], v[30:31], v[30:31] op_sel:[0,0,1] op_sel_hi:[1,0,1]\n"
"v_cvt_pk_bf16_f32 v136, v32, v33\n"
"v_cvt_pk_bf16_f32 v137, v34, v35\n"
"v_cvt_pk_bf16_f32 v138, v36, v37\n"
"buffer_load_dwordx4 v3, s[44:47], s90 offen offset:1024 lds\n"
"v_cvt_pk_bf16_f32 v139, v38, v39\n"
"ds_write_b128 v50, v[136:139] offset:40960\n"
"v_lshlrev_b32 v40, 16, v140\n"
"v_and_b32 v41, 0xffff0000, v140\n"
"v_lshlrev_b32 v42, 16, v141\n"
"v_and_b32 v43, 0xffff0000, v141\n"
"v_lshlrev_b32 v44, 16, v142\n"
"v_and_b32 v45, 0xffff0000, v142\n"
"v_lshlrev_b32 v46, 16, v143\n"
"v_and_b32 v47, 0xffff0000, v143\n"
"v_pk_fma_f32 v[40:41], v[40:41], v[30:31], v[30:31] op_sel:[0,0,1] op_sel_hi:[1,0,1]\n"
"v_pk_fma_f32 v[42:43], v[42:43], v[30:31], v[30:31] op_sel:[0,0,1] op_sel_hi:[1,0,1]\n"
"buffer_load_dwordx4 v4, s[40:43], s90 offen offset:2048 lds\n"
"v_pk_fma_f32 v[44:45], v[44:45], v[30:31], v[30:31] op_sel:[0,0,1] op_sel_hi:[1,0,1]\n"
"v_pk_fma_f32 v[46:47], v[46:47], v[30:31], v[30:31] op_sel:[0,0,1] op_sel_hi:[1,0,1]\n"
"v_cvt_pk_bf16_f32 v140, v40, v41\n"
"v_cvt_pk_bf16_f32 v141, v42, v43\n"
"v_cvt_pk_bf16_f32 v142, v44, v45\n"
"v_cvt_pk_bf16_f32 v143, v46, v47\n"
"ds_write_b128 v50, v[140:143] offset:61440\n"
"v_lshlrev_b32 v32, 16, v144\n"
"v_and_b32 v33, 0xffff0000, v144\n"
"v_lshlrev_b32 v34, 16, v145\n"
"v_and_b32 v35, 0xffff0000, v145\n"
"v_lshlrev_b32 v36, 16, v146\n"
"buffer_load_dwordx4 v4, s[44:47], s90 offen offset:3072 lds\n"
"v_and_b32 v37, 0xffff0000, v146\n"
"v_lshlrev_b32 v38, 16, v147\n"
"v_and_b32 v39, 0xffff0000, v147\n"
"v_pk_fma_f32 v[32:33], v[32:33], v[30:31], v[30:31] op_sel:[0,0,1] op_sel_hi:[1,0,1]\n"
"v_pk_fma_f32 v[34:35], v[34:35], v[30:31], v[30:31] op_sel:[0,0,1] op_sel_hi:[1,0,1]\n"
"v_pk_fma_f32 v[36:37], v[36:37], v[30:31], v[30:31] op_sel:[0,0,1] op_sel_hi:[1,0,1]\n"
"v_pk_fma_f32 v[38:39], v[38:39], v[30:31], v[30:31] op_sel:[0,0,1] op_sel_hi:[1,0,1]\n"
"v_cvt_pk_bf16_f32 v144, v32, v33\n"
"v_cvt_pk_bf16_f32 v145, v34, v35\n"
"v_cvt_pk_bf16_f32 v146, v36, v37\n"
"v_cvt_pk_bf16_f32 v147, v38, v39\n"
"ds_write_b128 v51, v[144:147] offset:0\n"
"s_waitcnt lgkmcnt(0) vmcnt(16)\n"
"s_barrier\n"
"ds_read_b128 v[56:59], v8 offset:0\n"
"ds_read_b128 v[60:63], v8 offset:1024\n"
"ds_read_b128 a[0:3], v54 offset:0\n"
"ds_read_b128 a[4:7], v54 offset:1024\n"
"ds_read_b128 a[8:11], v54 offset:2048\n"
"ds_read_b128 a[12:15], v54 offset:3072\n"
"ds_read_b128 a[16:19], v54 offset:4096\n"
"ds_read_b128 a[20:23], v55 offset:5120\n"
"ds_read_b128 a[24:27], v55 offset:6144\n"
"ds_read_b128 a[28:31], v55 offset:7168\n"
"ds_read_b128 a[32:35], v55 offset:8192\n"
"ds_read_b128 a[36:39], v55 offset:9216\n"
"s_waitcnt lgkmcnt(9)\n"
"v_mfma_f32_16x16x32_bf16 v[72:75], a[0:3], v[56:59], 0\n"
"ds_read_b128 v[64:67], v9 offset:0\n"
"ds_read_b128 a[40:43], v54 offset:10240\n"
"s_waitcnt lgkmcnt(10)\n"
"v_mfma_f32_16x16x32_bf16 v[76:79], a[4:7], v[56:59], 0\n"
"ds_read_b128 a[44:47], v54 offset:11264\n"
"s_waitcnt lgkmcnt(10)\n"
"v_mfma_f32_16x16x32_bf16 v[80:83], a[8:11], v[56:59], 0\n"
"ds_read_b128 a[48:51], v54 offset:12288\n"
"s_waitcnt lgkmcnt(10)\n"
"v_mfma_f32_16x16x32_bf16 v[84:87], a[12:15], v[56:59], 0\n"
"ds_read_b128 v[68:71], v9 offset:1024\n"
"s_waitcnt lgkmcnt(10)\n"
"v_mfma_f32_16x16x32_bf16 v[88:91], a[16:19], v[56:59], 0\n"
"ds_read_b128 a[52:55], v54 offset:13312\n"
"v_mfma_f32_16x16x32_bf16 v[92:95], a[0:3], v[60:63], 0\n"
"ds_read_b128 a[56:59], v54 offset:14336\n"
"v_mfma_f32_16x16x32_bf16 v[96:99], a[4:7], v[60:63], 0\n"
"v_mfma_f32_16x16x32_bf16 v[100:103], a[8:11], v[60:63], 0\n"
"s_add_u32 m0, s97, 0\n"
"v_mfma_f32_16x16x32_bf16 v[104:107], a[12:15], v[60:63], 0\n"
"v_mfma_f32_16x16x32_bf16 v[108:111], a[16:19], v[60:63], 0\n"
"s_waitcnt lgkmcnt(2) vmcnt(12)\n"
"s_barrier\n"
"v_mfma_f32_16x16x32_bf16 v[72:75], a[20:23], v[64:67], v[72:75]\n"
"ds_read_b128 v[56:59], v8 offset:16384\n"
"s_add_u32 s90, s90, 128\n"
"v_mfma_f32_16x16x32_bf16 v[76:79], a[24:27], v[64:67], v[76:79]\n"
"ds_read_b128 a[60:63], v55 offset:15360\n"
"v_mfma_f32_16x16x32_bf16 v[80:83], a[28:31], v[64:67], v[80:83]\n"
"ds_read_b128 a[64:67], v55 offset:16384\n"
"v_mfma_f32_16x16x32_bf16 v[84:87], a[32:35], v[64:67], v[84:87]\n"
"buffer_load_dwordx4 v3, s[40:43], s90 offen lds\n"
"v_mfma_f32_16x16x32_bf16 v[88:91], a[36:39], v[64:67], v[88:91]\n"
"ds_read_b128 a[68:71], v55 offset:17408\n"
"v_mfma_f32_16x16x32_bf16 v[92:95], a[20:23], v[68:71], v[92:95]\n"
"ds_read_b128 v[60:63], v8 offset:17408\n"
"v_mfma_f32_16x16x32_bf16 v[96:99], a[24:27], v[68:71], v[96:99]\n"
"ds_read_b128 a[72:75], v55 offset:18432\n"
"v_mfma_f32_16x16x32_bf16 v[100:103], a[28:31], v[68:71], v[100:103]\n"
"v_mfma_f32_16x16x32_bf16 v[104:107], a[32:35], v[68:71], v[104:107]\n"
"buffer_load_dwordx4 v3, s[44:47], s90 offen offset:1024 lds\n"
"ds_read_b128 a[76:79], v55 offset:19456\n"
"v_mfma_f32_16x16x32_bf16 v[108:111], a[36:39], v[68:71], v[108:111]\n"
"s_waitcnt lgkmcnt(6)\n"
"v_mfma_f32_16x16x32_bf16 v[72:75], a[40:43], v[56:59], v[72:75]\n"
"ds_read_b128 v[64:67], v9 offset:16384\n"
"ds_read_b128 a[80:83], v54 offset:20480\n"
"v_mfma_f32_16x16x32_bf16 v[76:79], a[44:47], v[56:59], v[76:79]\n"
"ds_read_b128 a[84:87], v54 offset:21504\n"
"s_waitcnt vmcnt(9)\n"
"v_lshlrev_b32 v13, 16, v13\n"
"v_mfma_f32_16x16x32_bf16 v[80:83], a[48:51], v[56:59], v[80:83]\n"
"buffer_load_dwordx4 v4, s[40:43], s90 offen offset:2048 lds\n"
"ds_read_b128 a[88:91], v54 offset:22528\n"
"v_mfma_f32_16x16x32_bf16 v[84:87], a[52:55], v[56:59], v[84:87]\n"
"ds_read_b128 v[68:71], v9 offset:17408\n"
"s_waitcnt vmcnt(9)\n"
"v_lshlrev_b32 v14, 16, v14\n"
"v_mfma_f32_16x16x32_bf16 v[88:91], a[56:59], v[56:59], v[88:91]\n"
"ds_read_b128 a[92:95], v54 offset:23552\n"
"s_waitcnt vmcnt(8)\n"
"v_lshlrev_b32 v15, 16, v15\n"
"s_waitcnt lgkmcnt(8)\n"
"v_mfma_f32_16x16x32_bf16 v[92:95], a[40:43], v[60:63], v[92:95]\n"
"ds_read_b128 a[96:99], v54 offset:24576\n"
"v_mfma_f32_16x16x32_bf16 v[96:99], a[44:47], v[60:63], v[96:99]\n"
"buffer_load_dwordx4 v4, s[44:47], s90 offen offset:3072 lds\n"
"s_waitcnt vmcnt(8)\n"
"v_readfirstlane_b32 s96, v16\n"
"v_mfma_f32_16x16x32_bf16 v[100:103], a[48:51], v[60:63], v[100:103]\n"
"s_add_u32 m0, s97, 16384\n"
"v_mul_f32 v15, 0x3fb8aa3b, v15\n"
"v_mfma_f32_16x16x32_bf16 v[104:107], a[52:55], v[60:63], v[104:107]\n"
"v_exp_f32 v15, v15\n"
"v_mfma_f32_16x16x32_bf16 v[108:111], a[56:59], v[60:63], v[108:111]\n"
"s_waitcnt lgkmcnt(2)\n"
"s_barrier\n"
"v_mfma_f32_16x16x32_bf16 v[72:75], a[60:63], v[64:67], v[72:75]\n"
"ds_read_b128 v[56:59], v8 offset:32768\n"
"s_add_u32 s90, s90, 128\n"
"v_mfma_f32_16x16x32_bf16 v[76:79], a[64:67], v[64:67], v[76:79]\n"
"ds_read_b128 a[100:103], v55 offset:25600\n"
"v_mfma_f32_16x16x32_bf16 v[80:83], a[68:71], v[64:67], v[80:83]\n"
"ds_read_b128 a[104:107], v55 offset:26624\n"
"s_mul_i32 s88, s96, 0x9e3779b1\n"
"v_mfma_f32_16x16x32_bf16 v[84:87], a[72:75], v[64:67], v[84:87]\n"
"buffer_load_dwordx4 v3, s[40:43], s90 offen lds\n"
"v_mfma_f32_16x16x32_bf16 v[88:91], a[76:79], v[64:67], v[88:91]\n"
"ds_read_b128 a[108:111], v55 offset:27648\n"
"s_add_u32 s88, s88, 0x7f4a7c15\n"
"v_mfma_f32_16x16x32_bf16 v[92:95], a[60:63], v[68:71], v[92:95]\n"
"ds_read_b128 v[60:63], v8 offset:33792\n"
"s_mov_b32 s89, s88\n"
"v_mfma_f32_16x16x32_bf16 v[96:99], a[64:67], v[68:71], v[96:99]\n"
"ds_read_b128 a[112:115], v55 offset:28672\n"
"v_lshrrev_b32 v33, 4, v0\n"
"v_mfma_f32_16x16x32_bf16 v[100:103], a[68:71], v[68:71], v[100:103]\n"
"v_cvt_pk_bf16_f32 v15, v15, v15\n"
"v_mfma_f32_16x16x32_bf16 v[104:107], a[72:75], v[68:71], v[104:107]\n"
"buffer_load_dwordx4 v3, s[44:47], s90 offen offset:1024 lds\n"
"ds_read_b128 a[116:119], v55 offset:29696\n"
"v_mfma_f32_16x16x32_bf16 v[108:111], a[76:79], v[68:71], v[108:111]\n"
"v_and_b32 v32, 1, v33\n"
"v_and_b32 v15, 0xffff0000, v15\n"
"s_waitcnt lgkmcnt(6)\n"
"v_mfma_f32_16x16x32_bf16 v[72:75], a[80:83], v[56:59], v[72:75]\n"
"ds_read_b128 v[64:67], v9 offset:32768\n"
"ds_read_b128 a[120:123], v54 offset:30720\n"
"v_mfma_f32_16x16x32_bf16 v[76:79], a[84:87], v[56:59], v[76:79]\n"
"ds_read_b128 a[124:127], v54 offset:31744\n"
"v_mfma_f32_16x16x32_bf16 v[80:83], a[88:91], v[56:59], v[80:83]\n"
"buffer_load_dwordx4 v4, s[40:43], s90 offen offset:2048 lds\n"
"ds_read_b128 a[128:131], v54 offset:32768\n"
"v_mfma_f32_16x16x32_bf16 v[84:87], a[92:95], v[56:59], v[84:87]\n"
"ds_read_b128 v[68:71], v9 offset:33792\n"
"v_lshrrev_b32 v33, 1, v33\n"
"v_mfma_f32_16x16x32_bf16 v[88:91], a[96:99], v[56:59], v[88:91]\n"
"ds_read_b128 a[132:135], v54 offset:33792\n"
"v_mul_f32 v17, v15, v13\n"
"s_waitcnt lgkmcnt(8)\n"
"v_mfma_f32_16x16x32_bf16 v[92:95], a[80:83], v[60:63], v[92:95]\n"
"ds_read_b128 a[136:139], v54 offset:34816\n"
"v_mfma_f32_16x16x32_bf16 v[96:99], a[84:87], v[60:63], v[96:99]\n"
"buffer_load_dwordx4 v4, s[44:47], s90 offen offset:3072 lds\n"
"v_mul_f32 v16, v15, v14\n"
"v_mfma_f32_16x16x32_bf16 v[100:103], a[88:91], v[60:63], v[100:103]\n"
"s_add_u32 m0, s97, 32768\n"
"v_mul_f32 v17, 0x3fb8aa3b, v17\n"
"v_mfma_f32_16x16x32_bf16 v[104:107], a[92:95], v[60:63], v[104:107]\n"
"v_mul_f32 v18, 0x3e22f983, v16\n"
"v_mfma_f32_16x16x32_bf16 v[108:111], a[96:99], v[60:63], v[108:111]\n"
"s_waitcnt lgkmcnt(2) vmcnt(8)\n"
"s_barrier\n"
"v_mfma_f32_16x16x32_bf16 v[72:75], a[100:103], v[64:67], v[72:75]\n"
"ds_read_b128 v[56:59], v8 offset:49152\n"
"s_add_u32 s90, s90, 128\n"
"v_mfma_f32_16x16x32_bf16 v[76:79], a[104:107], v[64:67], v[76:79]\n"
"ds_read_b128 a[140:143], v55 offset:35840\n"
"s_mov_b32 s98, 0x3e22f983\n"
"v_mfma_f32_16x16x32_bf16 v[80:83], a[108:111], v[64:67], v[80:83]\n"
"ds_read_b128 a[144:147], v55 offset:36864\n"
"s_mov_b32 s99, 0x31dc9c88\n"
"v_mfma_f32_16x16x32_bf16 v[84:87], a[112:115], v[64:67], v[84:87]\n"
"buffer_load_dwordx4 v3, s[40:43], s90 offen lds\n"
"v_mfma_f32_16x16x32_bf16 v[88:91], a[116:119], v[64:67], v[88:91]\n"
"ds_read_b128 a[148:151], v55 offset:37888\n"
"v_rndne_f32 v18, v18\n"
"v_mfma_f32_16x16x32_bf16 v[92:95], a[100:103], v[68:71], v[92:95]\n"
"ds_read_b128 v[60:63], v8 offset:50176\n"
"v_cvt_f32_u32 v32, v32\n"
"v_mfma_f32_16x16x32_bf16 v[96:99], a[104:107], v[68:71], v[96:99]\n"
"ds_read_b128 a[152:155], v55 offset:38912\n"
"v_fma_f32 v18, v16, s98, -v18\n"
"v_mfma_f32_16x16x32_bf16 v[100:103], a[108:111], v[68:71], v[100:103]\n"
"v_cvt_f32_u32 v33, v33\n"
"v_mfma_f32_16x16x32_bf16 v[104:107], a[112:115], v[68:71], v[104:107]\n"
"buffer_load_dwordx4 v3, s[44:47], s90 offen offset:1024 lds\n"
"ds_read_b128 a[156:159], v55 offset:39936\n"
"v_mfma_f32_16x16x32_bf16 v[108:111], a[116:119], v[68:71], v[108:111]\n"
"v_fma_f32 v18, v16, s99, v18\n"
"v_mul_f32 v15, 1.0, v17\n"
"s_waitcnt lgkmcnt(6)\n"
"v_mfma_f32_16x16x32_bf16 v[72:75], a[120:123], v[56:59], v[72:75]\n"
"ds_read_b128 v[64:67], v9 offset:49152\n"
"ds_read_b128 a[160:163], v54 offset:40960\n"
"v_mfma_f32_16x16x32_bf16 v[76:79], a[124:127], v[56:59], v[76:79]\n"
"ds_read_b128 a[164:167], v54 offset:41984\n"
"v_mfma_f32_16x16x32_bf16 v[80:83], a[128:131], v[56:59], v[80:83]\n"
"buffer_load_dwordx4 v4, s[40:43], s90 offen offset:2048 lds\n"
"ds_read_b128 a[168:171], v54 offset:43008\n"
"v_mfma_f32_16x16x32_bf16 v[84:87], a[132:135], v[56:59], v[84:87]\n"
"ds_read_b128 v[68:71], v9 offset:50176\n"
"v_mul_f32 v20, 1.0, v18\n"
"v_mfma_f32_16x16x32_bf16 v[88:91], a[136:139], v[56:59], v[88:91]\n"
"ds_read_b128 a[172:175], v54 offset:44032\n"
"v_exp_f32 v15, v15\n"
"s_waitcnt lgkmcnt(8)\n"
"v_mfma_f32_16x16x32_bf16 v[92:95], a[120:123], v[60:63], v[92:95]\n"
"ds_read_b128 a[176:179], v54 offset:45056\n"
"v_mfma_f32_16x16x32_bf16 v[96:99], a[124:127], v[60:63], v[96:99]\n"
"buffer_load_dwordx4 v4, s[44:47], s90 offen offset:3072 lds\n"
"v_mfma_f32_16x16x32_bf16 v[100:103], a[128:131], v[60:63], v[100:103]\n"
"s_add_u32 m0, s97, 49152\n"
"v_fract_f32 v20, v20\n"
"v_mfma_f32_16x16x32_bf16 v[104:107], a[132:135], v[60:63], v[104:107]\n"
"v_sin_f32 v21, v20\n"
"v_mfma_f32_16x16x32_bf16 v[108:111], a[136:139], v[60:63], v[108:111]\n"
"s_waitcnt lgkmcnt(2) vmcnt(8)\n"
"s_barrier\n"
"v_mfma_f32_16x16x32_bf16 v[72:75], a[140:143], v[64:67], v[72:75]\n"
"ds_read_b128 v[56:59], v8 offset:0\n"
"s_add_u32 s90, s90, 128\n"
"v_mfma_f32_16x16x32_bf16 v[76:79], a[144:147], v[64:67], v[76:79]\n"
"ds_read_b128 a[180:183], v55 offset:46080\n"
"v_mfma_f32_16x16x32_bf16 v[80:83], a[148:151], v[64:67], v[80:83]\n"
"ds_read_b128 a[184:187], v55 offset:47104\n"
"v_cos_f32 v20, v20\n"
"v_mfma_f32_16x16x32_bf16 v[84:87], a[152:155], v[64:67], v[84:87]\n"
"buffer_load_dwordx4 v3, s[40:43], s90 offen lds\n"
"v_mfma_f32_16x16x32_bf16 v[88:91], a[156:159], v[64:67], v[88:91]\n"
"ds_read_b128 a[188:191], v55 offset:48128\n"
"v_mfma_f32_16x16x32_bf16 v[92:95], a[140:143], v[68:71], v[92:95]\n"
"ds_read_b128 v[60:63], v8 offset:1024\n"
"v_mul_f32 v21, v21, v15\n"
"v_mfma_f32_16x16x32_bf16 v[96:99], a[144:147], v[68:71], v[96:99]\n"
"ds_read_b128 a[192:195], v55 offset:49152\n"
"v_mul_f32 v20, v20, v15\n"
"v_mfma_f32_16x16x32_bf16 v[100:103], a[148:151], v[68:71], v[100:103]\n"
"v_mul_f32 v15, 0x41a00000, v17\n"
"v_mfma_f32_16x16x32_bf16 v[104:107], a[152:155], v[68:71], v[104:107]\n"
"buffer_load_dwordx4 v3, s[44:47], s90 offen offset:1024 lds\n"
"ds_read_b128 a[196:199], v55 offset:50176\n"
"v_mfma_f32_16x16x32_bf16 v[108:111], a[156:159], v[68:71], v[108:111]\n"
"v_mul_f32 v26, 0x41a00000, v18\n"
"v_exp_f32 v15, v15\n"
"s_waitcnt lgkmcnt(6)\n"
"v_mfma_f32_16x16x32_bf16 v[72:75], a[160:163], v[56:59], v[72:75]\n"
"ds_read_b128 v[64:67], v9 offset:0\n"
"ds_read_b128 a[200:203], v54 offset:51200\n"
"v_mfma_f32_16x16x32_bf16 v[76:79], a[164:167], v[56:59], v[76:79]\n"
"ds_read_b128 a[204:207], v54 offset:52224\n"
"v_mfma_f32_16x16x32_bf16 v[80:83], a[168:171], v[56:59], v[80:83]\n"
"buffer_load_dwordx4 v4, s[40:43], s90 offen offset:2048 lds\n"
"ds_read_b128 a[208:211], v54 offset:53248\n"
"v_mfma_f32_16x16x32_bf16 v[84:87], a[172:175], v[56:59], v[84:87]\n"
"ds_read_b128 v[68:71], v9 offset:1024\n"
"v_mfma_f32_16x16x32_bf16 v[88:91], a[176:179], v[56:59], v[88:91]\n"
"ds_read_b128 a[212:215], v54 offset:54272\n"
"v_fract_f32 v26, v26\n"
"s_waitcnt lgkmcnt(8)\n"
"v_mfma_f32_16x16x32_bf16 v[92:95], a[160:163], v[60:63], v[92:95]\n"
"ds_read_b128 a[216:219], v54 offset:55296\n"
"v_mfma_f32_16x16x32_bf16 v[96:99], a[164:167], v[60:63], v[96:99]\n"
"buffer_load_dwordx4 v4, s[44:47], s90 offen offset:3072 lds\n"
"v_sin_f32 v27, v26\n"
"v_mfma_f32_16x16x32_bf16 v[100:103], a[168:171], v[60:63], v[100:103]\n"
"s_add_u32 m0, s97, 0\n"
"v_mfma_f32_16x16x32_bf16 v[104:107], a[172:175], v[60:63], v[104:107]\n"
"v_cos_f32 v26, v26\n"
"v_mfma_f32_16x16x32_bf16 v[108:111], a[176:179], v[60:63], v[108:111]\n"
"s_waitcnt lgkmcnt(2) vmcnt(8)\n"
"s_barrier\n"
"v_mfma_f32_16x16x32_bf16 v[72:75], a[180:183], v[64:67], v[72:75]\n"
"ds_read_b128 v[56:59], v8 offset:16384\n"
"s_add_u32 s90, s90, 128\n"
"v_mfma_f32_16x16x32_bf16 v[76:79], a[184:187], v[64:67], v[76:79]\n"
"ds_read_b128 a[220:223], v55 offset:56320\n"
"v_mfma_f32_16x16x32_bf16 v[80:83], a[188:191], v[64:67], v[80:83]\n"
"ds_read_b128 a[224:227], v55 offset:57344\n"
"v_mul_f32 v27, v27, v15\n"
"v_mfma_f32_16x16x32_bf16 v[84:87], a[192:195], v[64:67], v[84:87]\n"
"buffer_load_dwordx4 v3, s[40:43], s90 offen lds\n"
"v_mfma_f32_16x16x32_bf16 v[88:91], a[196:199], v[64:67], v[88:91]\n"
"ds_read_b128 a[228:231], v55 offset:58368\n"
"v_mul_f32 v26, v26, v15\n"
"v_mfma_f32_16x16x32_bf16 v[92:95], a[180:183], v[68:71], v[92:95]\n"
"ds_read_b128 v[60:63], v8 offset:17408\n"
"v_mul_f32 v15, 0x42200000, v17\n"
"v_mfma_f32_16x16x32_bf16 v[96:99], a[184:187], v[68:71], v[96:99]\n"
"ds_read_b128 a[232:235], v55 offset:59392\n"
"v_mul_f32 v28, 0x42200000, v18\n"
"v_mfma_f32_16x16x32_bf16 v[100:103], a[188:191], v[68:71], v[100:103]\n"
"v_exp_f32 v15, v15\n"
"v_mfma_f32_16x16x32_bf16 v[104:107], a[192:195], v[68:71], v[104:107]\n"
"buffer_load_dwordx4 v3, s[44:47], s90 offen offset:1024 lds\n"
"ds_read_b128 a[236:239], v55 offset:60416\n"
"v_mfma_f32_16x16x32_bf16 v[108:111], a[196:199], v[68:71], v[108:111]\n"
"v_fract_f32 v28, v28\n"
"s_waitcnt lgkmcnt(6)\n"
"v_mfma_f32_16x16x32_bf16 v[72:75], a[200:203], v[56:59], v[72:75]\n"
"ds_read_b128 v[64:67], v9 offset:16384\n"
"v_add_u32 v54, 61440, v54\n"
"v_mfma_f32_16x16x32_bf16 v[76:79], a[204:207], v[56:59], v[76:79]\n"
"ds_read_b128 a[240:243], v54 offset:0\n"
"v_mfma_f32_16x16x32_bf16 v[80:83], a[208:211], v[56:59], v[80:83]\n"
"buffer_load_dwordx4 v4, s[40:43], s90 offen offset:2048 lds\n"
"ds_read_b128 a[244:247], v54 offset:1024\n"
"v_mfma_f32_16x16x32_bf16 v[84:87], a[212:215], v[56:59], v[84:87]\n"
"ds_read_b128 v[68:71], v9 offset:17408\n"
"ds_read_b128 a[248:251], v54 offset:2048\n"
"v_mfma_f32_16x16x32_bf16 v[88:91], a[216:219], v[56:59], v[88:91]\n"
"ds_read_b128 a[252:255], v54 offset:3072\n"
"v_sin_f32 v29, v28\n"
"s_waitcnt lgkmcnt(8)\n"
"v_mfma_f32_16x16x32_bf16 v[92:95], a[200:203], v[60:63], v[92:95]\n"
"ds_read_b128 v[152:155], v54 offset:4096\n"
"v_mfma_f32_16x16x32_bf16 v[96:99], a[204:207], v[60:63], v[96:99]\n"
"buffer_load_dwordx4 v4, s[44:47], s90 offen offset:3072 lds\n"
"v_mfma_f32_16x16x32_bf16 v[100:103], a[208:211], v[60:63], v[100:103]\n"
"s_add_u32 m0, s97, 16384\n"
"v_cos_f32 v28, v28\n"
"v_mfma_f32_16x16x32_bf16 v[104:107], a[212:215], v[60:63], v[104:107]\n"
"v_mul_f32 v29, v29, v15\n"
"v_mfma_f32_16x16x32_bf16 v[108:111], a[216:219], v[60:63], v[108:111]\n"
"s_waitcnt lgkmcnt(3) vmcnt(8)\n"
"s_barrier\n"
"v_mfma_f32_16x16x32_bf16 v[72:75], a[220:223], v[64:67], v[72:75]\n"
"ds_read_b128 v[56:59], v8 offset:32768\n"
"s_mov_b32 s90, s72\n"
"v_mfma_f32_16x16x32_bf16 v[76:79], a[224:227], v[64:67], v[76:79]\n"
"v_add_u32 v55, 61440, v55\n"
"v_mfma_f32_16x16x32_bf16 v[80:83], a[228:231], v[64:67], v[80:83]\n"
"ds_read_b128 v[156:159], v55 offset:5120\n"
"v_mul_f32 v28, v28, v15\n"
"v_mfma_f32_16x16x32_bf16 v[84:87], a[232:235], v[64:67], v[84:87]\n"
"buffer_load_dwordx4 v1, s[48:51], s90 offen lds\n"
"v_mul_f32 v15, v13, v13\n"
"v_mfma_f32_16x16x32_bf16 v[88:91], a[236:239], v[64:67], v[88:91]\n"
"ds_read_b128 v[160:163], v55 offset:6144\n"
"v_add_f32 v16, -1.0, v20\n"
"v_mfma_f32_16x16x32_bf16 v[92:95], a[220:223], v[68:71], v[92:95]\n"
"ds_read_b128 v[60:63], v8 offset:33792\n"
"ds_read_b128 v[164:167], v55 offset:7168\n"
"v_mfma_f32_16x16x32_bf16 v[96:99], a[224:227], v[68:71], v[96:99]\n"
"v_fma_f32 v15, v14, v14, v15\n"
"v_mfma_f32_16x16x32_bf16 v[100:103], a[228:231], v[68:71], v[100:103]\n"
"ds_read_b128 v[168:171], v55 offset:8192\n"
"v_mul_f32 v22, v16, v13\n"
"v_mfma_f32_16x16x32_bf16 v[104:107], a[232:235], v[68:71], v[104:107]\n"
"buffer_load_dwordx4 v1, s[52:55], s90 offen offset:1024 lds\n"
"ds_read_b128 v[172:175], v55 offset:9216\n"
"v_mfma_f32_16x16x32_bf16 v[108:111], a[236:239], v[68:71], v[108:111]\n"
"v_rcp_f32 v15, v15\n"
"s_waitcnt lgkmcnt(6)\n"
"v_mfma_f32_16x16x32_bf16 v[72:75], a[240:243], v[56:59], v[72:75]\n"
"ds_read_b128 v[64:67], v9 offset:32768\n"
"ds_read_b128 v[176:179], v54 offset:10240\n"
"v_mfma_f32_16x16x32_bf16 v[76:79], a[244:247], v[56:59], v[76:79]\n"
"ds_read_b128 v[180:183], v54 offset:11264\n"
"v_mfma_f32_16x16x32_bf16 v[80:83], a[248:251], v[56:59], v[80:83]\n"
"buffer_load_dwordx4 v2, s[48:51], s90 offen offset:2048 lds\n"
"ds_read_b128 v[184:187], v54 offset:12288\n"
"v_mfma_f32_16x16x32_bf16 v[84:87], a[252:255], v[56:59], v[84:87]\n"
"ds_read_b128 v[68:71], v9 offset:33792\n"
"v_mul_f32 v23, v21, v13\n"
"v_mfma_f32_16x16x32_bf16 v[88:91], v[152:155], v[56:59], v[88:91]\n"
"ds_read_b128 v[188:191], v54 offset:13312\n"
"s_waitcnt lgkmcnt(9)\n"
"v_mfma_f32_16x16x32_bf16 v[92:95], a[240:243], v[60:63], v[92:95]\n"
"ds_read_b128 v[192:195], v54 offset:14336\n"
"v_fma_f32 v22, v21, v14, v22\n"
"v_mfma_f32_16x16x32_bf16 v[96:99], a[244:247], v[60:63], v[96:99]\n"
"buffer_load_dwordx4 v2, s[52:55], s90 offen offset:3072 lds\n"
"v_fma_f32 v23, -v16, v14, v23\n"
"v_mfma_f32_16x16x32_bf16 v[100:103], a[248:251], v[60:63], v[100:103]\n"
"s_add_u32 m0, s97, 32768\n"
"v_mfma_f32_16x16x32_bf16 v[104:107], a[252:255], v[60:63], v[104:107]\n"
"v_mul_f32 v15, 0x4038aa3b, v15\n"
"v_add_f32 v30, -1.0, v26\n"
"v_mfma_f32_16x16x32_bf16 v[108:111], v[152:155], v[60:63], v[108:111]\n"
"s_waitcnt lgkmcnt(2) vmcnt(8)\n"
"s_barrier\n"
"v_mfma_f32_16x16x32_bf16 v[72:75], v[156:159], v[64:67], v[72:75]\n"
"ds_read_b128 v[56:59], v8 offset:49152\n"
"s_add_u32 s90, s90, 589824\n"
"v_mfma_f32_16x16x32_bf16 v[76:79], v[160:163], v[64:67], v[76:79]\n"
"s_mov_b64 s[40:41], %[cre]\n"
"ds_read_b128 v[196:199], v55 offset:15360\n"
"v_mfma_f32_16x16x32_bf16 v[80:83], v[164:167], v[64:67], v[80:83]\n"
"ds_read_b128 v[200:203], v55 offset:16384\n"
"v_mul_f32 v22, v22, v15\n"
"v_mfma_f32_16x16x32_bf16 v[84:87], v[168:171], v[64:67], v[84:87]\n"
"buffer_load_dwordx4 v1, s[48:51], s90 offen lds\n"
"v_mfma_f32_16x16x32_bf16 v[88:91], v[172:175], v[64:67], v[88:91]\n"
"ds_read_b128 v[204:207], v55 offset:17408\n"
"v_mul_f32 v23, v23, v15\n"
"v_mfma_f32_16x16x32_bf16 v[92:95], v[156:159], v[68:71], v[92:95]\n"
"ds_read_b128 v[60:63], v8 offset:50176\n"
"s_mov_b32 s42, 663552\n"
"v_mfma_f32_16x16x32_bf16 v[96:99], v[160:163], v[68:71], v[96:99]\n"
"ds_read_b128 v[208:211], v55 offset:18432\n"
"v_fma_f32 v30, v32, v30, 1.0\n"
"v_mfma_f32_16x16x32_bf16 v[100:103], v[164:167], v[68:71], v[100:103]\n"
"ds_read_b128 v[212:215], v55 offset:19456\n"
"v_mul_f32 v31, v32, v27\n"
"v_mfma_f32_16x16x32_bf16 v[104:107], v[168:171], v[68:71], v[104:107]\n"
"buffer_load_dwordx4 v1, s[52:55], s90 offen offset:1024 lds\n"
"v_mfma_f32_16x16x32_bf16 v[108:111], v[172:175], v[68:71], v[108:111]\n"
"s_mov_b32 s43, 0x00020000\n"
"v_mul_f32 v30, v33, v30\n"
"s_waitcnt lgkmcnt(6)\n"
"v_mfma_f32_16x16x32_bf16 v[72:75], v[176:179], v[56:59], v[72:75]\n"
"ds_read_b128 v[64:67], v9 offset:49152\n"
"ds_read_b128 v[216:219], v54 offset:20480\n"
"v_mfma_f32_16x16x32_bf16 v[76:79], v[180:183], v[56:59], v[76:79]\n"
"ds_read_b128 v[220:223], v54 offset:21504\n"
"ds_read_b128 v[224:227], v54 offset:22528\n"
"v_mfma_f32_16x16x32_bf16 v[80:83], v[184:187], v[56:59], v[80:83]\n"
"buffer_load_dwordx4 v2, s[48:51], s90 offen offset:2048 lds\n"
"v_mul_f32 v31, v33, v31\n"
"v_mfma_f32_16x16x32_bf16 v[84:87], v[188:191], v[56:59], v[84:87]\n"
"ds_read_b128 v[68:71], v9 offset:50176\n"
"ds_read_b128 v[228:231], v54 offset:23552\n"
"v_mfma_f32_16x16x32_bf16 v[88:91], v[192:195], v[56:59], v[88:91]\n"
"s_mov_b64 s[44:45], %[cim]\n"
"s_sub_u32 s44, s44, 1024\n"
"s_subb_u32 s45, s45, 0\n"
"s_waitcnt lgkmcnt(8)\n"
"v_mfma_f32_16x16x32_bf16 v[92:95], v[176:179], v[60:63], v[92:95]\n"
"ds_read_b128 v[232:235], v54 offset:24576\n"
"v_mfma_f32_16x16x32_bf16 v[96:99], v[180:183], v[60:63], v[96:99]\n"
"buffer_load_dwordx4 v2, s[52:55], s90 offen offset:3072 lds\n"
"s_lshl_b32 s98, %[sl], 2\n"
"v_mfma_f32_16x16x32_bf16 v[100:103], v[184:187], v[60:63], v[100:103]\n"
"s_add_u32 m0, s97, 49152\n"
"s_add_u32 s98, s98, s95\n"
"v_mfma_f32_16x16x32_bf16 v[104:107], v[188:191], v[60:63], v[104:107]\n"
"s_lshl_b32 s98, s98, 13\n"
"v_mfma_f32_16x16x32_bf16 v[108:111], v[192:195], v[60:63], v[108:111]\n"
"s_waitcnt lgkmcnt(2) vmcnt(8)\n"
"s_barrier\n"
"v_mfma_f32_16x16x32_bf16 v[72:75], v[196:199], v[64:67], v[72:75]\n"
"ds_read_b128 v[56:59], v8 offset:0\n"
"s_add_u32 s90, s90, 589824\n"
"v_mfma_f32_16x16x32_bf16 v[76:79], v[200:203], v[64:67], v[76:79]\n"
"ds_read_b128 v[236:239], v55 offset:25600\n"
"s_add_u32 s98, s98, 4096\n"
"v_mfma_f32_16x16x32_bf16 v[80:83], v[204:207], v[64:67], v[80:83]\n"
"ds_read_b128 v[240:243], v55 offset:26624\n"
"v_and_b32 v19, 15, v0\n"
"v_mfma_f32_16x16x32_bf16 v[84:87], v[208:211], v[64:67], v[84:87]\n"
"buffer_load_dwordx4 v1, s[48:51], s90 offen lds\n"
"v_mfma_f32_16x16x32_bf16 v[88:91], v[212:215], v[64:67], v[88:91]\n"
"ds_read_b128 v[244:247], v55 offset:27648\n"
"v_lshlrev_b32 v19, 4, v19\n"
"v_mfma_f32_16x16x32_bf16 v[92:95], v[196:199], v[68:71], v[92:95]\n"
"ds_read_b128 v[60:63], v8 offset:1024\n"
"v_add_u32 v19, s98, v19\n"
"v_mfma_f32_16x16x32_bf16 v[96:99], v[200:203], v[68:71], v[96:99]\n"
"ds_read_b128 v[248:251], v55 offset:28672\n"
"v_mov_b32 v45, s88\n"
"v_mfma_f32_16x16x32_bf16 v[100:103], v[204:207], v[68:71], v[100:103]\n"
"ds_read_b128 v[252:255], v55 offset:29696\n"
"v_mfma_f32_16x16x32_bf16 v[104:107], v[208:211], v[68:71], v[104:107]\n"
"buffer_load_dwordx4 v1, s[52:55], s90 offen offset:1024 lds\n"
"v_mov_b32 v47, s88\n"
"v_mfma_f32_16x16x32_bf16 v[108:111], v[212:215], v[68:71], v[108:111]\n"
"s_mov_b32 s46, 664576\n"
"v_mov_b32 v49, s88\n"
"s_waitcnt lgkmcnt(6)\n"
"v_mfma_f32_16x16x32_bf16 v[72:75], v[216:219], v[56:59], v[72:75]\n"
"ds_read_b128 v[64:67], v9 offset:0\n"
"v_mov_b32 v51, s88\n"
"v_mfma_f32_16x16x32_bf16 v[76:79], v[220:223], v[56:59], v[76:79]\n"
"v_mov_b32 v48, 0\n"
"v_mfma_f32_16x16x32_bf16 v[80:83], v[224:227], v[56:59], v[80:83]\n"
"buffer_load_dwordx4 v2, s[48:51], s90 offen offset:2048 lds\n"
"v_mov_b32 v50, 0\n"
"v_mfma_f32_16x16x32_bf16 v[84:87], v[228:231], v[56:59], v[84:87]\n"
"ds_read_b128 v[68:71], v9 offset:1024\n"
"v_mfma_f32_16x16x32_bf16 v[88:91], v[232:235], v[56:59], v[88:91]\n"
"s_mov_b32 s47, 0x00020000\n"
"s_add_u32 s39, s95, 5\n"
"s_waitcnt lgkmcnt(4)\n"
"v_mfma_f32_16x16x32_bf16 v[92:95], v[216:219], v[60:63], v[92:95]\n"
"s_mul_i32 s39, s39, 10240\n"
"v_add_u32 v53, s39, v12\n"
"v_mfma_f32_16x16x32_bf16 v[96:99], v[220:223], v[60:63], v[96:99]\n"
"buffer_load_dwordx4 v2, s[52:55], s90 offen offset:3072 lds\n"
"v_mfma_f32_16x16x32_bf16 v[100:103], v[224:227], v[60:63], v[100:103]\n"
"s_add_u32 m0, s97, 0\n"
"s_add_u32 s38, %[ti], 1\n"
"v_mfma_f32_16x16x32_bf16 v[104:107], v[228:231], v[60:63], v[104:107]\n"
"s_lshl_b32 s38, s38, 8\n"
"v_mfma_f32_16x16x32_bf16 v[108:111], v[232:235], v[60:63], v[108:111]\n"
"s_waitcnt lgkmcnt(0) vmcnt(8)\n"
"s_barrier\n"
"v_mfma_f32_16x16x32_bf16 v[72:75], v[236:239], v[64:67], v[72:75]\n"
"ds_read_b64_tr_b16 v[56:57], v7 offset:16384\n"
"s_add_u32 s90, s90, 589824\n"
"v_mfma_f32_16x16x32_bf16 v[76:79], v[240:243], v[64:67], v[76:79]\n"
"ds_read_b64_tr_b16 v[58:59], v7 offset:16896\n"
"v_mfma_f32_16x16x32_bf16 v[80:83], v[244:247], v[64:67], v[80:83]\n"
"v_mfma_f32_16x16x32_bf16 v[84:87], v[248:251], v[64:67], v[84:87]\n"
"buffer_load_dwordx4 v1, s[48:51], s90 offen lds\n"
"v_mfma_f32_16x16x32_bf16 v[88:91], v[252:255], v[64:67], v[88:91]\n"
"v_mfma_f32_16x16x32_bf16 v[92:95], v[236:239], v[68:71], v[92:95]\n"
"ds_read_b64_tr_b16 v[60:61], v7 offset:17408\n"
"v_mfma_f32_16x16x32_bf16 v[96:99], v[240:243], v[68:71], v[96:99]\n"
"ds_read_b64_tr_b16 v[62:63], v7 offset:17920\n"
"v_mfma_f32_16x16x32_bf16 v[100:103], v[244:247], v[68:71], v[100:103]\n"
"v_mfma_f32_16x16x32_bf16 v[104:107], v[248:251], v[68:71], v[104:107]\n"
"buffer_load_dwordx4 v1, s[52:55], s90 offen offset:1024 lds\n"
"v_mfma_f32_16x16x32_bf16 v[108:111], v[252:255], v[68:71], v[108:111]\n"
"s_waitcnt lgkmcnt(2)\n"
"v_mfma_f32_16x16x32_bf16 v[112:115], v[56:59], a[0:3], 0\n"
"ds_read_b64_tr_b16 v[64:65], v7 offset:18432\n"
"v_mfma_f32_16x16x32_bf16 v[116:119], v[56:59], a[4:7], 0\n"
"ds_read_b64_tr_b16 v[66:67], v7 offset:18944\n"
"v_mfma_f32_16x16x32_bf16 v[120:123], v[56:59], a[8:11], 0\n"
"buffer_load_dwordx4 v2, s[48:51], s90 offen offset:2048 lds\n"
"v_mfma_f32_16x16x32_bf16 v[124:127], v[56:59], a[12:15], 0\n"
"ds_read_b64_tr_b16 v[68:69], v7 offset:19456\n"
"v_mfma_f32_16x16x32_bf16 v[128:131], v[56:59], a[16:19], 0\n"
"ds_read_b64_tr_b16 v[70:71], v7 offset:19968\n"
"s_waitcnt lgkmcnt(4)\n"
"v_mfma_f32_16x16x32_bf16 v[132:135], v[60:63], a[0:3], 0\n"
"v_mul_f32 v40, v23, v92\n"
"v_mul_f32 v41, v23, v72\n"
"v_mfma_f32_16x16x32_bf16 v[136:139], v[60:63], a[4:7], 0\n"
"buffer_load_dwordx4 v2, s[52:55], s90 offen offset:3072 lds\n"
"v_fma_f32 v72, v22, v72, -v40\n"
"v_mfma_f32_16x16x32_bf16 v[140:143], v[60:63], a[8:11], 0\n"
"s_add_u32 m0, s97, 16384\n"
"v_fma_f32 v92, v22, v92, v41\n"
"v_mfma_f32_16x16x32_bf16 v[144:147], v[60:63], a[12:15], 0\n"
"v_mul_f32 v42, v23, v93\n"
"v_mul_f32 v43, v23, v73\n"
"v_mfma_f32_16x16x32_bf16 v[148:151], v[60:63], a[16:19], 0\n"
"s_waitcnt lgkmcnt(0) vmcnt(8)\n"
"s_barrier\n"
"v_mfma_f32_16x16x32_bf16 v[112:115], v[64:67], a[20:23], v[112:115]\n"
"ds_read_b64_tr_b16 v[56:57], v7 offset:32768\n"
"s_add_u32 s90, s90, 589824\n"
"v_mfma_f32_16x16x32_bf16 v[116:119], v[64:67], a[24:27], v[116:119]\n"
"ds_read_b64_tr_b16 v[58:59], v7 offset:33280\n"
"v_fma_f32 v73, v22, v73, -v42\n"
"v_mfma_f32_16x16x32_bf16 v[120:123], v[64:67], a[28:31], v[120:123]\n"
"v_fma_f32 v93, v22, v93, v43\n"
"v_mul_f32 v40, v23, v94\n"
"v_mfma_f32_16x16x32_bf16 v[124:127], v[64:67], a[32:35], v[124:127]\n"
"buffer_load_dwordx4 v1, s[48:51], s90 offen lds\n"
"v_mul_f32 v41, v23, v74\n"
"v_mfma_f32_16x16x32_bf16 v[128:131], v[64:67], a[36:39], v[128:131]\n"
"v_fma_f32 v74, v22, v74, -v40\n"
"v_fma_f32 v94, v22, v94, v41\n"
"v_mfma_f32_16x16x32_bf16 v[132:135], v[68:71], a[20:23], v[132:135]\n"
"ds_read_b64_tr_b16 v[60:61], v7 offset:33792\n"
"v_mfma_f32_16x16x32_bf16 v[136:139], v[68:71], a[24:27], v[136:139]\n"
"ds_read_b64_tr_b16 v[62:63], v7 offset:34304\n"
"v_mul_f32 v42, v23, v95\n"
"v_mfma_f32_16x16x32_bf16 v[140:143], v[68:71], a[28:31], v[140:143]\n"
"v_mul_f32 v43, v23, v75\n"
"v_fma_f32 v75, v22, v75, -v42\n"
"v_mfma_f32_16x16x32_bf16 v[144:147], v[68:71], a[32:35], v[144:147]\n"
"buffer_load_dwordx4 v1, s[52:55], s90 offen offset:1024 lds\n"
"v_fma_f32 v95, v22, v95, v43\n"
"v_mfma_f32_16x16x32_bf16 v[148:151], v[68:71], a[36:39], v[148:151]\n"
"v_fma_f32 v36, v20, v72, v73\n"
"v_fma_f32 v37, v21, v72, v93\n"
"s_waitcnt lgkmcnt(2)\n"
"v_mfma_f32_16x16x32_bf16 v[112:115], v[56:59], a[40:43], v[112:115]\n"
"ds_read_b64_tr_b16 v[64:65], v7 offset:34816\n"
"v_fma_f32 v34, -v21, v92, v36\n"
"v_mfma_f32_16x16x32_bf16 v[116:119], v[56:59], a[44:47], v[116:119]\n"
"ds_read_b64_tr_b16 v[66:67], v7 offset:35328\n"
"v_fma_f32 v35, v20, v92, v37\n"
"v_mfma_f32_16x16x32_bf16 v[120:123], v[56:59], a[48:51], v[120:123]\n"
"buffer_load_dwordx4 v2, s[48:51], s90 offen offset:2048 lds\n"
"v_fma_f32 v36, v20, v34, v74\n"
"v_mfma_f32_16x16x32_bf16 v[124:127], v[56:59], a[52:55], v[124:127]\n"
"ds_read_b64_tr_b16 v[68:69], v7 offset:35840\n"
"v_fma_f32 v37, v21, v34, v94\n"
"v_mfma_f32_16x16x32_bf16 v[128:131], v[56:59], a[56:59], v[128:131]\n"
"ds_read_b64_tr_b16 v[70:71], v7 offset:36352\n"
"v_fma_f32 v34, -v21, v35, v36\n"
"s_waitcnt lgkmcnt(4)\n"
"v_mfma_f32_16x16x32_bf16 v[132:135], v[60:63], a[40:43], v[132:135]\n"
"v_fma_f32 v35, v20, v35, v37\n"
"v_mfma_f32_16x16x32_bf16 v[136:139], v[60:63], a[44:47], v[136:139]\n"
"buffer_load_dwordx4 v2, s[52:55], s90 offen offset:3072 lds\n"
"v_fma_f32 v36, v20, v34, v75\n"
"v_mfma_f32_16x16x32_bf16 v[140:143], v[60:63], a[48:51], v[140:143]\n"
"s_add_u32 m0, s97, 32768\n"
"v_fma_f32 v37, v21, v34, v95\n"
"v_mfma_f32_16x16x32_bf16 v[144:147], v[60:63], a[52:55], v[144:147]\n"
"v_fma_f32 v34, -v21, v35, v36\n"
"v_fma_f32 v35, v20, v35, v37\n"
"v_mfma_f32_16x16x32_bf16 v[148:151], v[60:63], a[56:59], v[148:151]\n"
"s_waitcnt lgkmcnt(0) vmcnt(8)\n"
"s_barrier\n"
"v_mfma_f32_16x16x32_bf16 v[112:115], v[64:67], a[60:63], v[112:115]\n"
"ds_read_b64_tr_b16 v[56:57], v7 offset:49152\n"
"s_add_u32 s90, s90, 589824\n"
"v_mfma_f32_16x16x32_bf16 v[116:119], v[64:67], a[64:67], v[116:119]\n"
"ds_read_b64_tr_b16 v[58:59], v7 offset:49664\n"
"ds_write_b128 v53, v[72:75] offset:0\n"
"v_mul_f32 v40, v23, v96\n"
"v_mfma_f32_16x16x32_bf16 v[120:123], v[64:67], a[68:71], v[120:123]\n"
"v_mul_f32 v41, v23, v76\n"
"v_fma_f32 v76, v22, v76, -v40\n"
"v_mfma_f32_16x16x32_bf16 v[124:127], v[64:67], a[72:75], v[124:127]\n"
"buffer_load_dwordx4 v1, s[48:51], s90 offen lds\n"
"v_fma_f32 v96, v22, v96, v41\n"
"v_mfma_f32_16x16x32_bf16 v[128:131], v[64:67], a[76:79], v[128:131]\n"
"v_mul_f32 v42, v23, v97\n"
"v_mul_f32 v43, v23, v77\n"
"v_mfma_f32_16x16x32_bf16 v[132:135], v[68:71], a[60:63], v[132:135]\n"
"ds_read_b64_tr_b16 v[60:61], v7 offset:50176\n"
"v_mfma_f32_16x16x32_bf16 v[136:139], v[68:71], a[64:67], v[136:139]\n"
"ds_read_b64_tr_b16 v[62:63], v7 offset:50688\n"
"v_fma_f32 v77, v22, v77, -v42\n"
"v_mfma_f32_16x16x32_bf16 v[140:143], v[68:71], a[68:71], v[140:143]\n"
"v_fma_f32 v97, v22, v97, v43\n"
"v_mul_f32 v40, v23, v98\n"
"v_mfma_f32_16x16x32_bf16 v[144:147], v[68:71], a[72:75], v[144:147]\n"
"buffer_load_dwordx4 v1, s[52:55], s90 offen offset:1024 lds\n"
"v_mul_f32 v41, v23, v78\n"
"v_mfma_f32_16x16x32_bf16 v[148:151], v[68:71], a[76:79], v[148:151]\n"
"v_fma_f32 v78, v22, v78, -v40\n"
"v_fma_f32 v98, v22, v98, v41\n"
"s_waitcnt lgkmcnt(3)\n"
"v_mfma_f32_16x16x32_bf16 v[112:115], v[56:59], a[80:83], v[112:115]\n"
"ds_read_b64_tr_b16 v[64:65], v7 offset:51200\n"
"v_mul_f32 v42, v23, v99\n"
"v_mfma_f32_16x16x32_bf16 v[116:119], v[56:59], a[84:87], v[116:119]\n"
"ds_read_b64_tr_b16 v[66:67], v7 offset:51712\n"
"v_mul_f32 v43, v23, v79\n"
"v_mfma_f32_16x16x32_bf16 v[120:123], v[56:59], a[88:91], v[120:123]\n"
"buffer_load_dwordx4 v2, s[48:51], s90 offen offset:2048 lds\n"
"v_fma_f32 v79, v22, v79, -v42\n"
"v_mfma_f32_16x16x32_bf16 v[124:127], v[56:59], a[92:95], v[124:127]\n"
"ds_read_b64_tr_b16 v[68:69], v7 offset:52224\n"
"v_fma_f32 v99, v22, v99, v43\n"
"v_mfma_f32_16x16x32_bf16 v[128:131], v[56:59], a[96:99], v[128:131]\n"
"ds_read_b64_tr_b16 v[70:71], v7 offset:52736\n"
"v_fma_f32 v36, v20, v34, v76\n"
"s_waitcnt lgkmcnt(4)\n"
"v_mfma_f32_16x16x32_bf16 v[132:135], v[60:63], a[80:83], v[132:135]\n"
"v_fma_f32 v37, v21, v34, v96\n"
"v_mfma_f32_16x16x32_bf16 v[136:139], v[60:63], a[84:87], v[136:139]\n"
"buffer_load_dwordx4 v2, s[52:55], s90 offen offset:3072 lds\n"
"v_fma_f32 v34, -v21, v35, v36\n"
"v_mfma_f32_16x16x32_bf16 v[140:143], v[60:63], a[88:91], v[140:143]\n"
"s_add_u32 m0, s97, 49152\n"
"v_fma_f32 v35, v20, v35, v37\n"
"v_mfma_f32_16x16x32_bf16 v[144:147], v[60:63], a[92:95], v[144:147]\n"
"v_fma_f32 v36, v20, v34, v77\n"
"v_fma_f32 v37, v21, v34, v97\n"
"v_mfma_f32_16x16x32_bf16 v[148:151], v[60:63], a[96:99], v[148:151]\n"
"s_waitcnt lgkmcnt(0) vmcnt(8)\n"
"s_barrier\n"
"v_mfma_f32_16x16x32_bf16 v[112:115], v[64:67], a[100:103], v[112:115]\n"
"ds_read_b64_tr_b16 v[56:57], v7 offset:0\n"
"s_add_u32 s90, s90, 589824\n"
"v_mfma_f32_16x16x32_bf16 v[116:119], v[64:67], a[104:107], v[116:119]\n"
"ds_read_b64_tr_b16 v[58:59], v7 offset:512\n"
"ds_write_b128 v53, v[92:95] offset:5120\n"
"v_fma_f32 v34, -v21, v35, v36\n"
"v_mfma_f32_16x16x32_bf16 v[120:123], v[64:67], a[108:111], v[120:123]\n"
"v_fma_f32 v35, v20, v35, v37\n"
"v_fma_f32 v36, v20, v34, v78\n"
"v_mfma_f32_16x16x32_bf16 v[124:127], v[64:67], a[112:115], v[124:127]\n"
"buffer_load_dwordx4 v1, s[48:51], s90 offen lds\n"
"v_fma_f32 v37, v21, v34, v98\n"
"v_mfma_f32_16x16x32_bf16 v[128:131], v[64:67], a[116:119], v[128:131]\n"
"v_fma_f32 v34, -v21, v35, v36\n"
"v_fma_f32 v35, v20, v35, v37\n"
"v_mfma_f32_16x16x32_bf16 v[132:135], v[68:71], a[100:103], v[132:135]\n"
"ds_read_b64_tr_b16 v[60:61], v7 offset:1024\n"
"v_fma_f32 v36, v20, v34, v79\n"
"v_mfma_f32_16x16x32_bf16 v[136:139], v[68:71], a[104:107], v[136:139]\n"
"ds_read_b64_tr_b16 v[62:63], v7 offset:1536\n"
"v_mfma_f32_16x16x32_bf16 v[140:143], v[68:71], a[108:111], v[140:143]\n"
"v_fma_f32 v37, v21, v34, v99\n"
"v_fma_f32 v34, -v21, v35, v36\n"
"v_mfma_f32_16x16x32_bf16 v[144:147], v[68:71], a[112:115], v[144:147]\n"
"buffer_load_dwordx4 v1, s[52:55], s90 offen offset:1024 lds\n"
"v_fma_f32 v35, v20, v35, v37\n"
"v_mfma_f32_16x16x32_bf16 v[148:151], v[68:71], a[116:119], v[148:151]\n"
"v_mul_f32 v40, v23, v100\n"
"v_mul_f32 v41, v23, v80\n"
"s_waitcnt lgkmcnt(3)\n"
"v_mfma_f32_16x16x32_bf16 v[112:115], v[56:59], a[120:123], v[112:115]\n"
"ds_read_b64_tr_b16 v[64:65], v7 offset:2048\n"
"v_fma_f32 v80, v22, v80, -v40\n"
"v_mfma_f32_16x16x32_bf16 v[116:119], v[56:59], a[124:127], v[116:119]\n"
"ds_read_b64_tr_b16 v[66:67], v7 offset:2560\n"
"v_fma_f32 v100, v22, v100, v41\n"
"v_mfma_f32_16x16x32_bf16 v[120:123], v[56:59], a[128:131], v[120:123]\n"
"buffer_load_dwordx4 v2, s[48:51], s90 offen offset:2048 lds\n"
"v_mul_f32 v42, v23, v101\n"
"v_mfma_f32_16x16x32_bf16 v[124:127], v[56:59], a[132:135], v[124:127]\n"
"ds_read_b64_tr_b16 v[68:69], v7 offset:3072\n"
"v_mul_f32 v43, v23, v81\n"
"v_mfma_f32_16x16x32_bf16 v[128:131], v[56:59], a[136:139], v[128:131]\n"
"ds_read_b64_tr_b16 v[70:71], v7 offset:3584\n"
"v_fma_f32 v81, v22, v81, -v42\n"
"s_waitcnt lgkmcnt(4)\n"
"v_mfma_f32_16x16x32_bf16 v[132:135], v[60:63], a[120:123], v[132:135]\n"
"v_fma_f32 v101, v22, v101, v43\n"
"v_mfma_f32_16x16x32_bf16 v[136:139], v[60:63], a[124:127], v[136:139]\n"
"buffer_load_dwordx4 v2, s[52:55], s90 offen offset:3072 lds\n"
"v_mul_f32 v40, v23, v102\n"
"v_mfma_f32_16x16x32_bf16 v[140:143], v[60:63], a[128:131], v[140:143]\n"
"s_add_u32 m0, s97, 0\n"
"v_mul_f32 v41, v23, v82\n"
"v_mfma_f32_16x16x32_bf16 v[144:147], v[60:63], a[132:135], v[144:147]\n"
"v_fma_f32 v82, v22, v82, -v40\n"
"v_fma_f32 v102, v22, v102, v41\n"
"v_mfma_f32_16x16x32_bf16 v[148:151], v[60:63], a[136:139], v[148:151]\n"
"s_waitcnt lgkmcnt(0) vmcnt(8)\n"
"s_barrier\n"
"v_mfma_f32_16x16x32_bf16 v[112:115], v[64:67], a[140:143], v[112:115]\n"
"ds_read_b64_tr_b16 v[56:57], v7 offset:16384\n"
"s_add_u32 s90, s90, 589824\n"
"v_mfma_f32_16x16x32_bf16 v[116:119], v[64:67], a[144:147], v[116:119]\n"
"ds_read_b64_tr_b16 v[58:59], v7 offset:16896\n"
"ds_write_b128 v53, v[76:79] offset:1024\n"
"v_mul_f32 v42, v23, v103\n"
"v_mfma_f32_16x16x32_bf16 v[120:123], v[64:67], a[148:151], v[120:123]\n"
"v_mul_f32 v43, v23, v83\n"
"v_fma_f32 v83, v22, v83, -v42\n"
"v_mfma_f32_16x16x32_bf16 v[124:127], v[64:67], a[152:155], v[124:127]\n"
"buffer_load_dwordx4 v1, s[48:51], s90 offen lds\n"
"v_fma_f32 v103, v22, v103, v43\n"
"v_mfma_f32_16x16x32_bf16 v[128:131], v[64:67], a[156:159], v[128:131]\n"
"v_fma_f32 v36, v20, v34, v80\n"
"v_fma_f32 v37, v21, v34, v100\n"
"v_mfma_f32_16x16x32_bf16 v[132:135], v[68:71], a[140:143], v[132:135]\n"
"ds_read_b64_tr_b16 v[60:61], v7 offset:17408\n"
"v_fma_f32 v34, -v21, v35, v36\n"
"v_mfma_f32_16x16x32_bf16 v[136:139], v[68:71], a[144:147], v[136:139]\n"
"ds_read_b64_tr_b16 v[62:63], v7 offset:17920\n"
"v_mfma_f32_16x16x32_bf16 v[140:143], v[68:71], a[148:151], v[140:143]\n"
"v_fma_f32 v35, v20, v35, v37\n"
"v_fma_f32 v36, v20, v34, v81\n"
"v_mfma_f32_16x16x32_bf16 v[144:147], v[68:71], a[152:155], v[144:147]\n"
"buffer_load_dwordx4 v1, s[52:55], s90 offen offset:1024 lds\n"
"v_fma_f32 v37, v21, v34, v101\n"
"v_mfma_f32_16x16x32_bf16 v[148:151], v[68:71], a[156:159], v[148:151]\n"
"v_fma_f32 v34, -v21, v35, v36\n"
"v_fma_f32 v35, v20, v35, v37\n"
"s_waitcnt lgkmcnt(3)\n"
"v_mfma_f32_16x16x32_bf16 v[112:115], v[56:59], a[160:163], v[112:115]\n"
"ds_read_b64_tr_b16 v[64:65], v7 offset:18432\n"
"v_fma_f32 v36, v20, v34, v82\n"
"v_mfma_f32_16x16x32_bf16 v[116:119], v[56:59], a[164:167], v[116:119]\n"
"ds_read_b64_tr_b16 v[66:67], v7 offset:18944\n"
"v_fma_f32 v37, v21, v34, v102\n"
"v_mfma_f32_16x16x32_bf16 v[120:123], v[56:59], a[168:171], v[120:123]\n"
"buffer_load_dwordx4 v2, s[48:51], s90 offen offset:2048 lds\n"
"v_fma_f32 v34, -v21, v35, v36\n"
"v_mfma_f32_16x16x32_bf16 v[124:127], v[56:59], a[172:175], v[124:127]\n"
"ds_read_b64_tr_b16 v[68:69], v7 offset:19456\n"
"v_fma_f32 v35, v20, v35, v37\n"
"v_mfma_f32_16x16x32_bf16 v[128:131], v[56:59], a[176:179], v[128:131]\n"
"ds_read_b64_tr_b16 v[70:71], v7 offset:19968\n"
"v_fma_f32 v36, v20, v34, v83\n"
"s_waitcnt lgkmcnt(4)\n"
"v_mfma_f32_16x16x32_bf16 v[132:135], v[60:63], a[160:163], v[132:135]\n"
"v_fma_f32 v37, v21, v34, v103\n"
"v_mfma_f32_16x16x32_bf16 v[136:139], v[60:63], a[164:167], v[136:139]\n"
"buffer_load_dwordx4 v2, s[52:55], s90 offen offset:3072 lds\n"
"v_fma_f32 v34, -v21, v35, v36\n"
"v_mfma_f32_16x16x32_bf16 v[140:143], v[60:63], a[168:171], v[140:143]\n"
"s_add_u32 m0, s97, 16384\n"
"v_fma_f32 v35, v20, v35, v37\n"
"v_mfma_f32_16x16x32_bf16 v[144:147], v[60:63], a[172:175], v[144:147]\n"
"v_mul_f32 v40, v23, v104\n"
"v_mul_f32 v41, v23, v84\n"
"v_mfma_f32_16x16x32_bf16 v[148:151], v[60:63], a[176:179], v[148:151]\n"
"s_waitcnt lgkmcnt(0) vmcnt(8)\n"
"s_barrier\n"
"v_mfma_f32_16x16x32_bf16 v[112:115], v[64:67], a[180:183], v[112:115]\n"
"ds_read_b64_tr_b16 v[56:57], v7 offset:32768\n"
"s_add_u32 s90, s90, 589824\n"
"v_mfma_f32_16x16x32_bf16 v[116:119], v[64:67], a[184:187], v[116:119]\n"
"ds_read_b64_tr_b16 v[58:59], v7 offset:33280\n"
"ds_write_b128 v53, v[96:99] offset:6144\n"
"v_fma_f32 v84, v22, v84, -v40\n"
"v_mfma_f32_16x16x32_bf16 v[120:123], v[64:67], a[188:191], v[120:123]\n"
"v_fma_f32 v104, v22, v104, v41\n"
"v_mul_f32 v42, v23, v105\n"
"v_mfma_f32_16x16x32_bf16 v[124:127], v[64:67], a[192:195], v[124:127]\n"
"buffer_load_dwordx4 v1, s[48:51], s90 offen lds\n"
"v_mul_f32 v43, v23, v85\n"
"v_mfma_f32_16x16x32_bf16 v[128:131], v[64:67], a[196:199], v[128:131]\n"
"v_fma_f32 v85, v22, v85, -v42\n"
"v_fma_f32 v105, v22, v105, v43\n"
"v_mfma_f32_16x16x32_bf16 v[132:135], v[68:71], a[180:183], v[132:135]\n"
"ds_read_b64_tr_b16 v[60:61], v7 offset:33792\n"
"v_mul_f32 v40, v23, v106\n"
"v_mfma_f32_16x16x32_bf16 v[136:139], v[68:71], a[184:187], v[136:139]\n"
"ds_read_b64_tr_b16 v[62:63], v7 offset:34304\n"
"v_mul_f32 v41, v23, v86\n"
"v_mfma_f32_16x16x32_bf16 v[140:143], v[68:71], a[188:191], v[140:143]\n"
"v_fma_f32 v86, v22, v86, -v40\n"
"v_mfma_f32_16x16x32_bf16 v[144:147], v[68:71], a[192:195], v[144:147]\n"
"buffer_load_dwordx4 v1, s[52:55], s90 offen offset:1024 lds\n"
"v_fma_f32 v106, v22, v106, v41\n"
"v_mfma_f32_16x16x32_bf16 v[148:151], v[68:71], a[196:199], v[148:151]\n"
"v_mul_f32 v42, v23, v107\n"
"v_mul_f32 v43, v23, v87\n"
"s_waitcnt lgkmcnt(3)\n"
"v_mfma_f32_16x16x32_bf16 v[112:115], v[56:59], a[200:203], v[112:115]\n"
"ds_read_b64_tr_b16 v[64:65], v7 offset:34816\n"
"v_fma_f32 v87, v22, v87, -v42\n"
"v_mfma_f32_16x16x32_bf16 v[116:119], v[56:59], a[204:207], v[116:119]\n"
"ds_read_b64_tr_b16 v[66:67], v7 offset:35328\n"
"v_fma_f32 v107, v22, v107, v43\n"
"v_mfma_f32_16x16x32_bf16 v[120:123], v[56:59], a[208:211], v[120:123]\n"
"buffer_load_dwordx4 v2, s[48:51], s90 offen offset:2048 lds\n"
"v_fma_f32 v36, v20, v34, v84\n"
"v_mfma_f32_16x16x32_bf16 v[124:127], v[56:59], a[212:215], v[124:127]\n"
"ds_read_b64_tr_b16 v[68:69], v7 offset:35840\n"
"v_fma_f32 v37, v21, v34, v104\n"
"v_mfma_f32_16x16x32_bf16 v[128:131], v[56:59], a[216:219], v[128:131]\n"
"ds_read_b64_tr_b16 v[70:71], v7 offset:36352\n"
"v_fma_f32 v34, -v21, v35, v36\n"
"s_waitcnt lgkmcnt(4)\n"
"v_mfma_f32_16x16x32_bf16 v[132:135], v[60:63], a[200:203], v[132:135]\n"
"v_fma_f32 v35, v20, v35, v37\n"
"v_fma_f32 v36, v20, v34, v85\n"
"v_mfma_f32_16x16x32_bf16 v[136:139], v[60:63], a[204:207], v[136:139]\n"
"buffer_load_dwordx4 v2, s[52:55], s90 offen offset:3072 lds\n"
"v_mfma_f32_16x16x32_bf16 v[140:143], v[60:63], a[208:211], v[140:143]\n"
"s_add_u32 m0, s97, 32768\n"
"v_fma_f32 v37, v21, v34, v105\n"
"v_mfma_f32_16x16x32_bf16 v[144:147], v[60:63], a[212:215], v[144:147]\n"
"v_fma_f32 v34, -v21, v35, v36\n"
"v_fma_f32 v35, v20, v35, v37\n"
"v_mfma_f32_16x16x32_bf16 v[148:151], v[60:63], a[216:219], v[148:151]\n"
"s_waitcnt lgkmcnt(0) vmcnt(8)\n"
"s_barrier\n"
"v_mfma_f32_16x16x32_bf16 v[112:115], v[64:67], a[220:223], v[112:115]\n"
"ds_read_b64_tr_b16 v[56:57], v7 offset:49152\n"
"s_mov_b32 s90, s73\n"
"v_mfma_f32_16x16x32_bf16 v[116:119], v[64:67], a[224:227], v[116:119]\n"
"ds_read_b64_tr_b16 v[58:59], v7 offset:49664\n"
"ds_write_b128 v53, v[80:83] offset:2048\n"
"v_fma_f32 v36, v20, v34, v86\n"
"v_mfma_f32_16x16x32_bf16 v[120:123], v[64:67], a[228:231], v[120:123]\n"
"v_fma_f32 v37, v21, v34, v106\n"
"v_fma_f32 v34, -v21, v35, v36\n"
"v_mfma_f32_16x16x32_bf16 v[124:127], v[64:67], a[232:235], v[124:127]\n"
"buffer_load_dwordx4 v1, s[48:51], s90 offen lds\n"
"v_fma_f32 v35, v20, v35, v37\n"
"v_mfma_f32_16x16x32_bf16 v[128:131], v[64:67], a[236:239], v[128:131]\n"
"v_fma_f32 v36, v20, v34, v87\n"
"v_fma_f32 v37, v21, v34, v107\n"
"v_mfma_f32_16x16x32_bf16 v[132:135], v[68:71], a[220:223], v[132:135]\n"
"ds_read_b64_tr_b16 v[60:61], v7 offset:50176\n"
"v_fma_f32 v34, -v21, v35, v36\n"
"v_mfma_f32_16x16x32_bf16 v[136:139], v[68:71], a[224:227], v[136:139]\n"
"ds_read_b64_tr_b16 v[62:63], v7 offset:50688\n"
"v_fma_f32 v35, v20, v35, v37\n"
"v_mfma_f32_16x16x32_bf16 v[140:143], v[68:71], a[228:231], v[140:143]\n"
"v_mul_f32 v40, v23, v108\n"
"v_mfma_f32_16x16x32_bf16 v[144:147], v[68:71], a[232:235], v[144:147]\n"
"buffer_load_dwordx4 v1, s[52:55], s90 offen offset:1024 lds\n"
"v_mul_f32 v41, v23, v88\n"
"v_mfma_f32_16x16x32_bf16 v[148:151], v[68:71], a[236:239], v[148:151]\n"
"v_fma_f32 v88, v22, v88, -v40\n"
"v_fma_f32 v108, v22, v108, v41\n"
"s_waitcnt lgkmcnt(3)\n"
"v_mfma_f32_16x16x32_bf16 v[112:115], v[56:59], a[240:243], v[112:115]\n"
"ds_read_b64_tr_b16 v[64:65], v7 offset:51200\n"
"v_mul_f32 v42, v23, v109\n"
"v_mfma_f32_16x16x32_bf16 v[116:119], v[56:59], a[244:247], v[116:119]\n"
"ds_read_b64_tr_b16 v[66:67], v7 offset:51712\n"
"v_mul_f32 v43, v23, v89\n"
"v_mfma_f32_16x16x32_bf16 v[120:123], v[56:59], a[248:251], v[120:123]\n"
"buffer_load_dwordx4 v2, s[48:51], s90 offen offset:2048 lds\n"
"v_fma_f32 v89, v22, v89, -v42\n"
"v_mfma_f32_16x16x32_bf16 v[124:127], v[56:59], a[252:255], v[124:127]\n"
"ds_read_b64_tr_b16 v[68:69], v7 offset:52224\n"
"v_fma_f32 v109, v22, v109, v43\n"
"v_mfma_f32_16x16x32_bf16 v[128:131], v[56:59], v[152:155], v[128:131]\n"
"ds_read_b64_tr_b16 v[70:71], v7 offset:52736\n"
"v_mul_f32 v40, v23, v110\n"
"s_waitcnt lgkmcnt(4)\n"
"v_mfma_f32_16x16x32_bf16 v[132:135], v[60:63], a[240:243], v[132:135]\n"
"v_mul_f32 v41, v23, v90\n"
"v_fma_f32 v90, v22, v90, -v40\n"
"v_mfma_f32_16x16x32_bf16 v[136:139], v[60:63], a[244:247], v[136:139]\n"
"buffer_load_dwordx4 v2, s[52:55], s90 offen offset:3072 lds\n"
"v_mfma_f32_16x16x32_bf16 v[140:143], v[60:63], a[248:251], v[140:143]\n"
"s_add_u32 m0, s97, 49152\n"
"v_fma_f32 v110, v22, v110, v41\n"
"v_mfma_f32_16x16x32_bf16 v[144:147], v[60:63], a[252:255], v[144:147]\n"
"v_mul_f32 v42, v23, v111\n"
"v_mul_f32 v43, v23, v91\n"
"v_mfma_f32_16x16x32_bf16 v[148:151], v[60:63], v[152:155], v[148:151]\n"
"s_waitcnt lgkmcnt(0) vmcnt(8)\n"
"s_barrier\n"
"v_mfma_f32_16x16x32_bf16 v[112:115], v[64:67], v[156:159], v[112:115]\n"
"ds_read_b64_tr_b16 v[56:57], v7 offset:0\n"
"s_add_u32 s90, s90, 589824\n"
"v_mfma_f32_16x16x32_bf16 v[116:119], v[64:67], v[160:163], v[116:119]\n"
"ds_read_b64_tr_b16 v[58:59], v7 offset:512\n"
"ds_write_b128 v53, v[100:103] offset:7168\n"
"v_fma_f32 v91, v22, v91, -v42\n"
"v_mfma_f32_16x16x32_bf16 v[120:123], v[64:67], v[164:167], v[120:123]\n"
"v_fma_f32 v111, v22, v111, v43\n"
"v_fma_f32 v36, v20, v34, v88\n"
"v_mfma_f32_16x16x32_bf16 v[124:127], v[64:67], v[168:171], v[124:127]\n"
"buffer_load_dwordx4 v1, s[48:51], s90 offen lds\n"
"v_fma_f32 v37, v21, v34, v108\n"
"v_mfma_f32_16x16x32_bf16 v[128:131], v[64:67], v[172:175], v[128:131]\n"
"v_fma_f32 v34, -v21, v35, v36\n"
"v_fma_f32 v35, v20, v35, v37\n"
"v_mfma_f32_16x16x32_bf16 v[132:135], v[68:71], v[156:159], v[132:135]\n"
"ds_read_b64_tr_b16 v[60:61], v7 offset:1024\n"
"v_fma_f32 v36, v20, v34, v89\n"
"v_mfma_f32_16x16x32_bf16 v[136:139], v[68:71], v[160:163], v[136:139]\n"
"ds_read_b64_tr_b16 v[62:63], v7 offset:1536\n"
"v_fma_f32 v37, v21, v34, v109\n"
"v_mfma_f32_16x16x32_bf16 v[140:143], v[68:71], v[164:167], v[140:143]\n"
"v_fma_f32 v34, -v21, v35, v36\n"
"v_mfma_f32_16x16x32_bf16 v[144:147], v[68:71], v[168:171], v[144:147]\n"
"buffer_load_dwordx4 v1, s[52:55], s90 offen offset:1024 lds\n"
"v_fma_f32 v35, v20, v35, v37\n"
"v_mfma_f32_16x16x32_bf16 v[148:151], v[68:71], v[172:175], v[148:151]\n"
"v_fma_f32 v36, v20, v34, v90\n"
"v_fma_f32 v37, v21, v34, v110\n"
"s_waitcnt lgkmcnt(3)\n"
"v_mfma_f32_16x16x32_bf16 v[112:115], v[56:59], v[176:179], v[112:115]\n"
"ds_read_b64_tr_b16 v[64:65], v7 offset:2048\n"
"v_fma_f32 v34, -v21, v35, v36\n"
"v_mfma_f32_16x16x32_bf16 v[116:119], v[56:59], v[180:183], v[116:119]\n"
"ds_read_b64_tr_b16 v[66:67], v7 offset:2560\n"
"v_fma_f32 v35, v20, v35, v37\n"
"v_mfma_f32_16x16x32_bf16 v[120:123], v[56:59], v[184:187], v[120:123]\n"
"buffer_load_dwordx4 v2, s[48:51], s90 offen offset:2048 lds\n"
"v_fma_f32 v36, v20, v34, v91\n"
"v_mfma_f32_16x16x32_bf16 v[124:127], v[56:59], v[188:191], v[124:127]\n"
"ds_read_b64_tr_b16 v[68:69], v7 offset:3072\n"
"v_fma_f32 v37, v21, v34, v111\n"
"v_mfma_f32_16x16x32_bf16 v[128:131], v[56:59], v[192:195], v[128:131]\n"
"ds_read_b64_tr_b16 v[70:71], v7 offset:3584\n"
"v_fma_f32 v34, -v21, v35, v36\n"
"s_waitcnt lgkmcnt(4)\n"
"v_mfma_f32_16x16x32_bf16 v[132:135], v[60:63], v[176:179], v[132:135]\n"
"v_fma_f32 v35, v20, v35, v37\n"
"v_mov_b32 v38, v34\n"
"v_mfma_f32_16x16x32_bf16 v[136:139], v[60:63], v[180:183], v[136:139]\n"
"buffer_load_dwordx4 v2, s[52:55], s90 offen offset:3072 lds\n"
"v_mov_b32 v39, v35\n"
"v_mfma_f32_16x16x32_bf16 v[140:143], v[60:63], v[184:187], v[140:143]\n"
"s_add_u32 m0, s97, 0\n"
"v_mfma_f32_16x16x32_bf16 v[144:147], v[60:63], v[188:191], v[144:147]\n"
"v_permlane16_swap_b32 v34, v38\n"
"v_mfma_f32_16x16x32_bf16 v[148:151], v[60:63], v[192:195], v[148:151]\n"
"s_waitcnt lgkmcnt(0) vmcnt(8)\n"
"s_barrier\n"
"v_mfma_f32_16x16x32_bf16 v[112:115], v[64:67], v[196:199], v[112:115]\n"
"ds_read_b64_tr_b16 v[56:57], v7 offset:16384\n"
"s_add_u32 s90, s90, 589824\n"
"v_mfma_f32_16x16x32_bf16 v[116:119], v[64:67], v[200:203], v[116:119]\n"
"ds_read_b64_tr_b16 v[58:59], v7 offset:16896\n"
"ds_write_b128 v53, v[84:87] offset:3072\n"
"v_permlane16_swap_b32 v35, v39\n"
"v_mfma_f32_16x16x32_bf16 v[120:123], v[64:67], v[204:207], v[120:123]\n"
"v_fma_f32 v38, v26, v34, v38\n"
"v_fma_f32 v39, v27, v34, v39\n"
"v_mfma_f32_16x16x32_bf16 v[124:127], v[64:67], v[208:211], v[124:127]\n"
"buffer_load_dwordx4 v1, s[48:51], s90 offen lds\n"
"v_fma_f32 v38, -v27, v35, v38\n"
"v_mfma_f32_16x16x32_bf16 v[128:131], v[64:67], v[212:215], v[128:131]\n"
"v_fma_f32 v39, v26, v35, v39\n"
"v_mov_b32 v40, v38\n"
"v_mfma_f32_16x16x32_bf16 v[132:135], v[68:71], v[196:199], v[132:135]\n"
"ds_read_b64_tr_b16 v[60:61], v7 offset:17408\n"
"v_mov_b32 v41, v39\n"
"v_mfma_f32_16x16x32_bf16 v[136:139], v[68:71], v[200:203], v[136:139]\n"
"ds_read_b64_tr_b16 v[62:63], v7 offset:17920\n"
"v_mul_f32 v24, v32, v34\n"
"v_mfma_f32_16x16x32_bf16 v[140:143], v[68:71], v[204:207], v[140:143]\n"
"v_mul_f32 v25, v32, v35\n"
"v_mfma_f32_16x16x32_bf16 v[144:147], v[68:71], v[208:211], v[144:147]\n"
"buffer_load_dwordx4 v1, s[52:55], s90 offen offset:1024 lds\n"
"v_permlane32_swap_b32 v38, v40\n"
"v_mfma_f32_16x16x32_bf16 v[148:151], v[68:71], v[212:215], v[148:151]\n"
"v_permlane32_swap_b32 v39, v41\n"
"v_fma_f32 v44, v28, v38, v40\n"
"s_waitcnt lgkmcnt(3)\n"
"v_mfma_f32_16x16x32_bf16 v[112:115], v[56:59], v[216:219], v[112:115]\n"
"ds_read_b64_tr_b16 v[64:65], v7 offset:18432\n"
"v_fma_f32 v46, v29, v38, v41\n"
"v_mfma_f32_16x16x32_bf16 v[116:119], v[56:59], v[220:223], v[116:119]\n"
"ds_read_b64_tr_b16 v[66:67], v7 offset:18944\n"
"v_fma_f32 v44, -v29, v39, v44\n"
"v_mfma_f32_16x16x32_bf16 v[120:123], v[56:59], v[224:227], v[120:123]\n"
"buffer_load_dwordx4 v2, s[48:51], s90 offen offset:2048 lds\n"
"v_fma_f32 v46, v28, v39, v46\n"
"v_mfma_f32_16x16x32_bf16 v[124:127], v[56:59], v[228:231], v[124:127]\n"
"ds_read_b64_tr_b16 v[68:69], v7 offset:19456\n"
"buffer_store_dwordx4 v[44:47], v19, s[64:67], s38 offen sc1\n"
"v_mfma_f32_16x16x32_bf16 v[128:131], v[56:59], v[232:235], v[128:131]\n"
"ds_read_b64_tr_b16 v[70:71], v7 offset:19968\n"
"buffer_store_dwordx4 v[48:51], v19, s[64:67], 0 offen sc1\n"
"s_waitcnt lgkmcnt(4)\n"
"v_mfma_f32_16x16x32_bf16 v[132:135], v[60:63], v[216:219], v[132:135]\n"
"v_fma_f32 v24, v30, v38, v24\n"
"v_fma_f32 v25, v31, v38, v25\n"
"v_mfma_f32_16x16x32_bf16 v[136:139], v[60:63], v[220:223], v[136:139]\n"
"buffer_load_dwordx4 v2, s[52:55], s90 offen offset:3072 lds\n"
"v_fma_f32 v24, -v31, v39, v24\n"
"v_mfma_f32_16x16x32_bf16 v[140:143], v[60:63], v[224:227], v[140:143]\n"
"s_add_u32 m0, s97, 16384\n"
"v_mfma_f32_16x16x32_bf16 v[144:147], v[60:63], v[228:231], v[144:147]\n"
"v_fma_f32 v25, v30, v39, v25\n"
"v_mfma_f32_16x16x32_bf16 v[148:151], v[60:63], v[232:235], v[148:151]\n"
"s_waitcnt lgkmcnt(0) vmcnt(10)\n"
"s_barrier\n"
"v_mfma_f32_16x16x32_bf16 v[112:115], v[64:67], v[236:239], v[112:115]\n"
"ds_read_b64_tr_b16 v[56:57], v7 offset:32768\n"
"s_add_u32 s90, s90, 589824\n"
"v_mfma_f32_16x16x32_bf16 v[116:119], v[64:67], v[240:243], v[116:119]\n"
"ds_read_b64_tr_b16 v[58:59], v7 offset:33280\n"
"ds_write_b128 v53, v[104:107] offset:8192\n"
"v_mfma_f32_16x16x32_bf16 v[120:123], v[64:67], v[244:247], v[120:123]\n"
"v_mfma_f32_16x16x32_bf16 v[124:127], v[64:67], v[248:251], v[124:127]\n"
"buffer_load_dwordx4 v1, s[48:51], s90 offen lds\n"
"v_mfma_f32_16x16x32_bf16 v[128:131], v[64:67], v[252:255], v[128:131]\n"
"v_mfma_f32_16x16x32_bf16 v[132:135], v[68:71], v[236:239], v[132:135]\n"
"ds_read_b64_tr_b16 v[60:61], v7 offset:33792\n"
"v_mfma_f32_16x16x32_bf16 v[136:139], v[68:71], v[240:243], v[136:139]\n"
"ds_read_b64_tr_b16 v[62:63], v7 offset:34304\n"
"v_mfma_f32_16x16x32_bf16 v[140:143], v[68:71], v[244:247], v[140:143]\n"
"ds_write_b128 v53, v[88:91] offset:4096\n"
"ds_write_b128 v53, v[108:111] offset:9216\n"
"v_mfma_f32_16x16x32_bf16 v[144:147], v[68:71], v[248:251], v[144:147]\n"
"buffer_load_dwordx4 v1, s[52:55], s90 offen offset:1024 lds\n"
"v_mfma_f32_16x16x32_bf16 v[148:151], v[68:71], v[252:255], v[148:151]\n"
"s_waitcnt lgkmcnt(5)\n"
"v_mfma_f32_16x16x32_bf16 v[72:75], v[56:59], a[0:3], 0\n"
"ds_read_b64_tr_b16 v[64:65], v7 offset:34816\n"
"v_mfma_f32_16x16x32_bf16 v[76:79], v[56:59], a[4:7], 0\n"
"ds_read_b64_tr_b16 v[66:67], v7 offset:35328\n"
"v_mfma_f32_16x16x32_bf16 v[80:83], v[56:59], a[8:11], 0\n"
"buffer_load_dwordx4 v2, s[48:51], s90 offen offset:2048 lds\n"
"v_mfma_f32_16x16x32_bf16 v[84:87], v[56:59], a[12:15], 0\n"
"ds_read_b64_tr_b16 v[68:69], v7 offset:35840\n"
"v_mfma_f32_16x16x32_bf16 v[88:91], v[56:59], a[16:19], 0\n"
"ds_read_b64_tr_b16 v[70:71], v7 offset:36352\n"
"s_waitcnt lgkmcnt(6)\n"
"v_mfma_f32_16x16x32_bf16 v[92:95], v[60:63], a[0:3], 0\n"
"v_mul_f32 v132, v112, v132\n"
"v_exp_f32_e64 v112, -v112\n"
"v_mfma_f32_16x16x32_bf16 v[96:99], v[60:63], a[4:7], 0\n"
"buffer_load_dwordx4 v2, s[52:55], s90 offen offset:3072 lds\n"
"v_mfma_f32_16x16x32_bf16 v[100:103], v[60:63], a[8:11], 0\n"
"s_add_u32 m0, s97, 32768\n"
"v_mul_f32 v133, v113, v133\n"
"v_mfma_f32_16x16x32_bf16 v[104:107], v[60:63], a[12:15], 0\n"
"v_exp_f32_e64 v113, -v113\n"
"v_mfma_f32_16x16x32_bf16 v[108:111], v[60:63], a[16:19], 0\n"
"s_waitcnt lgkmcnt(0) vmcnt(10)\n"
"s_barrier\n"
"v_mfma_f32_16x16x32_bf16 v[72:75], v[64:67], a[20:23], v[72:75]\n"
"ds_read_b64_tr_b16 v[56:57], v7 offset:49152\n"
"s_add_u32 s90, s90, 589824\n"
"v_mfma_f32_16x16x32_bf16 v[76:79], v[64:67], a[24:27], v[76:79]\n"
"ds_read_b64_tr_b16 v[58:59], v7 offset:49664\n"
"v_mul_f32 v15, 0x42a00000, v17\n"
"v_mfma_f32_16x16x32_bf16 v[80:83], v[64:67], a[28:31], v[80:83]\n"
"v_mul_f32 v134, v114, v134\n"
"v_exp_f32_e64 v114, -v114\n"
"v_mfma_f32_16x16x32_bf16 v[84:87], v[64:67], a[32:35], v[84:87]\n"
"buffer_load_dwordx4 v1, s[48:51], s90 offen lds\n"
"v_mfma_f32_16x16x32_bf16 v[88:91], v[64:67], a[36:39], v[88:91]\n"
"v_mul_f32 v13, 0x42a00000, v18\n"
"v_mfma_f32_16x16x32_bf16 v[92:95], v[68:71], a[20:23], v[92:95]\n"
"ds_read_b64_tr_b16 v[60:61], v7 offset:50176\n"
"v_mul_f32 v135, v115, v135\n"
"v_mfma_f32_16x16x32_bf16 v[96:99], v[68:71], a[24:27], v[96:99]\n"
"ds_read_b64_tr_b16 v[62:63], v7 offset:50688\n"
"v_exp_f32_e64 v115, -v115\n"
"v_mfma_f32_16x16x32_bf16 v[100:103], v[68:71], a[28:31], v[100:103]\n"
"v_add_f32 v112, 1.0, v112\n"
"v_mfma_f32_16x16x32_bf16 v[104:107], v[68:71], a[32:35], v[104:107]\n"
"buffer_load_dwordx4 v1, s[52:55], s90 offen offset:1024 lds\n"
"v_exp_f32 v15, v15\n"
"v_mfma_f32_16x16x32_bf16 v[108:111], v[68:71], a[36:39], v[108:111]\n"
"v_rcp_f32 v112, v112\n"
"s_waitcnt lgkmcnt(2)\n"
"v_mfma_f32_16x16x32_bf16 v[72:75], v[56:59], a[40:43], v[72:75]\n"
"ds_read_b64_tr_b16 v[64:65], v7 offset:51200\n"
"v_add_f32 v113, 1.0, v113\n"
"v_mfma_f32_16x16x32_bf16 v[76:79], v[56:59], a[44:47], v[76:79]\n"
"ds_read_b64_tr_b16 v[66:67], v7 offset:51712\n"
"v_rcp_f32 v113, v113\n"
"v_mfma_f32_16x16x32_bf16 v[80:83], v[56:59], a[48:51], v[80:83]\n"
"buffer_load_dwordx4 v2, s[48:51], s90 offen offset:2048 lds\n"
"v_mfma_f32_16x16x32_bf16 v[84:87], v[56:59], a[52:55], v[84:87]\n"
"ds_read_b64_tr_b16 v[68:69], v7 offset:52224\n"
"v_mfma_f32_16x16x32_bf16 v[88:91], v[56:59], a[56:59], v[88:91]\n"
"ds_read_b64_tr_b16 v[70:71], v7 offset:52736\n"
"v_add_f32 v114, 1.0, v114\n"
"s_waitcnt lgkmcnt(4)\n"
"v_mfma_f32_16x16x32_bf16 v[92:95], v[60:63], a[40:43], v[92:95]\n"
"v_rcp_f32 v114, v114\n"
"v_mfma_f32_16x16x32_bf16 v[96:99], v[60:63], a[44:47], v[96:99]\n"
"buffer_load_dwordx4 v2, s[52:55], s90 offen offset:3072 lds\n"
"v_add_f32 v115, 1.0, v115\n"
"v_mfma_f32_16x16x32_bf16 v[100:103], v[60:63], a[48:51], v[100:103]\n"
"s_add_u32 m0, s97, 49152\n"
"v_fract_f32 v13, v13\n"
"v_mfma_f32_16x16x32_bf16 v[104:107], v[60:63], a[52:55], v[104:107]\n"
"v_rcp_f32 v115, v115\n"
"v_mfma_f32_16x16x32_bf16 v[108:111], v[60:63], a[56:59], v[108:111]\n"
"s_waitcnt lgkmcnt(0) vmcnt(8)\n"
"s_barrier\n"
"v_mfma_f32_16x16x32_bf16 v[72:75], v[64:67], a[60:63], v[72:75]\n"
"ds_read_b64_tr_b16 v[56:57], v7 offset:0\n"
"s_add_u32 s90, s90, 589824\n"
"v_mfma_f32_16x16x32_bf16 v[76:79], v[64:67], a[64:67], v[76:79]\n"
"ds_read_b64_tr_b16 v[58:59], v7 offset:512\n"
"v_mul_f32 v112, v112, v132\n"
"v_mfma_f32_16x16x32_bf16 v[80:83], v[64:67], a[68:71], v[80:83]\n"
"v_sin_f32 v14, v13\n"
"v_mfma_f32_16x16x32_bf16 v[84:87], v[64:67], a[72:75], v[84:87]\n"
"buffer_load_dwordx4 v1, s[48:51], s90 offen lds\n"
"v_mul_f32 v113, v113, v133\n"
"v_mfma_f32_16x16x32_bf16 v[88:91], v[64:67], a[76:79], v[88:91]\n"
"v_mul_f32 v114, v114, v134\n"
"v_mfma_f32_16x16x32_bf16 v[92:95], v[68:71], a[60:63], v[92:95]\n"
"ds_read_b64_tr_b16 v[60:61], v7 offset:1024\n"
"v_mul_f32 v115, v115, v135\n"
"v_mfma_f32_16x16x32_bf16 v[96:99], v[68:71], a[64:67], v[96:99]\n"
"ds_read_b64_tr_b16 v[62:63], v7 offset:1536\n"
"v_cvt_pk_bf16_f32 v132, v112, v113\n"
"v_mfma_f32_16x16x32_bf16 v[100:103], v[68:71], a[68:71], v[100:103]\n"
"v_cvt_pk_bf16_f32 v133, v114, v115\n"
"v_mul_f32 v136, v116, v136\n"
"v_mfma_f32_16x16x32_bf16 v[104:107], v[68:71], a[72:75], v[104:107]\n"
"buffer_load_dwordx4 v1, s[52:55], s90 offen offset:1024 lds\n"
"v_exp_f32_e64 v116, -v116\n"
"v_mfma_f32_16x16x32_bf16 v[108:111], v[68:71], a[76:79], v[108:111]\n"
"v_mul_f32 v137, v117, v137\n"
"s_waitcnt lgkmcnt(2)\n"
"v_mfma_f32_16x16x32_bf16 v[72:75], v[56:59], a[80:83], v[72:75]\n"
"ds_read_b64_tr_b16 v[64:65], v7 offset:2048\n"
"v_cos_f32 v13, v13\n"
"v_mfma_f32_16x16x32_bf16 v[76:79], v[56:59], a[84:87], v[76:79]\n"
"ds_read_b64_tr_b16 v[66:67], v7 offset:2560\n"
"v_exp_f32_e64 v117, -v117\n"
"v_mfma_f32_16x16x32_bf16 v[80:83], v[56:59], a[88:91], v[80:83]\n"
"buffer_load_dwordx4 v2, s[48:51], s90 offen offset:2048 lds\n"
"v_mfma_f32_16x16x32_bf16 v[84:87], v[56:59], a[92:95], v[84:87]\n"
"ds_read_b64_tr_b16 v[68:69], v7 offset:3072\n"
"v_mfma_f32_16x16x32_bf16 v[88:91], v[56:59], a[96:99], v[88:91]\n"
"ds_read_b64_tr_b16 v[70:71], v7 offset:3584\n"
"v_mul_f32 v138, v118, v138\n"
"s_waitcnt lgkmcnt(4)\n"
"v_mfma_f32_16x16x32_bf16 v[92:95], v[60:63], a[80:83], v[92:95]\n"
"v_exp_f32_e64 v118, -v118\n"
"v_mfma_f32_16x16x32_bf16 v[96:99], v[60:63], a[84:87], v[96:99]\n"
"buffer_load_dwordx4 v2, s[52:55], s90 offen offset:3072 lds\n"
"v_mul_f32 v139, v119, v139\n"
"v_mfma_f32_16x16x32_bf16 v[100:103], v[60:63], a[88:91], v[100:103]\n"
"s_add_u32 m0, s97, 0\n"
"v_exp_f32_e64 v119, -v119\n"
"v_mfma_f32_16x16x32_bf16 v[104:107], v[60:63], a[92:95], v[104:107]\n"
"v_mul_f32 v14, v14, v15\n"
"v_mfma_f32_16x16x32_bf16 v[108:111], v[60:63], a[96:99], v[108:111]\n"
"s_waitcnt lgkmcnt(0) vmcnt(8)\n"
"s_barrier\n"
"v_mfma_f32_16x16x32_bf16 v[72:75], v[64:67], a[100:103], v[72:75]\n"
"ds_read_b64_tr_b16 v[56:57], v7 offset:16384\n"
"s_add_u32 s90, s90, 589824\n"
"v_mfma_f32_16x16x32_bf16 v[76:79], v[64:67], a[104:107], v[76:79]\n"
"ds_read_b64_tr_b16 v[58:59], v7 offset:16896\n"
"ds_write_b64 v11, v[132:133] offset:20480\n"
"v_add_f32 v116, 1.0, v116\n"
"v_mfma_f32_16x16x32_bf16 v[80:83], v[64:67], a[108:111], v[80:83]\n"
"v_rcp_f32 v116, v116\n"
"v_mfma_f32_16x16x32_bf16 v[84:87], v[64:67], a[112:115], v[84:87]\n"
"buffer_load_dwordx4 v1, s[48:51], s90 offen lds\n"
"v_mfma_f32_16x16x32_bf16 v[88:91], v[64:67], a[116:119], v[88:91]\n"
"v_mul_f32 v13, v13, v15\n"
"v_add_f32 v117, 1.0, v117\n"
"v_mfma_f32_16x16x32_bf16 v[92:95], v[68:71], a[100:103], v[92:95]\n"
"ds_read_b64_tr_b16 v[60:61], v7 offset:17408\n"
"v_rcp_f32 v117, v117\n"
"v_mfma_f32_16x16x32_bf16 v[96:99], v[68:71], a[104:107], v[96:99]\n"
"ds_read_b64_tr_b16 v[62:63], v7 offset:17920\n"
"v_mfma_f32_16x16x32_bf16 v[100:103], v[68:71], a[108:111], v[100:103]\n"
"v_add_f32 v118, 1.0, v118\n"
"v_rcp_f32 v118, v118\n"
"v_mfma_f32_16x16x32_bf16 v[104:107], v[68:71], a[112:115], v[104:107]\n"
"buffer_load_dwordx4 v1, s[52:55], s90 offen offset:1024 lds\n"
"v_lshrrev_b32 v15, 4, v0\n"
"v_mfma_f32_16x16x32_bf16 v[108:111], v[68:71], a[116:119], v[108:111]\n"
"v_add_f32 v119, 1.0, v119\n"
"s_waitcnt lgkmcnt(3)\n"
"v_mfma_f32_16x16x32_bf16 v[72:75], v[56:59], a[120:123], v[72:75]\n"
"ds_read_b64_tr_b16 v[64:65], v7 offset:18432\n"
"v_mfma_f32_16x16x32_bf16 v[76:79], v[56:59], a[124:127], v[76:79]\n"
"ds_read_b64_tr_b16 v[66:67], v7 offset:18944\n"
"v_rcp_f32 v119, v119\n"
"v_mfma_f32_16x16x32_bf16 v[80:83], v[56:59], a[128:131], v[80:83]\n"
"buffer_load_dwordx4 v2, s[48:51], s90 offen offset:2048 lds\n"
"v_mul_u32_u24 v15, 1792, v15\n"
"v_mfma_f32_16x16x32_bf16 v[84:87], v[56:59], a[132:135], v[84:87]\n"
"ds_read_b64_tr_b16 v[68:69], v7 offset:19456\n"
"v_mfma_f32_16x16x32_bf16 v[88:91], v[56:59], a[136:139], v[88:91]\n"
"ds_read_b64_tr_b16 v[70:71], v7 offset:19968\n"
"v_mul_f32 v116, v116, v136\n"
"s_waitcnt lgkmcnt(4)\n"
"v_mfma_f32_16x16x32_bf16 v[92:95], v[60:63], a[120:123], v[92:95]\n"
"v_mul_f32 v117, v117, v137\n"
"v_mul_f32 v118, v118, v138\n"
"v_mfma_f32_16x16x32_bf16 v[96:99], v[60:63], a[124:127], v[96:99]\n"
"buffer_load_dwordx4 v2, s[52:55], s90 offen offset:3072 lds\n"
"v_mul_f32 v119, v119, v139\n"
"v_mfma_f32_16x16x32_bf16 v[100:103], v[60:63], a[128:131], v[100:103]\n"
"s_add_u32 m0, s97, 16384\n"
"s_lshl_b32 s98, %[ti], 8\n"
"v_mfma_f32_16x16x32_bf16 v[104:107], v[60:63], a[132:135], v[104:107]\n"
"v_cvt_pk_bf16_f32 v136, v116, v117\n"
"v_cvt_pk_bf16_f32 v137, v118, v119\n"
"v_mfma_f32_16x16x32_bf16 v[108:111], v[60:63], a[136:139], v[108:111]\n"
"s_waitcnt lgkmcnt(0) vmcnt(8)\n"
"s_barrier\n"
"v_mfma_f32_16x16x32_bf16 v[72:75], v[64:67], a[140:143], v[72:75]\n"
"ds_read_b64_tr_b16 v[56:57], v7 offset:32768\n"
"s_add_u32 s90, s90, 589824\n"
"v_mfma_f32_16x16x32_bf16 v[76:79], v[64:67], a[144:147], v[76:79]\n"
"ds_read_b64_tr_b16 v[58:59], v7 offset:33280\n"
"ds_write_b64 v11, v[136:137] offset:21504\n"
"v_mul_f32 v140, v120, v140\n"
"v_mfma_f32_16x16x32_bf16 v[80:83], v[64:67], a[148:151], v[80:83]\n"
"v_add_u32 v54, s98, v19\n"
"v_mfma_f32_16x16x32_bf16 v[84:87], v[64:67], a[152:155], v[84:87]\n"
"buffer_load_dwordx4 v1, s[48:51], s90 offen lds\n"
"v_exp_f32_e64 v120, -v120\n"
"v_mfma_f32_16x16x32_bf16 v[88:91], v[64:67], a[156:159], v[88:91]\n"
"v_mul_f32 v141, v121, v141\n"
"v_mfma_f32_16x16x32_bf16 v[92:95], v[68:71], a[140:143], v[92:95]\n"
"ds_read_b64_tr_b16 v[60:61], v7 offset:33792\n"
"v_exp_f32_e64 v121, -v121\n"
"v_mfma_f32_16x16x32_bf16 v[96:99], v[68:71], a[144:147], v[96:99]\n"
"ds_read_b64_tr_b16 v[62:63], v7 offset:34304\n"
"v_sub_u32 v54, v54, v15\n"
"v_mfma_f32_16x16x32_bf16 v[100:103], v[68:71], a[148:151], v[100:103]\n"
"v_mul_f32 v142, v122, v142\n"
"v_mfma_f32_16x16x32_bf16 v[104:107], v[68:71], a[152:155], v[104:107]\n"
"buffer_load_dwordx4 v1, s[52:55], s90 offen offset:1024 lds\n"
"v_exp_f32_e64 v122, -v122\n"
"v_mfma_f32_16x16x32_bf16 v[108:111], v[68:71], a[156:159], v[108:111]\n"
"s_waitcnt lgkmcnt(3)\n"
"v_mfma_f32_16x16x32_bf16 v[72:75], v[56:59], a[160:163], v[72:75]\n"
"ds_read_b64_tr_b16 v[64:65], v7 offset:34816\n"
"v_mul_f32 v143, v123, v143\n"
"v_mfma_f32_16x16x32_bf16 v[76:79], v[56:59], a[164:167], v[76:79]\n"
"ds_read_b64_tr_b16 v[66:67], v7 offset:35328\n"
"v_exp_f32_e64 v123, -v123\n"
"v_mfma_f32_16x16x32_bf16 v[80:83], v[56:59], a[168:171], v[80:83]\n"
"buffer_load_dwordx4 v2, s[48:51], s90 offen offset:2048 lds\n"
"v_mfma_f32_16x16x32_bf16 v[84:87], v[56:59], a[172:175], v[84:87]\n"
"ds_read_b64_tr_b16 v[68:69], v7 offset:35840\n"
"v_add_f32 v120, 1.0, v120\n"
"v_mfma_f32_16x16x32_bf16 v[88:91], v[56:59], a[176:179], v[88:91]\n"
"ds_read_b64_tr_b16 v[70:71], v7 offset:36352\n"
"v_rcp_f32 v120, v120\n"
"s_waitcnt lgkmcnt(4)\n"
"v_mfma_f32_16x16x32_bf16 v[92:95], v[60:63], a[160:163], v[92:95]\n"
"v_add_f32 v121, 1.0, v121\n"
"v_mfma_f32_16x16x32_bf16 v[96:99], v[60:63], a[164:167], v[96:99]\n"
"buffer_load_dwordx4 v2, s[52:55], s90 offen offset:3072 lds\n"
"v_rcp_f32 v121, v121\n"
"v_mfma_f32_16x16x32_bf16 v[100:103], v[60:63], a[168:171], v[100:103]\n"
"s_add_u32 m0, s97, 32768\n"
"v_mfma_f32_16x16x32_bf16 v[104:107], v[60:63], a[172:175], v[104:107]\n"
"v_add_f32 v122, 1.0, v122\n"
"v_mfma_f32_16x16x32_bf16 v[108:111], v[60:63], a[176:179], v[108:111]\n"
"s_waitcnt lgkmcnt(0) vmcnt(8)\n"
"s_barrier\n"
"v_mfma_f32_16x16x32_bf16 v[72:75], v[64:67], a[180:183], v[72:75]\n"
"ds_read_b64_tr_b16 v[56:57], v7 offset:49152\n"
"s_add_u32 s90, s90, 589824\n"
"v_mfma_f32_16x16x32_bf16 v[76:79], v[64:67], a[184:187], v[76:79]\n"
"ds_read_b64_tr_b16 v[58:59], v7 offset:49664\n"
"v_rcp_f32 v122, v122\n"
"v_mfma_f32_16x16x32_bf16 v[80:83], v[64:67], a[188:191], v[80:83]\n"
"v_add_f32 v123, 1.0, v123\n"
"v_mfma_f32_16x16x32_bf16 v[84:87], v[64:67], a[192:195], v[84:87]\n"
"buffer_load_dwordx4 v1, s[48:51], s90 offen lds\n"
"v_rcp_f32 v123, v123\n"
"v_mfma_f32_16x16x32_bf16 v[88:91], v[64:67], a[196:199], v[88:91]\n"
"v_mul_f32 v120, v120, v140\n"
"v_mfma_f32_16x16x32_bf16 v[92:95], v[68:71], a[180:183], v[92:95]\n"
"ds_read_b64_tr_b16 v[60:61], v7 offset:50176\n"
"v_mul_f32 v121, v121, v141\n"
"v_mfma_f32_16x16x32_bf16 v[96:99], v[68:71], a[184:187], v[96:99]\n"
"ds_read_b64_tr_b16 v[62:63], v7 offset:50688\n"
"v_mul_f32 v122, v122, v142\n"
"v_mfma_f32_16x16x32_bf16 v[100:103], v[68:71], a[188:191], v[100:103]\n"
"v_mul_f32 v123, v123, v143\n"
"v_mfma_f32_16x16x32_bf16 v[104:107], v[68:71], a[192:195], v[104:107]\n"
"buffer_load_dwordx4 v1, s[52:55], s90 offen offset:1024 lds\n"
"v_cvt_pk_bf16_f32 v140, v120, v121\n"
"v_mfma_f32_16x16x32_bf16 v[108:111], v[68:71], a[196:199], v[108:111]\n"
"v_cvt_pk_bf16_f32 v141, v122, v123\n"
"v_mul_f32 v144, v124, v144\n"
"s_waitcnt lgkmcnt(2)\n"
"v_mfma_f32_16x16x32_bf16 v[72:75], v[56:59], a[200:203], v[72:75]\n"
"ds_read_b64_tr_b16 v[64:65], v7 offset:51200\n"
"v_exp_f32_e64 v124, -v124\n"
"v_mfma_f32_16x16x32_bf16 v[76:79], v[56:59], a[204:207], v[76:79]\n"
"ds_read_b64_tr_b16 v[66:67], v7 offset:51712\n"
"v_mfma_f32_16x16x32_bf16 v[80:83], v[56:59], a[208:211], v[80:83]\n"
"buffer_load_dwordx4 v2, s[48:51], s90 offen offset:2048 lds\n"
"v_mul_f32 v145, v125, v145\n"
"v_mfma_f32_16x16x32_bf16 v[84:87], v[56:59], a[212:215], v[84:87]\n"
"ds_read_b64_tr_b16 v[68:69], v7 offset:52224\n"
"v_exp_f32_e64 v125, -v125\n"
"v_mfma_f32_16x16x32_bf16 v[88:91], v[56:59], a[216:219], v[88:91]\n"
"ds_read_b64_tr_b16 v[70:71], v7 offset:52736\n"
"s_waitcnt lgkmcnt(4)\n"
"v_mfma_f32_16x16x32_bf16 v[92:95], v[60:63], a[200:203], v[92:95]\n"
"v_mul_f32 v146, v126, v146\n"
"v_mfma_f32_16x16x32_bf16 v[96:99], v[60:63], a[204:207], v[96:99]\n"
"buffer_load_dwordx4 v2, s[52:55], s90 offen offset:3072 lds\n"
"v_exp_f32_e64 v126, -v126\n"
"v_mfma_f32_16x16x32_bf16 v[100:103], v[60:63], a[208:211], v[100:103]\n"
"s_add_u32 m0, s97, 49152\n"
"v_mfma_f32_16x16x32_bf16 v[104:107], v[60:63], a[212:215], v[104:107]\n"
"v_mul_f32 v147, v127, v147\n"
"v_exp_f32_e64 v127, -v127\n"
"v_mfma_f32_16x16x32_bf16 v[108:111], v[60:63], a[216:219], v[108:111]\n"
"s_waitcnt lgkmcnt(0) vmcnt(8)\n"
"s_barrier\n"
"v_mfma_f32_16x16x32_bf16 v[72:75], v[64:67], a[220:223], v[72:75]\n"
"ds_read_b64_tr_b16 v[56:57], v7 offset:0\n"
"s_mov_b32 s90, s74\n"
"v_mfma_f32_16x16x32_bf16 v[76:79], v[64:67], a[224:227], v[76:79]\n"
"ds_read_b64_tr_b16 v[58:59], v7 offset:512\n"
"ds_write_b64 v11, v[140:141] offset:22528\n"
"v_mfma_f32_16x16x32_bf16 v[80:83], v[64:67], a[228:231], v[80:83]\n"
"v_add_f32 v124, 1.0, v124\n"
"v_rcp_f32 v124, v124\n"
"v_mfma_f32_16x16x32_bf16 v[84:87], v[64:67], a[232:235], v[84:87]\n"
"buffer_load_dwordx4 v1, s[48:51], s90 offen lds\n"
"v_mfma_f32_16x16x32_bf16 v[88:91], v[64:67], a[236:239], v[88:91]\n"
"v_add_f32 v125, 1.0, v125\n"
"v_mfma_f32_16x16x32_bf16 v[92:95], v[68:71], a[220:223], v[92:95]\n"
"ds_read_b64_tr_b16 v[60:61], v7 offset:1024\n"
"v_rcp_f32 v125, v125\n"
"v_mfma_f32_16x16x32_bf16 v[96:99], v[68:71], a[224:227], v[96:99]\n"
"ds_read_b64_tr_b16 v[62:63], v7 offset:1536\n"
"v_mfma_f32_16x16x32_bf16 v[100:103], v[68:71], a[228:231], v[100:103]\n"
"v_add_f32 v126, 1.0, v126\n"
"v_rcp_f32 v126, v126\n"
"v_mfma_f32_16x16x32_bf16 v[104:107], v[68:71], a[232:235], v[104:107]\n"
"buffer_load_dwordx4 v1, s[52:55], s90 offen offset:1024 lds\n"
"v_mfma_f32_16x16x32_bf16 v[108:111], v[68:71], a[236:239], v[108:111]\n"
"v_add_f32 v127, 1.0, v127\n"
"s_waitcnt lgkmcnt(3)\n"
"v_mfma_f32_16x16x32_bf16 v[72:75], v[56:59], a[240:243], v[72:75]\n"
"ds_read_b64_tr_b16 v[64:65], v7 offset:2048\n"
"v_subrev_u32 v26, 0, v54\n"
"v_mfma_f32_16x16x32_bf16 v[76:79], v[56:59], a[244:247], v[76:79]\n"
"ds_read_b64_tr_b16 v[66:67], v7 offset:2560\n"
"v_rcp_f32 v127, v127\n"
"v_mfma_f32_16x16x32_bf16 v[80:83], v[56:59], a[248:251], v[80:83]\n"
"buffer_load_dwordx4 v2, s[48:51], s90 offen offset:2048 lds\n"
"v_max_i32 v26, v26, v19\n"
"v_mfma_f32_16x16x32_bf16 v[84:87], v[56:59], a[252:255], v[84:87]\n"
"ds_read_b64_tr_b16 v[68:69], v7 offset:3072\n"
"v_mfma_f32_16x16x32_bf16 v[88:91], v[56:59], v[152:155], v[88:91]\n"
"ds_read_b64_tr_b16 v[70:71], v7 offset:3584\n"
"v_mul_f32 v124, v124, v144\n"
"s_waitcnt lgkmcnt(4)\n"
"v_mfma_f32_16x16x32_bf16 v[92:95], v[60:63], a[240:243], v[92:95]\n"
"buffer_load_dwordx4 v[26:29], v26, s[64:67], 0 offen sc1\n"
"v_mul_f32 v125, v125, v145\n"
"v_mfma_f32_16x16x32_bf16 v[96:99], v[60:63], a[244:247], v[96:99]\n"
"buffer_load_dwordx4 v2, s[52:55], s90 offen offset:3072 lds\n"
"v_mul_f32 v126, v126, v146\n"
"v_mfma_f32_16x16x32_bf16 v[100:103], v[60:63], a[248:251], v[100:103]\n"
"s_add_u32 m0, s97, 0\n"
"v_subrev_u32 v30, 256, v54\n"
"v_mfma_f32_16x16x32_bf16 v[104:107], v[60:63], a[252:255], v[104:107]\n"
"v_mul_f32 v127, v127, v147\n"
"v_cvt_pk_bf16_f32 v144, v124, v125\n"
"v_mfma_f32_16x16x32_bf16 v[108:111], v[60:63], v[152:155], v[108:111]\n"
"s_waitcnt lgkmcnt(0) vmcnt(9)\n"
"s_barrier\n"
"v_mfma_f32_16x16x32_bf16 v[72:75], v[64:67], v[156:159], v[72:75]\n"
"ds_read_b64_tr_b16 v[56:57], v7 offset:16384\n"
"s_add_u32 s90, s90, 589824\n"
"v_mfma_f32_16x16x32_bf16 v[76:79], v[64:67], v[160:163], v[76:79]\n"
"ds_read_b64_tr_b16 v[58:59], v7 offset:16896\n"
"v_max_i32 v30, v30, v19\n"
"v_mfma_f32_16x16x32_bf16 v[80:83], v[64:67], v[164:167], v[80:83]\n"
"v_cvt_pk_bf16_f32 v145, v126, v127\n"
"ds_write_b64 v11, v[144:145] offset:23552\n"
"v_mul_f32 v148, v128, v148\n"
"v_mfma_f32_16x16x32_bf16 v[84:87], v[64:67], v[168:171], v[84:87]\n"
"buffer_load_dwordx4 v1, s[48:51], s90 offen lds\n"
"buffer_load_dwordx4 v[30:33], v30, s[64:67], 0 offen sc1\n"
"v_mfma_f32_16x16x32_bf16 v[88:91], v[64:67], v[172:175], v[88:91]\n"
"v_subrev_u32 v34, 512, v54\n"
"v_mfma_f32_16x16x32_bf16 v[92:95], v[68:71], v[156:159], v[92:95]\n"
"ds_read_b64_tr_b16 v[60:61], v7 offset:17408\n"
"v_exp_f32_e64 v128, -v128\n"
"v_mfma_f32_16x16x32_bf16 v[96:99], v[68:71], v[160:163], v[96:99]\n"
"ds_read_b64_tr_b16 v[62:63], v7 offset:17920\n"
"v_mfma_f32_16x16x32_bf16 v[100:103], v[68:71], v[164:167], v[100:103]\n"
"v_max_i32 v34, v34, v19\n"
"v_mul_f32 v149, v129, v149\n"
"v_mfma_f32_16x16x32_bf16 v[104:107], v[68:71], v[168:171], v[104:107]\n"
"buffer_load_dwordx4 v1, s[52:55], s90 offen offset:1024 lds\n"
"v_exp_f32_e64 v129, -v129\n"
"v_mfma_f32_16x16x32_bf16 v[108:111], v[68:71], v[172:175], v[108:111]\n"
"buffer_load_dwordx4 v[34:37], v34, s[64:67], 0 offen sc1\n"
"s_waitcnt lgkmcnt(3)\n"
"v_mfma_f32_16x16x32_bf16 v[72:75], v[56:59], v[176:179], v[72:75]\n"
"ds_read_b64_tr_b16 v[64:65], v7 offset:18432\n"
"v_mul_f32 v150, v130, v150\n"
"v_mfma_f32_16x16x32_bf16 v[76:79], v[56:59], v[180:183], v[76:79]\n"
"ds_read_b64_tr_b16 v[66:67], v7 offset:18944\n"
"v_subrev_u32 v38, 768, v54\n"
"v_mfma_f32_16x16x32_bf16 v[80:83], v[56:59], v[184:187], v[80:83]\n"
"buffer_load_dwordx4 v2, s[48:51], s90 offen offset:2048 lds\n"
"v_exp_f32_e64 v130, -v130\n"
"v_mfma_f32_16x16x32_bf16 v[84:87], v[56:59], v[188:191], v[84:87]\n"
"ds_read_b64_tr_b16 v[68:69], v7 offset:19456\n"
"v_mfma_f32_16x16x32_bf16 v[88:91], v[56:59], v[192:195], v[88:91]\n"
"ds_read_b64_tr_b16 v[70:71], v7 offset:19968\n"
"v_max_i32 v38, v38, v19\n"
"s_waitcnt lgkmcnt(4)\n"
"v_mfma_f32_16x16x32_bf16 v[92:95], v[60:63], v[176:179], v[92:95]\n"
"buffer_load_dwordx4 v[38:41], v38, s[64:67], 0 offen sc1\n"
"v_mul_f32 v151, v131, v151\n"
"v_mfma_f32_16x16x32_bf16 v[96:99], v[60:63], v[180:183], v[96:99]\n"
"buffer_load_dwordx4 v2, s[52:55], s90 offen offset:3072 lds\n"
"v_mfma_f32_16x16x32_bf16 v[100:103], v[60:63], v[184:187], v[100:103]\n"
"s_add_u32 m0, s97, 16384\n"
"v_exp_f32_e64 v131, -v131\n"
"v_mfma_f32_16x16x32_bf16 v[104:107], v[60:63], v[188:191], v[104:107]\n"
"v_subrev_u32 v42, 1024, v54\n"
"v_mfma_f32_16x16x32_bf16 v[108:111], v[60:63], v[192:195], v[108:111]\n"
"s_waitcnt lgkmcnt(0) vmcnt(12)\n"
"s_barrier\n"
"v_mfma_f32_16x16x32_bf16 v[72:75], v[64:67], v[196:199], v[72:75]\n"
"ds_read_b64_tr_b16 v[56:57], v7 offset:32768\n"
"s_add_u32 s90, s90, 589824\n"
"v_mfma_f32_16x16x32_bf16 v[76:79], v[64:67], v[200:203], v[76:79]\n"
"ds_read_b64_tr_b16 v[58:59], v7 offset:33280\n"
"v_add_f32 v128, 1.0, v128\n"
"v_mfma_f32_16x16x32_bf16 v[80:83], v[64:67], v[204:207], v[80:83]\n"
"v_max_i32 v42, v42, v19\n"
"v_rcp_f32 v128, v128\n"
"v_mfma_f32_16x16x32_bf16 v[84:87], v[64:67], v[208:211], v[84:87]\n"
"buffer_load_dwordx4 v1, s[48:51], s90 offen lds\n"
"v_mfma_f32_16x16x32_bf16 v[88:91], v[64:67], v[212:215], v[88:91]\n"
"buffer_load_dwordx4 v[42:45], v42, s[64:67], 0 offen sc1\n"
"v_add_f32 v129, 1.0, v129\n"
"v_mfma_f32_16x16x32_bf16 v[92:95], v[68:71], v[196:199], v[92:95]\n"
"ds_read_b64_tr_b16 v[60:61], v7 offset:33792\n"
"v_rcp_f32 v129, v129\n"
"v_mfma_f32_16x16x32_bf16 v[96:99], v[68:71], v[200:203], v[96:99]\n"
"ds_read_b64_tr_b16 v[62:63], v7 offset:34304\n"
"v_subrev_u32 v46, 1280, v54\n"
"v_mfma_f32_16x16x32_bf16 v[100:103], v[68:71], v[204:207], v[100:103]\n"
"v_max_i32 v46, v46, v19\n"
"v_mfma_f32_16x16x32_bf16 v[104:107], v[68:71], v[208:211], v[104:107]\n"
"buffer_load_dwordx4 v1, s[52:55], s90 offen offset:1024 lds\n"
"v_mfma_f32_16x16x32_bf16 v[108:111], v[68:71], v[212:215], v[108:111]\n"
"buffer_load_dwordx4 v[46:49], v46, s[64:67], 0 offen sc1\n"
"v_add_f32 v130, 1.0, v130\n"
"s_waitcnt lgkmcnt(2)\n"
"v_mfma_f32_16x16x32_bf16 v[72:75], v[56:59], v[216:219], v[72:75]\n"
"ds_read_b64_tr_b16 v[64:65], v7 offset:34816\n"
"v_rcp_f32 v130, v130\n"
"v_mfma_f32_16x16x32_bf16 v[76:79], v[56:59], v[220:223], v[76:79]\n"
"ds_read_b64_tr_b16 v[66:67], v7 offset:35328\n"
"v_mfma_f32_16x16x32_bf16 v[80:83], v[56:59], v[224:227], v[80:83]\n"
"buffer_load_dwordx4 v2, s[48:51], s90 offen offset:2048 lds\n"
"v_subrev_u32 v50, 1536, v54\n"
"v_mfma_f32_16x16x32_bf16 v[84:87], v[56:59], v[228:231], v[84:87]\n"
"ds_read_b64_tr_b16 v[68:69], v7 offset:35840\n"
"v_add_f32 v131, 1.0, v131\n"
"v_mfma_f32_16x16x32_bf16 v[88:91], v[56:59], v[232:235], v[88:91]\n"
"ds_read_b64_tr_b16 v[70:71], v7 offset:36352\n"
"v_rcp_f32 v131, v131\n"
"s_waitcnt lgkmcnt(4)\n"
"v_mfma_f32_16x16x32_bf16 v[92:95], v[60:63], v[216:219], v[92:95]\n"
"v_max_i32 v50, v50, v19\n"
"v_mfma_f32_16x16x32_bf16 v[96:99], v[60:63], v[220:223], v[96:99]\n"
"buffer_load_dwordx4 v2, s[52:55], s90 offen offset:3072 lds\n"
"v_mul_f32 v128, v128, v148\n"
"v_mfma_f32_16x16x32_bf16 v[100:103], v[60:63], v[224:227], v[100:103]\n"
"s_add_u32 m0, s97, 32768\n"
"buffer_load_dwordx4 v[50:53], v50, s[64:67], 0 offen sc1\n"
"v_mfma_f32_16x16x32_bf16 v[104:107], v[60:63], v[228:231], v[104:107]\n"
"v_mul_f32 v129, v129, v149\n"
"v_mul_f32 v130, v130, v150\n"
"v_mfma_f32_16x16x32_bf16 v[108:111], v[60:63], v[232:235], v[108:111]\n"
"s_waitcnt lgkmcnt(0) vmcnt(14)\n"
"s_barrier\n"
"v_mfma_f32_16x16x32_bf16 v[72:75], v[64:67], v[236:239], v[72:75]\n"
"ds_read_b64_tr_b16 v[56:57], v7 offset:49152\n"
"s_add_u32 s90, s90, 589824\n"
"v_mfma_f32_16x16x32_bf16 v[76:79], v[64:67], v[240:243], v[76:79]\n"
"ds_read_b64_tr_b16 v[58:59], v7 offset:49664\n"
"v_mfma_f32_16x16x32_bf16 v[80:83], v[64:67], v[244:247], v[80:83]\n"
"v_mul_f32 v131, v131, v151\n"
"v_cvt_pk_bf16_f32 v148, v128, v129\n"
"v_mfma_f32_16x16x32_bf16 v[84:87], v[64:67], v[248:251], v[84:87]\n"
"buffer_load_dwordx4 v1, s[48:51], s90 offen lds\n"
"v_cvt_pk_bf16_f32 v149, v130, v131\n"
"v_mfma_f32_16x16x32_bf16 v[88:91], v[64:67], v[252:255], v[88:91]\n"
"v_mfma_f32_16x16x32_bf16 v[92:95], v[68:71], v[236:239], v[92:95]\n"
"ds_read_b64_tr_b16 v[60:61], v7 offset:50176\n"
"v_mfma_f32_16x16x32_bf16 v[96:99], v[68:71], v[240:243], v[96:99]\n"
"ds_read_b64_tr_b16 v[62:63], v7 offset:50688\n"
"v_mfma_f32_16x16x32_bf16 v[100:103], v[68:71], v[244:247], v[100:103]\n"
"ds_write_b64 v11, v[148:149] offset:24576\n"
"v_mfma_f32_16x16x32_bf16 v[104:107], v[68:71], v[248:251], v[104:107]\n"
"buffer_load_dwordx4 v1, s[52:55], s90 offen offset:1024 lds\n"
"v_mfma_f32_16x16x32_bf16 v[108:111], v[68:71], v[252:255], v[108:111]\n"
"s_waitcnt lgkmcnt(3)\n"
"v_mfma_f32_16x16x32_bf16 v[112:115], v[56:59], a[0:3], 0\n"
"ds_read_b64_tr_b16 v[64:65], v7 offset:51200\n"
"v_mfma_f32_16x16x32_bf16 v[116:119], v[56:59], a[4:7], 0\n"
"ds_read_b64_tr_b16 v[66:67], v7 offset:51712\n"
"v_mfma_f32_16x16x32_bf16 v[120:123], v[56:59], a[8:11], 0\n"
"buffer_load_dwordx4 v2, s[48:51], s90 offen offset:2048 lds\n"
"v_mfma_f32_16x16x32_bf16 v[124:127], v[56:59], a[12:15], 0\n"
"ds_read_b64_tr_b16 v[68:69], v7 offset:52224\n"
"v_mfma_f32_16x16x32_bf16 v[128:131], v[56:59], a[16:19], 0\n"
"ds_read_b64_tr_b16 v[70:71], v7 offset:52736\n"
"s_waitcnt lgkmcnt(5)\n"
"v_mfma_f32_16x16x32_bf16 v[132:135], v[60:63], a[0:3], 0\n"
"v_mul_f32 v92, v72, v92\n"
"v_exp_f32_e64 v72, -v72\n"
"v_mfma_f32_16x16x32_bf16 v[136:139], v[60:63], a[4:7], 0\n"
"buffer_load_dwordx4 v2, s[52:55], s90 offen offset:3072 lds\n"
"v_mfma_f32_16x16x32_bf16 v[140:143], v[60:63], a[8:11], 0\n"
"s_add_u32 m0, s97, 49152\n"
"v_mfma_f32_16x16x32_bf16 v[144:147], v[60:63], a[12:15], 0\n"
"v_mul_f32 v93, v73, v93\n"
"v_exp_f32_e64 v73, -v73\n"
"v_mfma_f32_16x16x32_bf16 v[148:151], v[60:63], a[16:19], 0\n"
"s_waitcnt lgkmcnt(0) vmcnt(11)\n"
"s_barrier\n"
"v_mfma_f32_16x16x32_bf16 v[112:115], v[64:67], a[20:23], v[112:115]\n"
"ds_read_b64_tr_b16 v[56:57], v7 offset:0\n"
"s_add_u32 s90, s90, 589824\n"
"v_bitop3_b32 v27, v27, v29, s88 bitop3:0x7e\n"
"v_mfma_f32_16x16x32_bf16 v[116:119], v[64:67], a[24:27], v[116:119]\n"
"ds_read_b64_tr_b16 v[58:59], v7 offset:512\n"
"v_bitop3_b32 v31, v31, v33, s88 bitop3:0x7e\n"
"v_mfma_f32_16x16x32_bf16 v[120:123], v[64:67], a[28:31], v[120:123]\n"
"v_mul_f32 v94, v74, v94\n"
"v_bitop3_b32 v35, v35, v37, s88 bitop3:0x7e\n"
"v_mfma_f32_16x16x32_bf16 v[124:127], v[64:67], a[32:35], v[124:127]\n"
"buffer_load_dwordx4 v1, s[48:51], s90 offen lds\n"
"v_exp_f32_e64 v74, -v74\n"
"v_mfma_f32_16x16x32_bf16 v[128:131], v[64:67], a[36:39], v[128:131]\n"
"v_bitop3_b32 v39, v39, v41, s88 bitop3:0x7e\n"
"v_mfma_f32_16x16x32_bf16 v[132:135], v[68:71], a[20:23], v[132:135]\n"
"ds_read_b64_tr_b16 v[60:61], v7 offset:1024\n"
"v_mul_f32 v95, v75, v95\n"
"s_waitcnt vmcnt(10)\n"
"v_bitop3_b32 v43, v43, v45, s88 bitop3:0x7e\n"
"v_mfma_f32_16x16x32_bf16 v[136:139], v[68:71], a[24:27], v[136:139]\n"
"ds_read_b64_tr_b16 v[62:63], v7 offset:1536\n"
"v_exp_f32_e64 v75, -v75\n"
"v_mfma_f32_16x16x32_bf16 v[140:143], v[68:71], a[28:31], v[140:143]\n"
"s_waitcnt vmcnt(8)\n"
"v_bitop3_b32 v47, v47, v49, s88 bitop3:0x7e\n"
"v_mfma_f32_16x16x32_bf16 v[144:147], v[68:71], a[32:35], v[144:147]\n"
"buffer_load_dwordx4 v1, s[52:55], s90 offen offset:1024 lds\n"
"v_add_f32 v72, 1.0, v72\n"
"s_waitcnt vmcnt(6)\n"
"v_bitop3_b32 v51, v51, v53, s88 bitop3:0x7e\n"
"v_mfma_f32_16x16x32_bf16 v[148:151], v[68:71], a[36:39], v[148:151]\n"
"v_rcp_f32 v72, v72\n"
"s_waitcnt lgkmcnt(2)\n"
"v_mfma_f32_16x16x32_bf16 v[112:115], v[56:59], a[40:43], v[112:115]\n"
"ds_read_b64_tr_b16 v[64:65], v7 offset:2048\n"
"v_or3_b32 v27, v27, v31, v35\n"
"v_mfma_f32_16x16x32_bf16 v[116:119], v[56:59], a[44:47], v[116:119]\n"
"ds_read_b64_tr_b16 v[66:67], v7 offset:2560\n"
"v_add_f32 v73, 1.0, v73\n"
"v_or3_b32 v39, v39, v43, v47\n"
"v_mfma_f32_16x16x32_bf16 v[120:123], v[56:59], a[48:51], v[120:123]\n"
"buffer_load_dwordx4 v2, s[48:51], s90 offen offset:2048 lds\n"
"v_rcp_f32 v73, v73\n"
"v_mfma_f32_16x16x32_bf16 v[124:127], v[56:59], a[52:55], v[124:127]\n"
"ds_read_b64_tr_b16 v[68:69], v7 offset:3072\n"
"v_or3_b32 v27, v27, v39, v51\n"
"v_mfma_f32_16x16x32_bf16 v[128:131], v[56:59], a[56:59], v[128:131]\n"
"ds_read_b64_tr_b16 v[70:71], v7 offset:3584\n"
"v_cmp_ne_u32 vcc, 0, v27\n"
"s_waitcnt lgkmcnt(4)\n"
"v_mfma_f32_16x16x32_bf16 v[132:135], v[60:63], a[40:43], v[132:135]\n"
"v_add_f32 v74, 1.0, v74\n"
"v_rcp_f32 v74, v74\n"
"v_mfma_f32_16x16x32_bf16 v[136:139], v[60:63], a[44:47], v[136:139]\n"
"buffer_load_dwordx4 v2, s[52:55], s90 offen offset:3072 lds\n"
"s_cbranch_vccnz .Lfz_rg\n"
".Lfz_cg:\n"
"v_mfma_f32_16x16x32_bf16 v[140:143], v[60:63], a[48:51], v[140:143]\n"
"s_add_u32 m0, s97, 0\n"
"v_fma_f32 v46, v13, v50, v46\n"
"v_mfma_f32_16x16x32_bf16 v[144:147], v[60:63], a[52:55], v[144:147]\n"
"v_add_f32 v75, 1.0, v75\n"
"v_rcp_f32 v75, v75\n"
"v_mfma_f32_16x16x32_bf16 v[148:151], v[60:63], a[56:59], v[148:151]\n"
"s_waitcnt lgkmcnt(0)\n"
"s_barrier\n"
"v_mfma_f32_16x16x32_bf16 v[112:115], v[64:67], a[60:63], v[112:115]\n"
"ds_read_b64_tr_b16 v[56:57], v7 offset:16384\n"
"s_add_u32 s90, s90, 589824\n"
"v_fma_f32 v48, v14, v50, v48\n"
"v_mfma_f32_16x16x32_bf16 v[116:119], v[64:67], a[64:67], v[116:119]\n"
"ds_read_b64_tr_b16 v[58:59], v7 offset:16896\n"
"v_fma_f32 v46, -v14, v52, v46\n"
"v_mfma_f32_16x16x32_bf16 v[120:123], v[64:67], a[68:71], v[120:123]\n"
"v_mul_f32 v72, v72, v92\n"
"v_fma_f32 v48, v13, v52, v48\n"
"v_mfma_f32_16x16x32_bf16 v[124:127], v[64:67], a[72:75], v[124:127]\n"
"buffer_load_dwordx4 v1, s[48:51], s90 offen lds\n"
"v_mul_f32 v73, v73, v93\n"
"v_mfma_f32_16x16x32_bf16 v[128:131], v[64:67], a[76:79], v[128:131]\n"
"v_mul_f32 v74, v74, v94\n"
"v_fma_f32 v42, v13, v46, v42\n"
"v_mfma_f32_16x16x32_bf16 v[132:135], v[68:71], a[60:63], v[132:135]\n"
"ds_read_b64_tr_b16 v[60:61], v7 offset:17408\n"
"v_mul_f32 v75, v75, v95\n"
"v_fma_f32 v44, v14, v46, v44\n"
"v_mfma_f32_16x16x32_bf16 v[136:139], v[68:71], a[64:67], v[136:139]\n"
"ds_read_b64_tr_b16 v[62:63], v7 offset:17920\n"
"v_cvt_pk_bf16_f32 v92, v72, v73\n"
"v_mfma_f32_16x16x32_bf16 v[140:143], v[68:71], a[68:71], v[140:143]\n"
"v_cvt_pk_bf16_f32 v93, v74, v75\n"
"v_fma_f32 v42, -v14, v48, v42\n"
"v_mfma_f32_16x16x32_bf16 v[144:147], v[68:71], a[72:75], v[144:147]\n"
"buffer_load_dwordx4 v1, s[52:55], s90 offen offset:1024 lds\n"
"v_mul_f32 v96, v76, v96\n"
"v_fma_f32 v44, v13, v48, v44\n"
"v_mfma_f32_16x16x32_bf16 v[148:151], v[68:71], a[76:79], v[148:151]\n"
"v_exp_f32_e64 v76, -v76\n"
"s_waitcnt lgkmcnt(2)\n"
"v_mfma_f32_16x16x32_bf16 v[112:115], v[56:59], a[80:83], v[112:115]\n"
"ds_read_b64_tr_b16 v[64:65], v7 offset:18432\n"
"v_fma_f32 v38, v13, v42, v38\n"
"v_mfma_f32_16x16x32_bf16 v[116:119], v[56:59], a[84:87], v[116:119]\n"
"ds_read_b64_tr_b16 v[66:67], v7 offset:18944\n"
"v_mul_f32 v97, v77, v97\n"
"v_fma_f32 v40, v14, v42, v40\n"
"v_mfma_f32_16x16x32_bf16 v[120:123], v[56:59], a[88:91], v[120:123]\n"
"buffer_load_dwordx4 v2, s[48:51], s90 offen offset:2048 lds\n"
"v_exp_f32_e64 v77, -v77\n"
"v_mfma_f32_16x16x32_bf16 v[124:127], v[56:59], a[92:95], v[124:127]\n"
"ds_read_b64_tr_b16 v[68:69], v7 offset:19456\n"
"v_fma_f32 v38, -v14, v44, v38\n"
"v_mfma_f32_16x16x32_bf16 v[128:131], v[56:59], a[96:99], v[128:131]\n"
"ds_read_b64_tr_b16 v[70:71], v7 offset:19968\n"
"v_fma_f32 v40, v13, v44, v40\n"
"s_waitcnt lgkmcnt(4)\n"
"v_mfma_f32_16x16x32_bf16 v[132:135], v[60:63], a[80:83], v[132:135]\n"
"v_mul_f32 v98, v78, v98\n"
"v_exp_f32_e64 v78, -v78\n"
"v_mfma_f32_16x16x32_bf16 v[136:139], v[60:63], a[84:87], v[136:139]\n"
"buffer_load_dwordx4 v2, s[52:55], s90 offen offset:3072 lds\n"
"v_fma_f32 v34, v13, v38, v34\n"
"v_mfma_f32_16x16x32_bf16 v[140:143], v[60:63], a[88:91], v[140:143]\n"
"s_add_u32 m0, s97, 16384\n"
"v_fma_f32 v36, v14, v38, v36\n"
"v_mfma_f32_16x16x32_bf16 v[144:147], v[60:63], a[92:95], v[144:147]\n"
"v_mul_f32 v99, v79, v99\n"
"v_exp_f32_e64 v79, -v79\n"
"v_mfma_f32_16x16x32_bf16 v[148:151], v[60:63], a[96:99], v[148:151]\n"
"s_waitcnt lgkmcnt(0) vmcnt(8)\n"
"s_barrier\n"
"v_mfma_f32_16x16x32_bf16 v[112:115], v[64:67], a[100:103], v[112:115]\n"
"ds_read_b64_tr_b16 v[56:57], v7 offset:32768\n"
"s_add_u32 s90, s90, 589824\n"
"v_fma_f32 v34, -v14, v40, v34\n"
"v_mfma_f32_16x16x32_bf16 v[116:119], v[64:67], a[104:107], v[116:119]\n"
"ds_read_b64_tr_b16 v[58:59], v7 offset:33280\n"
"ds_write_b64 v11, v[92:93] offset:30720\n"
"v_mfma_f32_16x16x32_bf16 v[120:123], v[64:67], a[108:111], v[120:123]\n"
"v_add_f32 v76, 1.0, v76\n"
"v_fma_f32 v36, v13, v40, v36\n"
"v_mfma_f32_16x16x32_bf16 v[124:127], v[64:67], a[112:115], v[124:127]\n"
"buffer_load_dwordx4 v1, s[48:51], s90 offen lds\n"
"v_rcp_f32 v76, v76\n"
"v_mfma_f32_16x16x32_bf16 v[128:131], v[64:67], a[116:119], v[128:131]\n"
"v_fma_f32 v30, v13, v34, v30\n"
"v_mfma_f32_16x16x32_bf16 v[132:135], v[68:71], a[100:103], v[132:135]\n"
"ds_read_b64_tr_b16 v[60:61], v7 offset:33792\n"
"v_add_f32 v77, 1.0, v77\n"
"v_fma_f32 v32, v14, v34, v32\n"
"v_mfma_f32_16x16x32_bf16 v[136:139], v[68:71], a[104:107], v[136:139]\n"
"ds_read_b64_tr_b16 v[62:63], v7 offset:34304\n"
"v_rcp_f32 v77, v77\n"
"v_mfma_f32_16x16x32_bf16 v[140:143], v[68:71], a[108:111], v[140:143]\n"
"v_fma_f32 v30, -v14, v36, v30\n"
"v_mfma_f32_16x16x32_bf16 v[144:147], v[68:71], a[112:115], v[144:147]\n"
"buffer_load_dwordx4 v1, s[52:55], s90 offen offset:1024 lds\n"
"v_add_f32 v78, 1.0, v78\n"
"v_fma_f32 v32, v13, v36, v32\n"
"v_mfma_f32_16x16x32_bf16 v[148:151], v[68:71], a[116:119], v[148:151]\n"
"v_rcp_f32 v78, v78\n"
"s_waitcnt lgkmcnt(3)\n"
"v_mfma_f32_16x16x32_bf16 v[112:115], v[56:59], a[120:123], v[112:115]\n"
"ds_read_b64_tr_b16 v[64:65], v7 offset:34816\n"
"v_fma_f32 v26, v13, v30, v26\n"
"v_mfma_f32_16x16x32_bf16 v[116:119], v[56:59], a[124:127], v[116:119]\n"
"ds_read_b64_tr_b16 v[66:67], v7 offset:35328\n"
"v_add_f32 v79, 1.0, v79\n"
"v_fma_f32 v28, v14, v30, v28\n"
"v_mfma_f32_16x16x32_bf16 v[120:123], v[56:59], a[128:131], v[120:123]\n"
"buffer_load_dwordx4 v2, s[48:51], s90 offen offset:2048 lds\n"
"v_rcp_f32 v79, v79\n"
"v_mfma_f32_16x16x32_bf16 v[124:127], v[56:59], a[132:135], v[124:127]\n"
"ds_read_b64_tr_b16 v[68:69], v7 offset:35840\n"
"v_fma_f32 v26, -v14, v32, v26\n"
"v_mfma_f32_16x16x32_bf16 v[128:131], v[56:59], a[136:139], v[128:131]\n"
"ds_read_b64_tr_b16 v[70:71], v7 offset:36352\n"
"v_fma_f32 v28, v13, v32, v28\n"
"s_waitcnt lgkmcnt(4)\n"
"v_mfma_f32_16x16x32_bf16 v[132:135], v[60:63], a[120:123], v[132:135]\n"
"v_mul_f32 v76, v76, v96\n"
"v_mul_f32 v77, v77, v97\n"
"v_mfma_f32_16x16x32_bf16 v[136:139], v[60:63], a[124:127], v[136:139]\n"
"buffer_load_dwordx4 v2, s[52:55], s90 offen offset:3072 lds\n"
"v_lshrrev_b32 v15, 4, v0\n"
"v_mfma_f32_16x16x32_bf16 v[140:143], v[60:63], a[128:131], v[140:143]\n"
"s_add_u32 m0, s97, 32768\n"
"v_mul_f32 v78, v78, v98\n"
"v_mul_u32_u24 v15, 20, v15\n"
"v_mfma_f32_16x16x32_bf16 v[144:147], v[60:63], a[132:135], v[144:147]\n"
"v_mul_f32 v79, v79, v99\n"
"v_cvt_pk_bf16_f32 v96, v76, v77\n"
"v_mfma_f32_16x16x32_bf16 v[148:151], v[60:63], a[136:139], v[148:151]\n"
"v_cvt_f32_u32 v15, v15\n"
"s_waitcnt lgkmcnt(0) vmcnt(8)\n"
"s_barrier\n"
"v_mfma_f32_16x16x32_bf16 v[112:115], v[64:67], a[140:143], v[112:115]\n"
"ds_read_b64_tr_b16 v[56:57], v7 offset:49152\n"
"s_add_u32 s90, s90, 589824\n"
"v_mfma_f32_16x16x32_bf16 v[116:119], v[64:67], a[144:147], v[116:119]\n"
"ds_read_b64_tr_b16 v[58:59], v7 offset:49664\n"
"v_mul_f32 v16, 0x41e00000, v15\n"
"v_mfma_f32_16x16x32_bf16 v[120:123], v[64:67], a[148:151], v[120:123]\n"
"v_cvt_pk_bf16_f32 v97, v78, v79\n"
"ds_write_b64 v11, v[96:97] offset:31744\n"
"v_mul_f32 v100, v80, v100\n"
"v_mfma_f32_16x16x32_bf16 v[124:127], v[64:67], a[152:155], v[124:127]\n"
"buffer_load_dwordx4 v1, s[48:51], s90 offen lds\n"
"v_exp_f32_e64 v80, -v80\n"
"v_mfma_f32_16x16x32_bf16 v[128:131], v[64:67], a[156:159], v[128:131]\n"
"v_mul_f32 v32, v16, v17\n"
"v_mul_f32 v30, v16, v18\n"
"v_mfma_f32_16x16x32_bf16 v[132:135], v[68:71], a[140:143], v[132:135]\n"
"ds_read_b64_tr_b16 v[60:61], v7 offset:50176\n"
"v_mul_f32 v101, v81, v101\n"
"v_mfma_f32_16x16x32_bf16 v[136:139], v[68:71], a[144:147], v[136:139]\n"
"ds_read_b64_tr_b16 v[62:63], v7 offset:50688\n"
"v_exp_f32_e64 v81, -v81\n"
"v_mfma_f32_16x16x32_bf16 v[140:143], v[68:71], a[148:151], v[140:143]\n"
"v_exp_f32 v32, v32\n"
"v_mfma_f32_16x16x32_bf16 v[144:147], v[68:71], a[152:155], v[144:147]\n"
"buffer_load_dwordx4 v1, s[52:55], s90 offen offset:1024 lds\n"
"v_mul_f32 v102, v82, v102\n"
"v_mfma_f32_16x16x32_bf16 v[148:151], v[68:71], a[156:159], v[148:151]\n"
"v_exp_f32_e64 v82, -v82\n"
"v_fract_f32 v30, v30\n"
"s_waitcnt lgkmcnt(3)\n"
"v_mfma_f32_16x16x32_bf16 v[112:115], v[56:59], a[160:163], v[112:115]\n"
"ds_read_b64_tr_b16 v[64:65], v7 offset:51200\n"
"v_sin_f32 v31, v30\n"
"v_mfma_f32_16x16x32_bf16 v[116:119], v[56:59], a[164:167], v[116:119]\n"
"ds_read_b64_tr_b16 v[66:67], v7 offset:51712\n"
"v_mul_f32 v103, v83, v103\n"
"v_mfma_f32_16x16x32_bf16 v[120:123], v[56:59], a[168:171], v[120:123]\n"
"buffer_load_dwordx4 v2, s[48:51], s90 offen offset:2048 lds\n"
"v_exp_f32_e64 v83, -v83\n"
"v_mfma_f32_16x16x32_bf16 v[124:127], v[56:59], a[172:175], v[124:127]\n"
"ds_read_b64_tr_b16 v[68:69], v7 offset:52224\n"
"v_mfma_f32_16x16x32_bf16 v[128:131], v[56:59], a[176:179], v[128:131]\n"
"ds_read_b64_tr_b16 v[70:71], v7 offset:52736\n"
"v_cos_f32 v30, v30\n"
"s_waitcnt lgkmcnt(4)\n"
"v_mfma_f32_16x16x32_bf16 v[132:135], v[60:63], a[160:163], v[132:135]\n"
"v_add_f32 v80, 1.0, v80\n"
"v_rcp_f32 v80, v80\n"
"v_mfma_f32_16x16x32_bf16 v[136:139], v[60:63], a[164:167], v[136:139]\n"
"buffer_load_dwordx4 v2, s[52:55], s90 offen offset:3072 lds\n"
"v_mfma_f32_16x16x32_bf16 v[140:143], v[60:63], a[168:171], v[140:143]\n"
"s_add_u32 m0, s97, 49152\n"
"v_mul_f32 v31, v31, v32\n"
"v_mfma_f32_16x16x32_bf16 v[144:147], v[60:63], a[172:175], v[144:147]\n"
"v_add_f32 v81, 1.0, v81\n"
"v_rcp_f32 v81, v81\n"
"v_mfma_f32_16x16x32_bf16 v[148:151], v[60:63], a[176:179], v[148:151]\n"
"v_mul_f32 v30, v30, v32\n"
"s_waitcnt lgkmcnt(0) vmcnt(8)\n"
"s_barrier\n"
"v_mfma_f32_16x16x32_bf16 v[112:115], v[64:67], a[180:183], v[112:115]\n"
"ds_read_b64_tr_b16 v[56:57], v7 offset:0\n"
"s_add_u32 s90, s90, 589824\n"
"v_mfma_f32_16x16x32_bf16 v[116:119], v[64:67], a[184:187], v[116:119]\n"
"ds_read_b64_tr_b16 v[58:59], v7 offset:512\n"
"v_mul_f32 v33, v15, v17\n"
"v_mfma_f32_16x16x32_bf16 v[120:123], v[64:67], a[188:191], v[120:123]\n"
"v_add_f32 v82, 1.0, v82\n"
"v_mul_f32 v13, v15, v18\n"
"v_mfma_f32_16x16x32_bf16 v[124:127], v[64:67], a[192:195], v[124:127]\n"
"buffer_load_dwordx4 v1, s[48:51], s90 offen lds\n"
"v_exp_f32 v33, v33\n"
"v_mfma_f32_16x16x32_bf16 v[128:131], v[64:67], a[196:199], v[128:131]\n"
"v_rcp_f32 v82, v82\n"
"v_mfma_f32_16x16x32_bf16 v[132:135], v[68:71], a[180:183], v[132:135]\n"
"ds_read_b64_tr_b16 v[60:61], v7 offset:1024\n"
"v_add_f32 v83, 1.0, v83\n"
"v_mfma_f32_16x16x32_bf16 v[136:139], v[68:71], a[184:187], v[136:139]\n"
"ds_read_b64_tr_b16 v[62:63], v7 offset:1536\n"
"v_fract_f32 v13, v13\n"
"v_mfma_f32_16x16x32_bf16 v[140:143], v[68:71], a[188:191], v[140:143]\n"
"v_rcp_f32 v83, v83\n"
"v_sin_f32 v14, v13\n"
"v_mfma_f32_16x16x32_bf16 v[144:147], v[68:71], a[192:195], v[144:147]\n"
"buffer_load_dwordx4 v1, s[52:55], s90 offen offset:1024 lds\n"
"v_mul_f32 v80, v80, v100\n"
"v_mfma_f32_16x16x32_bf16 v[148:151], v[68:71], a[196:199], v[148:151]\n"
"v_mul_f32 v81, v81, v101\n"
"s_waitcnt lgkmcnt(2)\n"
"v_mfma_f32_16x16x32_bf16 v[112:115], v[56:59], a[200:203], v[112:115]\n"
"ds_read_b64_tr_b16 v[64:65], v7 offset:2048\n"
"v_mul_f32 v82, v82, v102\n"
"v_mfma_f32_16x16x32_bf16 v[116:119], v[56:59], a[204:207], v[116:119]\n"
"ds_read_b64_tr_b16 v[66:67], v7 offset:2560\n"
"v_mul_f32 v83, v83, v103\n"
"v_cos_f32 v13, v13\n"
"v_mfma_f32_16x16x32_bf16 v[120:123], v[56:59], a[208:211], v[120:123]\n"
"buffer_load_dwordx4 v2, s[48:51], s90 offen offset:2048 lds\n"
"v_mfma_f32_16x16x32_bf16 v[124:127], v[56:59], a[212:215], v[124:127]\n"
"ds_read_b64_tr_b16 v[68:69], v7 offset:3072\n"
"v_cvt_pk_bf16_f32 v100, v80, v81\n"
"v_mfma_f32_16x16x32_bf16 v[128:131], v[56:59], a[216:219], v[128:131]\n"
"ds_read_b64_tr_b16 v[70:71], v7 offset:3584\n"
"v_cvt_pk_bf16_f32 v101, v82, v83\n"
"v_mul_f32 v14, v14, v33\n"
"s_waitcnt lgkmcnt(4)\n"
"v_mfma_f32_16x16x32_bf16 v[132:135], v[60:63], a[200:203], v[132:135]\n"
"v_mul_f32 v104, v84, v104\n"
"v_mul_f32 v13, v13, v33\n"
"v_mfma_f32_16x16x32_bf16 v[136:139], v[60:63], a[204:207], v[136:139]\n"
"buffer_load_dwordx4 v2, s[52:55], s90 offen offset:3072 lds\n"
"v_exp_f32_e64 v84, -v84\n"
"v_mfma_f32_16x16x32_bf16 v[140:143], v[60:63], a[208:211], v[140:143]\n"
"s_add_u32 m0, s97, 0\n"
"v_mul_f32 v32, v30, v26\n"
"v_mfma_f32_16x16x32_bf16 v[144:147], v[60:63], a[212:215], v[144:147]\n"
"v_mul_f32 v105, v85, v105\n"
"v_mul_f32 v33, v31, v26\n"
"v_mfma_f32_16x16x32_bf16 v[148:151], v[60:63], a[216:219], v[148:151]\n"
"s_waitcnt lgkmcnt(0) vmcnt(8)\n"
"s_barrier\n"
"v_mfma_f32_16x16x32_bf16 v[112:115], v[64:67], a[220:223], v[112:115]\n"
"ds_read_b64_tr_b16 v[56:57], v7 offset:16384\n"
"s_mov_b32 s90, s75\n"
"v_fma_f32 v32, -v31, v28, v32\n"
"v_mfma_f32_16x16x32_bf16 v[116:119], v[64:67], a[224:227], v[116:119]\n"
"ds_read_b64_tr_b16 v[58:59], v7 offset:16896\n"
"ds_write_b64 v11, v[100:101] offset:32768\n"
"v_exp_f32_e64 v85, -v85\n"
"v_mfma_f32_16x16x32_bf16 v[120:123], v[64:67], a[228:231], v[120:123]\n"
"v_mul_f32 v106, v86, v106\n"
"v_fma_f32 v33, v30, v28, v33\n"
"v_mfma_f32_16x16x32_bf16 v[124:127], v[64:67], a[232:235], v[124:127]\n"
"buffer_load_dwordx4 v1, s[48:51], s90 offen lds\n"
"v_mov_b32 v30, v32\n"
"v_mfma_f32_16x16x32_bf16 v[128:131], v[64:67], a[236:239], v[128:131]\n"
"v_exp_f32_e64 v86, -v86\n"
"v_mfma_f32_16x16x32_bf16 v[132:135], v[68:71], a[220:223], v[132:135]\n"
"ds_read_b64_tr_b16 v[60:61], v7 offset:17408\n"
"v_mul_f32 v107, v87, v107\n"
"v_mov_b32 v31, v33\n"
"v_mfma_f32_16x16x32_bf16 v[136:139], v[68:71], a[224:227], v[136:139]\n"
"ds_read_b64_tr_b16 v[62:63], v7 offset:17920\n"
"v_mfma_f32_16x16x32_bf16 v[140:143], v[68:71], a[228:231], v[140:143]\n"
"v_exp_f32_e64 v87, -v87\n"
"v_mfma_f32_16x16x32_bf16 v[144:147], v[68:71], a[232:235], v[144:147]\n"
"buffer_load_dwordx4 v1, s[52:55], s90 offen offset:1024 lds\n"
"v_add_f32 v84, 1.0, v84\n"
"v_permlane16_swap_b32 v32, v30\n"
"v_mfma_f32_16x16x32_bf16 v[148:151], v[68:71], a[236:239], v[148:151]\n"
"v_rcp_f32 v84, v84\n"
"s_waitcnt lgkmcnt(3)\n"
"v_mfma_f32_16x16x32_bf16 v[112:115], v[56:59], a[240:243], v[112:115]\n"
"ds_read_b64_tr_b16 v[64:65], v7 offset:18432\n"
"v_permlane16_swap_b32 v33, v31\n"
"v_mfma_f32_16x16x32_bf16 v[116:119], v[56:59], a[244:247], v[116:119]\n"
"ds_read_b64_tr_b16 v[66:67], v7 offset:18944\n"
"v_add_f32 v85, 1.0, v85\n"
"v_add_f32 v32, v32, v30\n"
"v_mfma_f32_16x16x32_bf16 v[120:123], v[56:59], a[248:251], v[120:123]\n"
"buffer_load_dwordx4 v2, s[48:51], s90 offen offset:2048 lds\n"
"v_add_f32 v33, v33, v31\n"
"v_mfma_f32_16x16x32_bf16 v[124:127], v[56:59], a[252:255], v[124:127]\n"
"ds_read_b64_tr_b16 v[68:69], v7 offset:19456\n"
"v_rcp_f32 v85, v85\n"
"v_mfma_f32_16x16x32_bf16 v[128:131], v[56:59], v[152:155], v[128:131]\n"
"ds_read_b64_tr_b16 v[70:71], v7 offset:19968\n"
"v_mov_b32 v30, v32\n"
"s_waitcnt lgkmcnt(4)\n"
"v_mfma_f32_16x16x32_bf16 v[132:135], v[60:63], a[240:243], v[132:135]\n"
"v_add_f32 v86, 1.0, v86\n"
"v_mov_b32 v31, v33\n"
"v_mfma_f32_16x16x32_bf16 v[136:139], v[60:63], a[244:247], v[136:139]\n"
"buffer_load_dwordx4 v2, s[52:55], s90 offen offset:3072 lds\n"
"v_rcp_f32 v86, v86\n"
"v_mfma_f32_16x16x32_bf16 v[140:143], v[60:63], a[248:251], v[140:143]\n"
"s_add_u32 m0, s97, 16384\n"
"v_mfma_f32_16x16x32_bf16 v[144:147], v[60:63], a[252:255], v[144:147]\n"
"v_add_f32 v87, 1.0, v87\n"
"v_permlane32_swap_b32 v32, v30\n"
"v_mfma_f32_16x16x32_bf16 v[148:151], v[60:63], v[152:155], v[148:151]\n"
"s_waitcnt lgkmcnt(0) vmcnt(8)\n"
"s_barrier\n"
"v_mfma_f32_16x16x32_bf16 v[112:115], v[64:67], v[156:159], v[112:115]\n"
"ds_read_b64_tr_b16 v[56:57], v7 offset:32768\n"
"s_add_u32 s90, s90, 589824\n"
"v_permlane32_swap_b32 v33, v31\n"
"v_mfma_f32_16x16x32_bf16 v[116:119], v[64:67], v[160:163], v[116:119]\n"
"ds_read_b64_tr_b16 v[58:59], v7 offset:33280\n"
"v_rcp_f32 v87, v87\n"
"v_mfma_f32_16x16x32_bf16 v[120:123], v[64:67], v[164:167], v[120:123]\n"
"v_mul_f32 v84, v84, v104\n"
"v_add_f32 v32, v32, v30\n"
"v_mfma_f32_16x16x32_bf16 v[124:127], v[64:67], v[168:171], v[124:127]\n"
"buffer_load_dwordx4 v1, s[48:51], s90 offen lds\n"
"v_add_f32 v33, v33, v31\n"
"v_mfma_f32_16x16x32_bf16 v[128:131], v[64:67], v[172:175], v[128:131]\n"
"v_mul_f32 v85, v85, v105\n"
"v_mul_f32 v86, v86, v106\n"
"v_mfma_f32_16x16x32_bf16 v[132:135], v[68:71], v[156:159], v[132:135]\n"
"ds_read_b64_tr_b16 v[60:61], v7 offset:33792\n"
"v_fma_f32 v24, v13, v32, v24\n"
"v_mfma_f32_16x16x32_bf16 v[136:139], v[68:71], v[160:163], v[136:139]\n"
"ds_read_b64_tr_b16 v[62:63], v7 offset:34304\n"
"v_mul_f32 v87, v87, v107\n"
"v_fma_f32 v25, v14, v32, v25\n"
"v_mfma_f32_16x16x32_bf16 v[140:143], v[68:71], v[164:167], v[140:143]\n"
"v_cvt_pk_bf16_f32 v104, v84, v85\n"
"v_cvt_pk_bf16_f32 v105, v86, v87\n"
"v_mfma_f32_16x16x32_bf16 v[144:147], v[68:71], v[168:171], v[144:147]\n"
"buffer_load_dwordx4 v1, s[52:55], s90 offen offset:1024 lds\n"
"v_fma_f32 v24, -v14, v33, v24\n"
"v_mfma_f32_16x16x32_bf16 v[148:151], v[68:71], v[172:175], v[148:151]\n"
"v_mul_f32 v108, v88, v108\n"
"v_exp_f32_e64 v88, -v88\n"
"s_waitcnt lgkmcnt(2)\n"
"v_mfma_f32_16x16x32_bf16 v[112:115], v[56:59], v[176:179], v[112:115]\n"
"ds_read_b64_tr_b16 v[64:65], v7 offset:34816\n"
"v_fma_f32 v25, v13, v33, v25\n"
"v_mfma_f32_16x16x32_bf16 v[116:119], v[56:59], v[180:183], v[116:119]\n"
"ds_read_b64_tr_b16 v[66:67], v7 offset:35328\n"
"v_mov_b32 v51, v24\n"
"v_mfma_f32_16x16x32_bf16 v[120:123], v[56:59], v[184:187], v[120:123]\n"
"buffer_load_dwordx4 v2, s[48:51], s90 offen offset:2048 lds\n"
"v_mul_f32 v109, v89, v109\n"
"v_mfma_f32_16x16x32_bf16 v[124:127], v[56:59], v[188:191], v[124:127]\n"
"ds_read_b64_tr_b16 v[68:69], v7 offset:35840\n"
"v_exp_f32_e64 v89, -v89\n"
"v_mfma_f32_16x16x32_bf16 v[128:131], v[56:59], v[192:195], v[128:131]\n"
"ds_read_b64_tr_b16 v[70:71], v7 offset:36352\n"
"v_mov_b32 v55, v25\n"
"s_waitcnt lgkmcnt(4)\n"
"v_mfma_f32_16x16x32_bf16 v[132:135], v[60:63], v[176:179], v[132:135]\n"
"v_mul_f32 v110, v90, v110\n"
"v_add_u32 v34, s39, v12\n"
"v_mfma_f32_16x16x32_bf16 v[136:139], v[60:63], v[180:183], v[136:139]\n"
"buffer_load_dwordx4 v2, s[52:55], s90 offen offset:3072 lds\n"
"v_exp_f32_e64 v90, -v90\n"
"v_mfma_f32_16x16x32_bf16 v[140:143], v[60:63], v[184:187], v[140:143]\n"
"s_add_u32 m0, s97, 32768\n"
"v_and_b32 v35, 15, v0\n"
"v_mfma_f32_16x16x32_bf16 v[144:147], v[60:63], v[188:191], v[144:147]\n"
"v_mul_f32 v111, v91, v111\n"
"v_lshrrev_b32 v30, 3, v35\n"
"v_mfma_f32_16x16x32_bf16 v[148:151], v[60:63], v[192:195], v[148:151]\n"
"v_lshlrev_b32 v30, 2, v30\n"
"s_waitcnt lgkmcnt(0) vmcnt(8)\n"
"s_barrier\n"
"v_mfma_f32_16x16x32_bf16 v[112:115], v[64:67], v[196:199], v[112:115]\n"
"ds_read_b64_tr_b16 v[56:57], v7 offset:49152\n"
"s_add_u32 s90, s90, 589824\n"
"v_mfma_f32_16x16x32_bf16 v[116:119], v[64:67], v[200:203], v[116:119]\n"
"ds_read_b64_tr_b16 v[58:59], v7 offset:49664\n"
"ds_write_b64 v11, v[104:105] offset:33792\n"
"v_exp_f32_e64 v91, -v91\n"
"v_mfma_f32_16x16x32_bf16 v[120:123], v[64:67], v[204:207], v[120:123]\n"
"v_xor_b32 v35, v35, v30\n"
"v_lshrrev_b32 v30, 4, v0\n"
"v_mfma_f32_16x16x32_bf16 v[124:127], v[64:67], v[208:211], v[124:127]\n"
"buffer_load_dwordx4 v1, s[48:51], s90 offen lds\n"
"v_add_f32 v88, 1.0, v88\n"
"v_mfma_f32_16x16x32_bf16 v[128:131], v[64:67], v[212:215], v[128:131]\n"
"v_rcp_f32 v88, v88\n"
"v_lshrrev_b32 v31, 2, v35\n"
"v_mfma_f32_16x16x32_bf16 v[132:135], v[68:71], v[196:199], v[132:135]\n"
"ds_read_b64_tr_b16 v[60:61], v7 offset:50176\n"
"v_xor_b32 v30, v30, v31\n"
"v_mfma_f32_16x16x32_bf16 v[136:139], v[68:71], v[200:203], v[136:139]\n"
"ds_read_b64_tr_b16 v[62:63], v7 offset:50688\n"
"v_add_f32 v89, 1.0, v89\n"
"v_mfma_f32_16x16x32_bf16 v[140:143], v[68:71], v[204:207], v[140:143]\n"
"v_rcp_f32 v89, v89\n"
"v_lshlrev_b32 v35, 5, v35\n"
"v_mfma_f32_16x16x32_bf16 v[144:147], v[68:71], v[208:211], v[144:147]\n"
"buffer_load_dwordx4 v1, s[52:55], s90 offen offset:1024 lds\n"
"v_lshl_add_u32 v35, v30, 3, v35\n"
"v_mfma_f32_16x16x32_bf16 v[148:151], v[68:71], v[212:215], v[148:151]\n"
"v_add_f32 v90, 1.0, v90\n"
"v_rcp_f32 v90, v90\n"
"s_waitcnt lgkmcnt(3)\n"
"v_mfma_f32_16x16x32_bf16 v[112:115], v[56:59], v[216:219], v[112:115]\n"
"ds_read_b64_tr_b16 v[64:65], v7 offset:51200\n"
"s_lshl_b32 s98, s95, 9\n"
"v_mfma_f32_16x16x32_bf16 v[116:119], v[56:59], v[220:223], v[116:119]\n"
"ds_read_b64_tr_b16 v[66:67], v7 offset:51712\n"
"s_add_u32 s98, s98, 65536\n"
"v_mfma_f32_16x16x32_bf16 v[120:123], v[56:59], v[224:227], v[120:123]\n"
"buffer_load_dwordx4 v2, s[48:51], s90 offen offset:2048 lds\n"
"v_add_f32 v91, 1.0, v91\n"
"v_mfma_f32_16x16x32_bf16 v[124:127], v[56:59], v[228:231], v[124:127]\n"
"ds_read_b64_tr_b16 v[68:69], v7 offset:52224\n"
"v_rcp_f32 v91, v91\n"
"v_mfma_f32_16x16x32_bf16 v[128:131], v[56:59], v[232:235], v[128:131]\n"
"ds_read_b64_tr_b16 v[70:71], v7 offset:52736\n"
"v_add_u32 v35, s98, v35\n"
"s_waitcnt lgkmcnt(4)\n"
"v_mfma_f32_16x16x32_bf16 v[132:135], v[60:63], v[216:219], v[132:135]\n"
"v_mul_f32 v88, v88, v108\n"
"ds_read_b128 v[40:43], v34 offset:0\n"
"v_mfma_f32_16x16x32_bf16 v[136:139], v[60:63], v[220:223], v[136:139]\n"
"buffer_load_dwordx4 v2, s[52:55], s90 offen offset:3072 lds\n"
"v_mul_f32 v89, v89, v109\n"
"v_mfma_f32_16x16x32_bf16 v[140:143], v[60:63], v[224:227], v[140:143]\n"
"s_add_u32 m0, s97, 49152\n"
"v_mul_f32 v90, v90, v110\n"
"v_mfma_f32_16x16x32_bf16 v[144:147], v[60:63], v[228:231], v[144:147]\n"
"v_mul_f32 v91, v91, v111\n"
"v_mfma_f32_16x16x32_bf16 v[148:151], v[60:63], v[232:235], v[148:151]\n"
"s_waitcnt lgkmcnt(1) vmcnt(8)\n"
"s_barrier\n"
"v_mfma_f32_16x16x32_bf16 v[112:115], v[64:67], v[236:239], v[112:115]\n"
"ds_read_b64_tr_b16 v[56:57], v7 offset:0\n"
"s_add_u32 s90, s90, 589824\n"
"ds_read_b128 v[44:47], v34 offset:5120\n"
"v_mfma_f32_16x16x32_bf16 v[116:119], v[64:67], v[240:243], v[116:119]\n"
"ds_read_b64_tr_b16 v[58:59], v7 offset:512\n"
"v_cvt_pk_bf16_f32 v108, v88, v89\n"
"v_mfma_f32_16x16x32_bf16 v[120:123], v[64:67], v[244:247], v[120:123]\n"
"v_cvt_pk_bf16_f32 v109, v90, v91\n"
"v_mfma_f32_16x16x32_bf16 v[124:127], v[64:67], v[248:251], v[124:127]\n"
"buffer_load_dwordx4 v1, s[48:51], s90 offen lds\n"
"v_mfma_f32_16x16x32_bf16 v[128:131], v[64:67], v[252:255], v[128:131]\n"
"v_mfma_f32_16x16x32_bf16 v[132:135], v[68:71], v[236:239], v[132:135]\n"
"ds_read_b64_tr_b16 v[60:61], v7 offset:1024\n"
"s_waitcnt lgkmcnt(4)\n"
"v_fma_f32 v40, v20, v51, v40\n"
"v_mfma_f32_16x16x32_bf16 v[136:139], v[68:71], v[240:243], v[136:139]\n"
"ds_read_b64_tr_b16 v[62:63], v7 offset:1536\n"
"s_waitcnt lgkmcnt(3)\n"
"v_fma_f32 v44, v21, v51, v44\n"
"v_mfma_f32_16x16x32_bf16 v[140:143], v[68:71], v[244:247], v[140:143]\n"
"ds_write_b64 v11, v[108:109] offset:34816\n"
"v_fma_f32 v40, -v21, v55, v40\n"
"v_mfma_f32_16x16x32_bf16 v[144:147], v[68:71], v[248:251], v[144:147]\n"
"buffer_load_dwordx4 v1, s[52:55], s90 offen offset:1024 lds\n"
"v_fma_f32 v44, v20, v55, v44\n"
"v_mfma_f32_16x16x32_bf16 v[148:151], v[68:71], v[252:255], v[148:151]\n"
"ds_read_b128 v[48:51], v34 offset:1024\n"
"ds_read_b128 v[52:55], v34 offset:6144\n"
"s_waitcnt lgkmcnt(5)\n"
"v_mfma_f32_16x16x32_bf16 v[72:75], v[56:59], a[0:3], 0\n"
"ds_read_b64_tr_b16 v[64:65], v7 offset:2048\n"
"v_mfma_f32_16x16x32_bf16 v[76:79], v[56:59], a[4:7], 0\n"
"ds_read_b64_tr_b16 v[66:67], v7 offset:2560\n"
"v_fma_f32 v41, v20, v40, v41\n"
"v_mfma_f32_16x16x32_bf16 v[80:83], v[56:59], a[8:11], 0\n"
"buffer_load_dwordx4 v2, s[48:51], s90 offen offset:2048 lds\n"
"v_fma_f32 v45, v21, v40, v45\n"
"v_mfma_f32_16x16x32_bf16 v[84:87], v[56:59], a[12:15], 0\n"
"ds_read_b64_tr_b16 v[68:69], v7 offset:3072\n"
"v_fma_f32 v41, -v21, v44, v41\n"
"v_mfma_f32_16x16x32_bf16 v[88:91], v[56:59], a[16:19], 0\n"
"ds_read_b64_tr_b16 v[70:71], v7 offset:3584\n"
"v_fma_f32 v45, v20, v44, v45\n"
"s_waitcnt lgkmcnt(7)\n"
"v_mfma_f32_16x16x32_bf16 v[92:95], v[60:63], a[0:3], 0\n"
"v_mul_f32 v132, v112, v132\n"
"v_exp_f32_e64 v112, -v112\n"
"v_mfma_f32_16x16x32_bf16 v[96:99], v[60:63], a[4:7], 0\n"
"buffer_load_dwordx4 v2, s[52:55], s90 offen offset:3072 lds\n"
"v_fma_f32 v42, v20, v41, v42\n"
"v_mfma_f32_16x16x32_bf16 v[100:103], v[60:63], a[8:11], 0\n"
"s_add_u32 m0, s97, 0\n"
"v_fma_f32 v46, v21, v41, v46\n"
"v_mfma_f32_16x16x32_bf16 v[104:107], v[60:63], a[12:15], 0\n"
"v_mul_f32 v133, v113, v133\n"
"v_exp_f32_e64 v113, -v113\n"
"v_mfma_f32_16x16x32_bf16 v[108:111], v[60:63], a[16:19], 0\n"
"v_fma_f32 v42, -v21, v45, v42\n"
"s_waitcnt lgkmcnt(0) vmcnt(8)\n"
"s_barrier\n"
"v_mfma_f32_16x16x32_bf16 v[72:75], v[64:67], a[20:23], v[72:75]\n"
"ds_read_b64_tr_b16 v[56:57], v7 offset:16384\n"
"s_add_u32 s90, s90, 589824\n"
"v_mfma_f32_16x16x32_bf16 v[76:79], v[64:67], a[24:27], v[76:79]\n"
"ds_read_b64_tr_b16 v[58:59], v7 offset:16896\n"
"v_fma_f32 v46, v20, v45, v46\n"
"v_mfma_f32_16x16x32_bf16 v[80:83], v[64:67], a[28:31], v[80:83]\n"
"v_mul_f32 v134, v114, v134\n"
"v_fma_f32 v43, v20, v42, v43\n"
"v_mfma_f32_16x16x32_bf16 v[84:87], v[64:67], a[32:35], v[84:87]\n"
"buffer_load_dwordx4 v1, s[48:51], s90 offen lds\n"
"v_exp_f32_e64 v114, -v114\n"
"v_mfma_f32_16x16x32_bf16 v[88:91], v[64:67], a[36:39], v[88:91]\n"
"v_fma_f32 v47, v21, v42, v47\n"
"v_fma_f32 v43, -v21, v46, v43\n"
"v_mfma_f32_16x16x32_bf16 v[92:95], v[68:71], a[20:23], v[92:95]\n"
"ds_read_b64_tr_b16 v[60:61], v7 offset:17408\n"
"v_mul_f32 v135, v115, v135\n"
"v_mfma_f32_16x16x32_bf16 v[96:99], v[68:71], a[24:27], v[96:99]\n"
"ds_read_b64_tr_b16 v[62:63], v7 offset:17920\n"
"v_exp_f32_e64 v115, -v115\n"
"v_mfma_f32_16x16x32_bf16 v[100:103], v[68:71], a[28:31], v[100:103]\n"
"v_fma_f32 v47, v20, v46, v47\n"
"v_cvt_pk_bf16_f32 v36, v40, v41\n"
"v_mfma_f32_16x16x32_bf16 v[104:107], v[68:71], a[32:35], v[104:107]\n"
"buffer_load_dwordx4 v1, s[52:55], s90 offen offset:1024 lds\n"
"v_add_f32 v112, 1.0, v112\n"
"v_mfma_f32_16x16x32_bf16 v[108:111], v[68:71], a[36:39], v[108:111]\n"
"v_rcp_f32 v112, v112\n"
"v_cvt_pk_bf16_f32 v37, v42, v43\n"
"s_waitcnt lgkmcnt(2)\n"
"v_mfma_f32_16x16x32_bf16 v[72:75], v[56:59], a[40:43], v[72:75]\n"
"ds_read_b64_tr_b16 v[64:65], v7 offset:18432\n"
"v_cvt_pk_bf16_f32 v38, -v44, -v45\n"
"v_mfma_f32_16x16x32_bf16 v[76:79], v[56:59], a[44:47], v[76:79]\n"
"ds_read_b64_tr_b16 v[66:67], v7 offset:18944\n"
"v_add_f32 v113, 1.0, v113\n"
"v_mfma_f32_16x16x32_bf16 v[80:83], v[56:59], a[48:51], v[80:83]\n"
"buffer_load_dwordx4 v2, s[48:51], s90 offen offset:2048 lds\n"
"v_rcp_f32 v113, v113\n"
"v_mfma_f32_16x16x32_bf16 v[84:87], v[56:59], a[52:55], v[84:87]\n"
"ds_read_b64_tr_b16 v[68:69], v7 offset:19456\n"
"v_cvt_pk_bf16_f32 v39, -v46, -v47\n"
"v_mfma_f32_16x16x32_bf16 v[88:91], v[56:59], a[56:59], v[88:91]\n"
"ds_read_b64_tr_b16 v[70:71], v7 offset:19968\n"
"v_fma_f32 v48, v20, v43, v48\n"
"s_waitcnt lgkmcnt(4)\n"
"v_mfma_f32_16x16x32_bf16 v[92:95], v[60:63], a[40:43], v[92:95]\n"
"v_add_f32 v114, 1.0, v114\n"
"v_rcp_f32 v114, v114\n"
"v_mfma_f32_16x16x32_bf16 v[96:99], v[60:63], a[44:47], v[96:99]\n"
"buffer_load_dwordx4 v2, s[52:55], s90 offen offset:3072 lds\n"
"v_fma_f32 v52, v21, v43, v52\n"
"v_mfma_f32_16x16x32_bf16 v[100:103], v[60:63], a[48:51], v[100:103]\n"
"s_add_u32 m0, s97, 16384\n"
"ds_write_b64 v35, v[36:37] offset:0\n"
"v_fma_f32 v48, -v21, v47, v48\n"
"v_mfma_f32_16x16x32_bf16 v[104:107], v[60:63], a[52:55], v[104:107]\n"
"v_add_f32 v115, 1.0, v115\n"
"v_rcp_f32 v115, v115\n"
"v_mfma_f32_16x16x32_bf16 v[108:111], v[60:63], a[56:59], v[108:111]\n"
"v_fma_f32 v52, v20, v47, v52\n"
"s_waitcnt lgkmcnt(1) vmcnt(8)\n"
"s_barrier\n"
"v_mfma_f32_16x16x32_bf16 v[72:75], v[64:67], a[60:63], v[72:75]\n"
"ds_read_b64_tr_b16 v[56:57], v7 offset:32768\n"
"s_add_u32 s90, s90, 589824\n"
"v_mfma_f32_16x16x32_bf16 v[76:79], v[64:67], a[64:67], v[76:79]\n"
"ds_read_b64_tr_b16 v[58:59], v7 offset:33280\n"
"ds_write_b64 v35, v[38:39] offset:10240\n"
"ds_read_b128 v[40:43], v34 offset:2048\n"
"v_mfma_f32_16x16x32_bf16 v[80:83], v[64:67], a[68:71], v[80:83]\n"
"v_mul_f32 v112, v112, v132\n"
"ds_read_b128 v[44:47], v34 offset:7168\n"
"v_mfma_f32_16x16x32_bf16 v[84:87], v[64:67], a[72:75], v[84:87]\n"
"buffer_load_dwordx4 v1, s[48:51], s90 offen lds\n"
"v_mul_f32 v113, v113, v133\n"
"v_fma_f32 v49, v20, v48, v49\n"
"v_mfma_f32_16x16x32_bf16 v[88:91], v[64:67], a[76:79], v[88:91]\n"
"v_mul_f32 v114, v114, v134\n"
"v_fma_f32 v53, v21, v48, v53\n"
"v_mfma_f32_16x16x32_bf16 v[92:95], v[68:71], a[60:63], v[92:95]\n"
"ds_read_b64_tr_b16 v[60:61], v7 offset:33792\n"
"v_mul_f32 v115, v115, v135\n"
"v_mfma_f32_16x16x32_bf16 v[96:99], v[68:71], a[64:67], v[96:99]\n"
"ds_read_b64_tr_b16 v[62:63], v7 offset:34304\n"
"v_cvt_pk_bf16_f32 v132, v112, v113\n"
"v_fma_f32 v49, -v21, v52, v49\n"
"v_mfma_f32_16x16x32_bf16 v[100:103], v[68:71], a[68:71], v[100:103]\n"
"v_cvt_pk_bf16_f32 v133, v114, v115\n"
"v_fma_f32 v53, v20, v52, v53\n"
"v_mfma_f32_16x16x32_bf16 v[104:107], v[68:71], a[72:75], v[104:107]\n"
"buffer_load_dwordx4 v1, s[52:55], s90 offen offset:1024 lds\n"
"v_mul_f32 v136, v116, v136\n"
"v_mfma_f32_16x16x32_bf16 v[108:111], v[68:71], a[76:79], v[108:111]\n"
"v_exp_f32_e64 v116, -v116\n"
"v_fma_f32 v50, v20, v49, v50\n"
"s_waitcnt lgkmcnt(5)\n"
"v_mfma_f32_16x16x32_bf16 v[72:75], v[56:59], a[80:83], v[72:75]\n"
"ds_read_b64_tr_b16 v[64:65], v7 offset:34816\n"
"v_fma_f32 v54, v21, v49, v54\n"
"v_mfma_f32_16x16x32_bf16 v[76:79], v[56:59], a[84:87], v[76:79]\n"
"ds_read_b64_tr_b16 v[66:67], v7 offset:35328\n"
"v_mul_f32 v137, v117, v137\n"
"v_mfma_f32_16x16x32_bf16 v[80:83], v[56:59], a[88:91], v[80:83]\n"
"buffer_load_dwordx4 v2, s[48:51], s90 offen offset:2048 lds\n"
"v_exp_f32_e64 v117, -v117\n"
"v_mfma_f32_16x16x32_bf16 v[84:87], v[56:59], a[92:95], v[84:87]\n"
"ds_read_b64_tr_b16 v[68:69], v7 offset:35840\n"
"v_fma_f32 v50, -v21, v53, v50\n"
"v_mfma_f32_16x16x32_bf16 v[88:91], v[56:59], a[96:99], v[88:91]\n"
"ds_read_b64_tr_b16 v[70:71], v7 offset:36352\n"
"v_fma_f32 v54, v20, v53, v54\n"
"s_waitcnt lgkmcnt(4)\n"
"v_mfma_f32_16x16x32_bf16 v[92:95], v[60:63], a[80:83], v[92:95]\n"
"v_mul_f32 v138, v118, v138\n"
"v_exp_f32_e64 v118, -v118\n"
"v_mfma_f32_16x16x32_bf16 v[96:99], v[60:63], a[84:87], v[96:99]\n"
"buffer_load_dwordx4 v2, s[52:55], s90 offen offset:3072 lds\n"
"v_fma_f32 v51, v20, v50, v51\n"
"v_mfma_f32_16x16x32_bf16 v[100:103], v[60:63], a[88:91], v[100:103]\n"
"s_add_u32 m0, s97, 32768\n"
"ds_write_b64 v11, v[132:133] offset:40960\n"
"v_mfma_f32_16x16x32_bf16 v[104:107], v[60:63], a[92:95], v[104:107]\n"
"v_mul_f32 v139, v119, v139\n"
"v_exp_f32_e64 v119, -v119\n"
"v_mfma_f32_16x16x32_bf16 v[108:111], v[60:63], a[96:99], v[108:111]\n"
"v_fma_f32 v55, v21, v50, v55\n"
"s_waitcnt lgkmcnt(1) vmcnt(8)\n"
"s_barrier\n"
"v_mfma_f32_16x16x32_bf16 v[72:75], v[64:67], a[100:103], v[72:75]\n"
"ds_read_b64_tr_b16 v[56:57], v7 offset:49152\n"
"s_add_u32 s90, s90, 589824\n"
"v_mfma_f32_16x16x32_bf16 v[76:79], v[64:67], a[104:107], v[76:79]\n"
"ds_read_b64_tr_b16 v[58:59], v7 offset:49664\n"
"v_fma_f32 v51, -v21, v54, v51\n"
"v_mfma_f32_16x16x32_bf16 v[80:83], v[64:67], a[108:111], v[80:83]\n"
"v_add_f32 v116, 1.0, v116\n"
"v_fma_f32 v55, v20, v54, v55\n"
"v_mfma_f32_16x16x32_bf16 v[84:87], v[64:67], a[112:115], v[84:87]\n"
"buffer_load_dwordx4 v1, s[48:51], s90 offen lds\n"
"v_rcp_f32 v116, v116\n"
"v_mfma_f32_16x16x32_bf16 v[88:91], v[64:67], a[116:119], v[88:91]\n"
"v_cvt_pk_bf16_f32 v26, v48, v49\n"
"v_cvt_pk_bf16_f32 v27, v50, v51\n"
"v_mfma_f32_16x16x32_bf16 v[92:95], v[68:71], a[100:103], v[92:95]\n"
"ds_read_b64_tr_b16 v[60:61], v7 offset:50176\n"
"v_add_f32 v117, 1.0, v117\n"
"v_mfma_f32_16x16x32_bf16 v[96:99], v[68:71], a[104:107], v[96:99]\n"
"ds_read_b64_tr_b16 v[62:63], v7 offset:50688\n"
"v_rcp_f32 v117, v117\n"
"v_mfma_f32_16x16x32_bf16 v[100:103], v[68:71], a[108:111], v[100:103]\n"
"v_cvt_pk_bf16_f32 v28, -v52, -v53\n"
"v_cvt_pk_bf16_f32 v29, -v54, -v55\n"
"v_mfma_f32_16x16x32_bf16 v[104:107], v[68:71], a[112:115], v[104:107]\n"
"buffer_load_dwordx4 v1, s[52:55], s90 offen offset:1024 lds\n"
"v_add_f32 v118, 1.0, v118\n"
"v_mfma_f32_16x16x32_bf16 v[108:111], v[68:71], a[116:119], v[108:111]\n"
"v_rcp_f32 v118, v118\n"
"v_fma_f32 v40, v20, v51, v40\n"
"s_waitcnt lgkmcnt(2)\n"
"v_mfma_f32_16x16x32_bf16 v[72:75], v[56:59], a[120:123], v[72:75]\n"
"ds_read_b64_tr_b16 v[64:65], v7 offset:51200\n"
"v_fma_f32 v44, v21, v51, v44\n"
"v_mfma_f32_16x16x32_bf16 v[76:79], v[56:59], a[124:127], v[76:79]\n"
"ds_read_b64_tr_b16 v[66:67], v7 offset:51712\n"
"v_add_f32 v119, 1.0, v119\n"
"v_mfma_f32_16x16x32_bf16 v[80:83], v[56:59], a[128:131], v[80:83]\n"
"buffer_load_dwordx4 v2, s[48:51], s90 offen offset:2048 lds\n"
"v_rcp_f32 v119, v119\n"
"v_mfma_f32_16x16x32_bf16 v[84:87], v[56:59], a[132:135], v[84:87]\n"
"ds_read_b64_tr_b16 v[68:69], v7 offset:52224\n"
"v_fma_f32 v40, -v21, v55, v40\n"
"v_mfma_f32_16x16x32_bf16 v[88:91], v[56:59], a[136:139], v[88:91]\n"
"ds_read_b64_tr_b16 v[70:71], v7 offset:52736\n"
"v_fma_f32 v44, v20, v55, v44\n"
"s_waitcnt lgkmcnt(4)\n"
"v_mfma_f32_16x16x32_bf16 v[92:95], v[60:63], a[120:123], v[92:95]\n"
"v_mul_f32 v116, v116, v136\n"
"v_mul_f32 v117, v117, v137\n"
"ds_read_b128 v[48:51], v34 offset:3072\n"
"v_mfma_f32_16x16x32_bf16 v[96:99], v[60:63], a[124:127], v[96:99]\n"
"buffer_load_dwordx4 v2, s[52:55], s90 offen offset:3072 lds\n"
"v_mfma_f32_16x16x32_bf16 v[100:103], v[60:63], a[128:131], v[100:103]\n"
"s_add_u32 m0, s97, 49152\n"
"v_mul_f32 v118, v118, v138\n"
"ds_write_b64 v35, v[26:27] offset:2048\n"
"v_mfma_f32_16x16x32_bf16 v[104:107], v[60:63], a[132:135], v[104:107]\n"
"v_mul_f32 v119, v119, v139\n"
"v_cvt_pk_bf16_f32 v136, v116, v117\n"
"v_mfma_f32_16x16x32_bf16 v[108:111], v[60:63], a[136:139], v[108:111]\n"
"s_waitcnt lgkmcnt(2) vmcnt(8)\n"
"s_barrier\n"
"v_mfma_f32_16x16x32_bf16 v[72:75], v[64:67], a[140:143], v[72:75]\n"
"ds_read_b64_tr_b16 v[56:57], v7 offset:0\n"
"s_add_u32 s90, s90, 589824\n"
"ds_read_b128 v[52:55], v34 offset:8192\n"
"v_fma_f32 v41, v20, v40, v41\n"
"v_fma_f32 v45, v21, v40, v45\n"
"v_mfma_f32_16x16x32_bf16 v[76:79], v[64:67], a[144:147], v[76:79]\n"
"ds_read_b64_tr_b16 v[58:59], v7 offset:512\n"
"v_fma_f32 v41, -v21, v44, v41\n"
"v_mfma_f32_16x16x32_bf16 v[80:83], v[64:67], a[148:151], v[80:83]\n"
"v_cvt_pk_bf16_f32 v137, v118, v119\n"
"ds_write_b64 v11, v[136:137] offset:41984\n"
"v_mul_f32 v140, v120, v140\n"
"v_mfma_f32_16x16x32_bf16 v[84:87], v[64:67], a[152:155], v[84:87]\n"
"buffer_load_dwordx4 v1, s[48:51], s90 offen lds\n"
"v_exp_f32_e64 v120, -v120\n"
"v_mfma_f32_16x16x32_bf16 v[88:91], v[64:67], a[156:159], v[88:91]\n"
"v_fma_f32 v45, v20, v44, v45\n"
"v_mfma_f32_16x16x32_bf16 v[92:95], v[68:71], a[140:143], v[92:95]\n"
"ds_read_b64_tr_b16 v[60:61], v7 offset:1024\n"
"v_mul_f32 v141, v121, v141\n"
"v_fma_f32 v42, v20, v41, v42\n"
"v_mfma_f32_16x16x32_bf16 v[96:99], v[68:71], a[144:147], v[96:99]\n"
"ds_read_b64_tr_b16 v[62:63], v7 offset:1536\n"
"v_exp_f32_e64 v121, -v121\n"
"v_mfma_f32_16x16x32_bf16 v[100:103], v[68:71], a[148:151], v[100:103]\n"
"v_fma_f32 v46, v21, v41, v46\n"
"v_mfma_f32_16x16x32_bf16 v[104:107], v[68:71], a[152:155], v[104:107]\n"
"buffer_load_dwordx4 v1, s[52:55], s90 offen offset:1024 lds\n"
"v_mul_f32 v142, v122, v142\n"
"v_fma_f32 v42, -v21, v45, v42\n"
"v_mfma_f32_16x16x32_bf16 v[108:111], v[68:71], a[156:159], v[108:111]\n"
"v_exp_f32_e64 v122, -v122\n"
"s_waitcnt lgkmcnt(3)\n"
"v_mfma_f32_16x16x32_bf16 v[72:75], v[56:59], a[160:163], v[72:75]\n"
"ds_read_b64_tr_b16 v[64:65], v7 offset:2048\n"
"v_fma_f32 v46, v20, v45, v46\n"
"v_mfma_f32_16x16x32_bf16 v[76:79], v[56:59], a[164:167], v[76:79]\n"
"ds_read_b64_tr_b16 v[66:67], v7 offset:2560\n"
"v_mul_f32 v143, v123, v143\n"
"v_fma_f32 v43, v20, v42, v43\n"
"v_mfma_f32_16x16x32_bf16 v[80:83], v[56:59], a[168:171], v[80:83]\n"
"buffer_load_dwordx4 v2, s[48:51], s90 offen offset:2048 lds\n"
"v_exp_f32_e64 v123, -v123\n"
"v_mfma_f32_16x16x32_bf16 v[84:87], v[56:59], a[172:175], v[84:87]\n"
"ds_read_b64_tr_b16 v[68:69], v7 offset:3072\n"
"v_fma_f32 v47, v21, v42, v47\n"
"v_mfma_f32_16x16x32_bf16 v[88:91], v[56:59], a[176:179], v[88:91]\n"
"ds_read_b64_tr_b16 v[70:71], v7 offset:3584\n"
"v_fma_f32 v43, -v21, v46, v43\n"
"s_waitcnt lgkmcnt(4)\n"
"v_mfma_f32_16x16x32_bf16 v[92:95], v[60:63], a[160:163], v[92:95]\n"
"v_add_f32 v120, 1.0, v120\n"
"v_rcp_f32 v120, v120\n"
"v_mfma_f32_16x16x32_bf16 v[96:99], v[60:63], a[164:167], v[96:99]\n"
"buffer_load_dwordx4 v2, s[52:55], s90 offen offset:3072 lds\n"
"v_fma_f32 v47, v20, v46, v47\n"
"v_mfma_f32_16x16x32_bf16 v[100:103], v[60:63], a[168:171], v[100:103]\n"
"s_add_u32 m0, s97, 0\n"
"ds_write_b64 v35, v[28:29] offset:12288\n"
"v_mfma_f32_16x16x32_bf16 v[104:107], v[60:63], a[172:175], v[104:107]\n"
"v_add_f32 v121, 1.0, v121\n"
"v_rcp_f32 v121, v121\n"
"v_mfma_f32_16x16x32_bf16 v[108:111], v[60:63], a[176:179], v[108:111]\n"
"v_cvt_pk_bf16_f32 v36, v40, v41\n"
"s_waitcnt lgkmcnt(1) vmcnt(8)\n"
"s_barrier\n"
"v_mfma_f32_16x16x32_bf16 v[72:75], v[64:67], a[180:183], v[72:75]\n"
"ds_read_b64_tr_b16 v[56:57], v7 offset:16384\n"
"s_add_u32 s90, s90, 589824\n"
"v_mfma_f32_16x16x32_bf16 v[76:79], v[64:67], a[184:187], v[76:79]\n"
"ds_read_b64_tr_b16 v[58:59], v7 offset:16896\n"
"v_cvt_pk_bf16_f32 v37, v42, v43\n"
"v_mfma_f32_16x16x32_bf16 v[80:83], v[64:67], a[188:191], v[80:83]\n"
"v_add_f32 v122, 1.0, v122\n"
"v_cvt_pk_bf16_f32 v38, -v44, -v45\n"
"v_mfma_f32_16x16x32_bf16 v[84:87], v[64:67], a[192:195], v[84:87]\n"
"buffer_load_dwordx4 v1, s[48:51], s90 offen lds\n"
"v_cvt_pk_bf16_f32 v39, -v46, -v47\n"
"v_mfma_f32_16x16x32_bf16 v[88:91], v[64:67], a[196:199], v[88:91]\n"
"v_rcp_f32 v122, v122\n"
"v_fma_f32 v48, v20, v43, v48\n"
"v_mfma_f32_16x16x32_bf16 v[92:95], v[68:71], a[180:183], v[92:95]\n"
"ds_read_b64_tr_b16 v[60:61], v7 offset:17408\n"
"v_add_f32 v123, 1.0, v123\n"
"v_mfma_f32_16x16x32_bf16 v[96:99], v[68:71], a[184:187], v[96:99]\n"
"ds_read_b64_tr_b16 v[62:63], v7 offset:17920\n"
"v_fma_f32 v52, v21, v43, v52\n"
"v_mfma_f32_16x16x32_bf16 v[100:103], v[68:71], a[188:191], v[100:103]\n"
"v_rcp_f32 v123, v123\n"
"v_fma_f32 v48, -v21, v47, v48\n"
"v_mfma_f32_16x16x32_bf16 v[104:107], v[68:71], a[192:195], v[104:107]\n"
"buffer_load_dwordx4 v1, s[52:55], s90 offen offset:1024 lds\n"
"v_mul_f32 v120, v120, v140\n"
"v_mfma_f32_16x16x32_bf16 v[108:111], v[68:71], a[196:199], v[108:111]\n"
"v_mul_f32 v121, v121, v141\n"
"v_fma_f32 v52, v20, v47, v52\n"
"s_waitcnt lgkmcnt(2)\n"
"v_mfma_f32_16x16x32_bf16 v[72:75], v[56:59], a[200:203], v[72:75]\n"
"ds_read_b64_tr_b16 v[64:65], v7 offset:18432\n"
"v_mul_f32 v122, v122, v142\n"
"ds_read_b128 v[40:43], v34 offset:4096\n"
"v_mfma_f32_16x16x32_bf16 v[76:79], v[56:59], a[204:207], v[76:79]\n"
"ds_read_b64_tr_b16 v[66:67], v7 offset:18944\n"
"v_mul_f32 v123, v123, v143\n"
"v_mfma_f32_16x16x32_bf16 v[80:83], v[56:59], a[208:211], v[80:83]\n"
"buffer_load_dwordx4 v2, s[48:51], s90 offen offset:2048 lds\n"
"ds_read_b128 v[44:47], v34 offset:9216\n"
"v_mfma_f32_16x16x32_bf16 v[84:87], v[56:59], a[212:215], v[84:87]\n"
"ds_read_b64_tr_b16 v[68:69], v7 offset:19456\n"
"v_cvt_pk_bf16_f32 v140, v120, v121\n"
"v_fma_f32 v49, v20, v48, v49\n"
"v_mfma_f32_16x16x32_bf16 v[88:91], v[56:59], a[216:219], v[88:91]\n"
"ds_read_b64_tr_b16 v[70:71], v7 offset:19968\n"
"v_cvt_pk_bf16_f32 v141, v122, v123\n"
"s_waitcnt lgkmcnt(6)\n"
"v_mfma_f32_16x16x32_bf16 v[92:95], v[60:63], a[200:203], v[92:95]\n"
"v_mul_f32 v144, v124, v144\n"
"v_fma_f32 v53, v21, v48, v53\n"
"v_mfma_f32_16x16x32_bf16 v[96:99], v[60:63], a[204:207], v[96:99]\n"
"buffer_load_dwordx4 v2, s[52:55], s90 offen offset:3072 lds\n"
"v_exp_f32_e64 v124, -v124\n"
"v_mfma_f32_16x16x32_bf16 v[100:103], v[60:63], a[208:211], v[100:103]\n"
"s_add_u32 m0, s97, 16384\n"
"ds_write_b64 v11, v[140:141] offset:43008\n"
"v_fma_f32 v49, -v21, v52, v49\n"
"v_mfma_f32_16x16x32_bf16 v[104:107], v[60:63], a[212:215], v[104:107]\n"
"v_mul_f32 v145, v125, v145\n"
"v_fma_f32 v53, v20, v52, v53\n"
"v_mfma_f32_16x16x32_bf16 v[108:111], v[60:63], a[216:219], v[108:111]\n"
"s_waitcnt lgkmcnt(1) vmcnt(8)\n"
"s_barrier\n"
"v_mfma_f32_16x16x32_bf16 v[72:75], v[64:67], a[220:223], v[72:75]\n"
"ds_read_b64_tr_b16 v[56:57], v7 offset:32768\n"
"s_mov_b32 s90, s76\n"
"v_fma_f32 v50, v20, v49, v50\n"
"v_mfma_f32_16x16x32_bf16 v[76:79], v[64:67], a[224:227], v[76:79]\n"
"ds_read_b64_tr_b16 v[58:59], v7 offset:33280\n"
"v_exp_f32_e64 v125, -v125\n"
"v_mfma_f32_16x16x32_bf16 v[80:83], v[64:67], a[228:231], v[80:83]\n"
"v_mul_f32 v146, v126, v146\n"
"ds_write_b64 v35, v[36:37] offset:4096\n"
"v_fma_f32 v54, v21, v49, v54\n"
"v_mfma_f32_16x16x32_bf16 v[84:87], v[64:67], a[232:235], v[84:87]\n"
"buffer_load_dwordx4 v1, s[48:51], s90 offen lds\n"
"v_fma_f32 v50, -v21, v53, v50\n"
"v_mfma_f32_16x16x32_bf16 v[88:91], v[64:67], a[236:239], v[88:91]\n"
"v_exp_f32_e64 v126, -v126\n"
"v_mfma_f32_16x16x32_bf16 v[92:95], v[68:71], a[220:223], v[92:95]\n"
"ds_read_b64_tr_b16 v[60:61], v7 offset:33792\n"
"v_mul_f32 v147, v127, v147\n"
"v_fma_f32 v54, v20, v53, v54\n"
"v_mfma_f32_16x16x32_bf16 v[96:99], v[68:71], a[224:227], v[96:99]\n"
"ds_read_b64_tr_b16 v[62:63], v7 offset:34304\n"
"v_fma_f32 v51, v20, v50, v51\n"
"v_mfma_f32_16x16x32_bf16 v[100:103], v[68:71], a[228:231], v[100:103]\n"
"v_exp_f32_e64 v127, -v127\n"
"v_mfma_f32_16x16x32_bf16 v[104:107], v[68:71], a[232:235], v[104:107]\n"
"buffer_load_dwordx4 v1, s[52:55], s90 offen offset:1024 lds\n"
"v_add_f32 v124, 1.0, v124\n"
"v_fma_f32 v55, v21, v50, v55\n"
"v_mfma_f32_16x16x32_bf16 v[108:111], v[68:71], a[236:239], v[108:111]\n"
"v_rcp_f32 v124, v124\n"
"s_waitcnt lgkmcnt(3)\n"
"v_mfma_f32_16x16x32_bf16 v[72:75], v[56:59], a[240:243], v[72:75]\n"
"ds_read_b64_tr_b16 v[64:65], v7 offset:34816\n"
"v_fma_f32 v51, -v21, v54, v51\n"
"v_mfma_f32_16x16x32_bf16 v[76:79], v[56:59], a[244:247], v[76:79]\n"
"ds_read_b64_tr_b16 v[66:67], v7 offset:35328\n"
"v_add_f32 v125, 1.0, v125\n"
"v_fma_f32 v55, v20, v54, v55\n"
"v_mfma_f32_16x16x32_bf16 v[80:83], v[56:59], a[248:251], v[80:83]\n"
"buffer_load_dwordx4 v2, s[48:51], s90 offen offset:2048 lds\n"
"v_cvt_pk_bf16_f32 v26, v48, v49\n"
"v_mfma_f32_16x16x32_bf16 v[84:87], v[56:59], a[252:255], v[84:87]\n"
"ds_read_b64_tr_b16 v[68:69], v7 offset:35840\n"
"v_rcp_f32 v125, v125\n"
"v_mfma_f32_16x16x32_bf16 v[88:91], v[56:59], v[152:155], v[88:91]\n"
"ds_read_b64_tr_b16 v[70:71], v7 offset:36352\n"
"v_cvt_pk_bf16_f32 v27, v50, v51\n"
"s_waitcnt lgkmcnt(4)\n"
"v_mfma_f32_16x16x32_bf16 v[92:95], v[60:63], a[240:243], v[92:95]\n"
"v_add_f32 v126, 1.0, v126\n"
"v_cvt_pk_bf16_f32 v28, -v52, -v53\n"
"v_mfma_f32_16x16x32_bf16 v[96:99], v[60:63], a[244:247], v[96:99]\n"
"buffer_load_dwordx4 v2, s[52:55], s90 offen offset:3072 lds\n"
"v_rcp_f32 v126, v126\n"
"v_mfma_f32_16x16x32_bf16 v[100:103], v[60:63], a[248:251], v[100:103]\n"
"s_add_u32 m0, s97, 32768\n"
"ds_write_b64 v35, v[38:39] offset:14336\n"
"v_cvt_pk_bf16_f32 v29, -v54, -v55\n"
"v_mfma_f32_16x16x32_bf16 v[104:107], v[60:63], a[252:255], v[104:107]\n"
"v_add_f32 v127, 1.0, v127\n"
"v_fma_f32 v40, v20, v51, v40\n"
"v_mfma_f32_16x16x32_bf16 v[108:111], v[60:63], v[152:155], v[108:111]\n"
"s_waitcnt lgkmcnt(1) vmcnt(8)\n"
"s_barrier\n"
"v_mfma_f32_16x16x32_bf16 v[72:75], v[64:67], v[156:159], v[72:75]\n"
"ds_read_b64_tr_b16 v[56:57], v7 offset:49152\n"
"s_add_u32 s90, s90, 589824\n"
"v_fma_f32 v44, v21, v51, v44\n"
"v_mfma_f32_16x16x32_bf16 v[76:79], v[64:67], v[160:163], v[76:79]\n"
"ds_read_b64_tr_b16 v[58:59], v7 offset:49664\n"
"v_rcp_f32 v127, v127\n"
"v_mfma_f32_16x16x32_bf16 v[80:83], v[64:67], v[164:167], v[80:83]\n"
"v_mul_f32 v124, v124, v144\n"
"ds_write_b64 v35, v[26:27] offset:6144\n"
"v_mfma_f32_16x16x32_bf16 v[84:87], v[64:67], v[168:171], v[84:87]\n"
"buffer_load_dwordx4 v1, s[48:51], s90 offen lds\n"
"v_fma_f32 v40, -v21, v55, v40\n"
"v_mfma_f32_16x16x32_bf16 v[88:91], v[64:67], v[172:175], v[88:91]\n"
"v_mul_f32 v125, v125, v145\n"
"v_mul_f32 v126, v126, v146\n"
"v_fma_f32 v44, v20, v55, v44\n"
"v_mfma_f32_16x16x32_bf16 v[92:95], v[68:71], v[156:159], v[92:95]\n"
"ds_read_b64_tr_b16 v[60:61], v7 offset:50176\n"
"v_fma_f32 v41, v20, v40, v41\n"
"v_mfma_f32_16x16x32_bf16 v[96:99], v[68:71], v[160:163], v[96:99]\n"
"ds_read_b64_tr_b16 v[62:63], v7 offset:50688\n"
"v_mul_f32 v127, v127, v147\n"
"v_mfma_f32_16x16x32_bf16 v[100:103], v[68:71], v[164:167], v[100:103]\n"
"v_cvt_pk_bf16_f32 v144, v124, v125\n"
"v_cvt_pk_bf16_f32 v145, v126, v127\n"
"v_fma_f32 v45, v21, v40, v45\n"
"v_mfma_f32_16x16x32_bf16 v[104:107], v[68:71], v[168:171], v[104:107]\n"
"buffer_load_dwordx4 v1, s[52:55], s90 offen offset:1024 lds\n"
"v_fma_f32 v41, -v21, v44, v41\n"
"v_mfma_f32_16x16x32_bf16 v[108:111], v[68:71], v[172:175], v[108:111]\n"
"v_mul_f32 v148, v128, v148\n"
"v_exp_f32_e64 v128, -v128\n"
"s_waitcnt lgkmcnt(3)\n"
"v_mfma_f32_16x16x32_bf16 v[72:75], v[56:59], v[176:179], v[72:75]\n"
"ds_read_b64_tr_b16 v[64:65], v7 offset:51200\n"
"v_fma_f32 v45, v20, v44, v45\n"
"v_mfma_f32_16x16x32_bf16 v[76:79], v[56:59], v[180:183], v[76:79]\n"
"ds_read_b64_tr_b16 v[66:67], v7 offset:51712\n"
"v_fma_f32 v42, v20, v41, v42\n"
"v_mfma_f32_16x16x32_bf16 v[80:83], v[56:59], v[184:187], v[80:83]\n"
"buffer_load_dwordx4 v2, s[48:51], s90 offen offset:2048 lds\n"
"v_mul_f32 v149, v129, v149\n"
"v_mfma_f32_16x16x32_bf16 v[84:87], v[56:59], v[188:191], v[84:87]\n"
"ds_read_b64_tr_b16 v[68:69], v7 offset:52224\n"
"v_exp_f32_e64 v129, -v129\n"
"v_mfma_f32_16x16x32_bf16 v[88:91], v[56:59], v[192:195], v[88:91]\n"
"ds_read_b64_tr_b16 v[70:71], v7 offset:52736\n"
"v_fma_f32 v46, v21, v41, v46\n"
"s_waitcnt lgkmcnt(4)\n"
"v_mfma_f32_16x16x32_bf16 v[92:95], v[60:63], v[176:179], v[92:95]\n"
"v_mul_f32 v150, v130, v150\n"
"v_fma_f32 v42, -v21, v45, v42\n"
"v_mfma_f32_16x16x32_bf16 v[96:99], v[60:63], v[180:183], v[96:99]\n"
"buffer_load_dwordx4 v2, s[52:55], s90 offen offset:3072 lds\n"
"v_exp_f32_e64 v130, -v130\n"
"v_mfma_f32_16x16x32_bf16 v[100:103], v[60:63], v[184:187], v[100:103]\n"
"s_add_u32 m0, s97, 49152\n"
"ds_write_b64 v11, v[144:145] offset:44032\n"
"v_fma_f32 v46, v20, v45, v46\n"
"v_mfma_f32_16x16x32_bf16 v[104:107], v[60:63], v[188:191], v[104:107]\n"
"v_mul_f32 v151, v131, v151\n"
"v_fma_f32 v43, v20, v42, v43\n"
"v_mfma_f32_16x16x32_bf16 v[108:111], v[60:63], v[192:195], v[108:111]\n"
"s_waitcnt lgkmcnt(1) vmcnt(8)\n"
"s_barrier\n"
"v_mfma_f32_16x16x32_bf16 v[72:75], v[64:67], v[196:199], v[72:75]\n"
"ds_read_b64_tr_b16 v[56:57], v7 offset:0\n"
"s_add_u32 s90, s90, 589824\n"
"v_fma_f32 v47, v21, v42, v47\n"
"v_mfma_f32_16x16x32_bf16 v[76:79], v[64:67], v[200:203], v[76:79]\n"
"ds_read_b64_tr_b16 v[58:59], v7 offset:512\n"
"v_exp_f32_e64 v131, -v131\n"
"v_mfma_f32_16x16x32_bf16 v[80:83], v[64:67], v[204:207], v[80:83]\n"
"ds_write_b64 v35, v[28:29] offset:16384\n"
"v_fma_f32 v43, -v21, v46, v43\n"
"v_mfma_f32_16x16x32_bf16 v[84:87], v[64:67], v[208:211], v[84:87]\n"
"buffer_load_dwordx4 v1, s[48:51], s90 offen lds\n"
"v_add_f32 v128, 1.0, v128\n"
"v_fma_f32 v47, v20, v46, v47\n"
"v_mfma_f32_16x16x32_bf16 v[88:91], v[64:67], v[212:215], v[88:91]\n"
"v_rcp_f32 v128, v128\n"
"v_mfma_f32_16x16x32_bf16 v[92:95], v[68:71], v[196:199], v[92:95]\n"
"ds_read_b64_tr_b16 v[60:61], v7 offset:1024\n"
"v_cvt_pk_bf16_f32 v36, v40, v41\n"
"v_mfma_f32_16x16x32_bf16 v[96:99], v[68:71], v[200:203], v[96:99]\n"
"ds_read_b64_tr_b16 v[62:63], v7 offset:1536\n"
"v_add_f32 v129, 1.0, v129\n"
"v_cvt_pk_bf16_f32 v37, v42, v43\n"
"v_mfma_f32_16x16x32_bf16 v[100:103], v[68:71], v[204:207], v[100:103]\n"
"v_rcp_f32 v129, v129\n"
"v_mfma_f32_16x16x32_bf16 v[104:107], v[68:71], v[208:211], v[104:107]\n"
"buffer_load_dwordx4 v1, s[52:55], s90 offen offset:1024 lds\n"
"v_cvt_pk_bf16_f32 v38, -v44, -v45\n"
"v_mfma_f32_16x16x32_bf16 v[108:111], v[68:71], v[212:215], v[108:111]\n"
"v_add_f32 v130, 1.0, v130\n"
"v_rcp_f32 v130, v130\n"
"s_waitcnt lgkmcnt(3)\n"
"v_mfma_f32_16x16x32_bf16 v[72:75], v[56:59], v[216:219], v[72:75]\n"
"ds_read_b64_tr_b16 v[64:65], v7 offset:2048\n"
"v_cvt_pk_bf16_f32 v39, -v46, -v47\n"
"v_mfma_f32_16x16x32_bf16 v[76:79], v[56:59], v[220:223], v[76:79]\n"
"ds_read_b64_tr_b16 v[66:67], v7 offset:2560\n"
"v_mfma_f32_16x16x32_bf16 v[80:83], v[56:59], v[224:227], v[80:83]\n"
"buffer_load_dwordx4 v2, s[48:51], s90 offen offset:2048 lds\n"
"v_add_f32 v131, 1.0, v131\n"
"v_mfma_f32_16x16x32_bf16 v[84:87], v[56:59], v[228:231], v[84:87]\n"
"ds_read_b64_tr_b16 v[68:69], v7 offset:3072\n"
"v_rcp_f32 v131, v131\n"
"v_mfma_f32_16x16x32_bf16 v[88:91], v[56:59], v[232:235], v[88:91]\n"
"ds_read_b64_tr_b16 v[70:71], v7 offset:3584\n"
"s_waitcnt lgkmcnt(4)\n"
"v_mfma_f32_16x16x32_bf16 v[92:95], v[60:63], v[216:219], v[92:95]\n"
"v_mul_f32 v128, v128, v148\n"
"v_mfma_f32_16x16x32_bf16 v[96:99], v[60:63], v[220:223], v[96:99]\n"
"buffer_load_dwordx4 v2, s[52:55], s90 offen offset:3072 lds\n"
"v_mul_f32 v129, v129, v149\n"
"v_mfma_f32_16x16x32_bf16 v[100:103], v[60:63], v[224:227], v[100:103]\n"
"s_add_u32 m0, s97, 0\n"
"v_mul_f32 v130, v130, v150\n"
"ds_write_b64 v35, v[36:37] offset:8192\n"
"v_mfma_f32_16x16x32_bf16 v[104:107], v[60:63], v[228:231], v[104:107]\n"
"v_mul_f32 v131, v131, v151\n"
"v_mfma_f32_16x16x32_bf16 v[108:111], v[60:63], v[232:235], v[108:111]\n"
"s_waitcnt lgkmcnt(1) vmcnt(8)\n"
"s_barrier\n"
"v_mfma_f32_16x16x32_bf16 v[72:75], v[64:67], v[236:239], v[72:75]\n"
"ds_read_b64_tr_b16 v[56:57], v7 offset:16384\n"
"s_add_u32 s90, s90, 589824\n"
"v_mfma_f32_16x16x32_bf16 v[76:79], v[64:67], v[240:243], v[76:79]\n"
"ds_read_b64_tr_b16 v[58:59], v7 offset:16896\n"
"v_cvt_pk_bf16_f32 v148, v128, v129\n"
"ds_write_b64 v35, v[38:39] offset:18432\n"
"v_mfma_f32_16x16x32_bf16 v[80:83], v[64:67], v[244:247], v[80:83]\n"
"v_cvt_pk_bf16_f32 v149, v130, v131\n"
"v_mfma_f32_16x16x32_bf16 v[84:87], v[64:67], v[248:251], v[84:87]\n"
"buffer_load_dwordx4 v1, s[48:51], s90 offen lds\n"
"v_mfma_f32_16x16x32_bf16 v[88:91], v[64:67], v[252:255], v[88:91]\n"
"v_mfma_f32_16x16x32_bf16 v[92:95], v[68:71], v[236:239], v[92:95]\n"
"ds_read_b64_tr_b16 v[60:61], v7 offset:17408\n"
"v_mfma_f32_16x16x32_bf16 v[96:99], v[68:71], v[240:243], v[96:99]\n"
"ds_read_b64_tr_b16 v[62:63], v7 offset:17920\n"
"v_mfma_f32_16x16x32_bf16 v[100:103], v[68:71], v[244:247], v[100:103]\n"
"ds_write_b64 v11, v[148:149] offset:45056\n"
"v_mfma_f32_16x16x32_bf16 v[104:107], v[68:71], v[248:251], v[104:107]\n"
"buffer_load_dwordx4 v1, s[52:55], s90 offen offset:1024 lds\n"
"v_mfma_f32_16x16x32_bf16 v[108:111], v[68:71], v[252:255], v[108:111]\n"
"s_waitcnt lgkmcnt(4)\n"
"v_mfma_f32_16x16x32_bf16 v[112:115], v[56:59], a[0:3], 0\n"
"ds_read_b64_tr_b16 v[64:65], v7 offset:18432\n"
"v_lshrrev_b32 v35, 3, v0\n"
"v_mfma_f32_16x16x32_bf16 v[116:119], v[56:59], a[4:7], 0\n"
"ds_read_b64_tr_b16 v[66:67], v7 offset:18944\n"
"v_and_b32 v34, 7, v0\n"
"v_mfma_f32_16x16x32_bf16 v[120:123], v[56:59], a[8:11], 0\n"
"buffer_load_dwordx4 v2, s[48:51], s90 offen offset:2048 lds\n"
"v_xor_b32 v34, v34, v35\n"
"v_mfma_f32_16x16x32_bf16 v[124:127], v[56:59], a[12:15], 0\n"
"ds_read_b64_tr_b16 v[68:69], v7 offset:19456\n"
"v_mul_u32_u24 v35, 1152, v35\n"
"v_mfma_f32_16x16x32_bf16 v[128:131], v[56:59], a[16:19], 0\n"
"ds_read_b64_tr_b16 v[70:71], v7 offset:19968\n"
"v_lshl_add_u32 v34, v34, 4, v35\n"
"s_waitcnt lgkmcnt(5)\n"
"v_mfma_f32_16x16x32_bf16 v[132:135], v[60:63], a[0:3], 0\n"
"s_mul_i32 s38, s95, 9216\n"
"s_lshl_b32 s39, %[cs], 7\n"
"v_mfma_f32_16x16x32_bf16 v[136:139], v[60:63], a[4:7], 0\n"
"buffer_load_dwordx4 v2, s[52:55], s90 offen offset:3072 lds\n"
"s_add_u32 s38, s38, s39\n"
"v_mfma_f32_16x16x32_bf16 v[140:143], v[60:63], a[8:11], 0\n"
"s_add_u32 m0, s97, 16384\n"
"v_add_u32 v34, s38, v34\n"
"v_mfma_f32_16x16x32_bf16 v[144:147], v[60:63], a[12:15], 0\n"
"s_lshl_b32 s39, s95, 10\n"
"s_add_u32 s39, s39, 147456\n"
"v_mfma_f32_16x16x32_bf16 v[148:151], v[60:63], a[16:19], 0\n"
"s_waitcnt lgkmcnt(0) vmcnt(8)\n"
"s_barrier\n"
"v_mfma_f32_16x16x32_bf16 v[112:115], v[64:67], a[20:23], v[112:115]\n"
"ds_read_b64_tr_b16 v[56:57], v7 offset:32768\n"
"s_add_u32 s90, s90, 589824\n"
"v_mfma_f32_16x16x32_bf16 v[116:119], v[64:67], a[24:27], v[116:119]\n"
"ds_read_b64_tr_b16 v[58:59], v7 offset:33280\n"
"v_mul_f32 v92, v72, v92\n"
"v_mfma_f32_16x16x32_bf16 v[120:123], v[64:67], a[28:31], v[120:123]\n"
"v_exp_f32_e64 v72, -v72\n"
"v_mfma_f32_16x16x32_bf16 v[124:127], v[64:67], a[32:35], v[124:127]\n"
"buffer_load_dwordx4 v1, s[48:51], s90 offen lds\n"
"v_mul_f32 v93, v73, v93\n"
"v_mfma_f32_16x16x32_bf16 v[128:131], v[64:67], a[36:39], v[128:131]\n"
"v_exp_f32_e64 v73, -v73\n"
"v_mfma_f32_16x16x32_bf16 v[132:135], v[68:71], a[20:23], v[132:135]\n"
"ds_read_b64_tr_b16 v[60:61], v7 offset:33792\n"
"s_cmp_lt_u32 s95, 2\n"
"s_cselect_b32 s38, 4096, 0\n"
"v_mfma_f32_16x16x32_bf16 v[136:139], v[68:71], a[24:27], v[136:139]\n"
"ds_read_b64_tr_b16 v[62:63], v7 offset:34304\n"
"v_mul_f32 v94, v74, v94\n"
"v_mfma_f32_16x16x32_bf16 v[140:143], v[68:71], a[28:31], v[140:143]\n"
"v_exp_f32_e64 v74, -v74\n"
"v_mfma_f32_16x16x32_bf16 v[144:147], v[68:71], a[32:35], v[144:147]\n"
"buffer_load_dwordx4 v1, s[52:55], s90 offen offset:1024 lds\n"
"v_mfma_f32_16x16x32_bf16 v[148:151], v[68:71], a[36:39], v[148:151]\n"
"v_mul_f32 v95, v75, v95\n"
"v_exp_f32_e64 v75, -v75\n"
"s_waitcnt lgkmcnt(2)\n"
"v_mfma_f32_16x16x32_bf16 v[112:115], v[56:59], a[40:43], v[112:115]\n"
"ds_read_b64_tr_b16 v[64:65], v7 offset:34816\n"
"v_and_b32 v13, 15, v0\n"
"v_mfma_f32_16x16x32_bf16 v[116:119], v[56:59], a[44:47], v[116:119]\n"
"ds_read_b64_tr_b16 v[66:67], v7 offset:35328\n"
"v_add_f32 v72, 1.0, v72\n"
"v_mfma_f32_16x16x32_bf16 v[120:123], v[56:59], a[48:51], v[120:123]\n"
"buffer_load_dwordx4 v2, s[48:51], s90 offen offset:2048 lds\n"
"v_mfma_f32_16x16x32_bf16 v[124:127], v[56:59], a[52:55], v[124:127]\n"
"ds_read_b64_tr_b16 v[68:69], v7 offset:35840\n"
"v_rcp_f32 v72, v72\n"
"v_mfma_f32_16x16x32_bf16 v[128:131], v[56:59], a[56:59], v[128:131]\n"
"ds_read_b64_tr_b16 v[70:71], v7 offset:36352\n"
"v_lshrrev_b32 v54, 2, v13\n"
"s_waitcnt lgkmcnt(4)\n"
"v_mfma_f32_16x16x32_bf16 v[132:135], v[60:63], a[40:43], v[132:135]\n"
"v_add_f32 v73, 1.0, v73\n"
"v_rcp_f32 v73, v73\n"
"v_mfma_f32_16x16x32_bf16 v[136:139], v[60:63], a[44:47], v[136:139]\n"
"buffer_load_dwordx4 v2, s[52:55], s90 offen offset:3072 lds\n"
"v_mfma_f32_16x16x32_bf16 v[140:143], v[60:63], a[48:51], v[140:143]\n"
"s_mov_b32 m0, s39\n"
"v_mul_u32_u24 v54, 20, v54\n"
"v_mfma_f32_16x16x32_bf16 v[144:147], v[60:63], a[52:55], v[144:147]\n"
"buffer_load_dwordx4 v34, s[60:63], 0 offen lds\n"
"v_add_f32 v74, 1.0, v74\n"
"v_mfma_f32_16x16x32_bf16 v[148:151], v[60:63], a[56:59], v[148:151]\n"
"s_waitcnt lgkmcnt(0) vmcnt(9)\n"
"s_barrier\n"
"v_mfma_f32_16x16x32_bf16 v[112:115], v[64:67], a[60:63], v[112:115]\n"
"ds_read_b64_tr_b16 v[56:57], v7 offset:49152\n"
"s_add_u32 m0, s97, 32768\n"
"v_mfma_f32_16x16x32_bf16 v[116:119], v[64:67], a[64:67], v[116:119]\n"
"ds_read_b64_tr_b16 v[58:59], v7 offset:49664\n"
"v_rcp_f32 v74, v74\n"
"v_mfma_f32_16x16x32_bf16 v[120:123], v[64:67], a[68:71], v[120:123]\n"
"s_add_u32 s90, s90, 589824\n"
"v_mfma_f32_16x16x32_bf16 v[124:127], v[64:67], a[72:75], v[124:127]\n"
"buffer_load_dwordx4 v1, s[48:51], s90 offen lds\n"
"v_mfma_f32_16x16x32_bf16 v[128:131], v[64:67], a[76:79], v[128:131]\n"
"v_add_f32 v75, 1.0, v75\n"
"v_rcp_f32 v75, v75\n"
"v_mfma_f32_16x16x32_bf16 v[132:135], v[68:71], a[60:63], v[132:135]\n"
"ds_read_b64_tr_b16 v[60:61], v7 offset:50176\n"
"v_and_b32 v13, 3, v13\n"
"v_mfma_f32_16x16x32_bf16 v[136:139], v[68:71], a[64:67], v[136:139]\n"
"ds_read_b64_tr_b16 v[62:63], v7 offset:50688\n"
"v_mul_f32 v72, v72, v92\n"
"v_mfma_f32_16x16x32_bf16 v[140:143], v[68:71], a[68:71], v[140:143]\n"
"v_mul_f32 v73, v73, v93\n"
"v_mfma_f32_16x16x32_bf16 v[144:147], v[68:71], a[72:75], v[144:147]\n"
"buffer_load_dwordx4 v1, s[52:55], s90 offen offset:1024 lds\n"
"v_mul_f32 v74, v74, v94\n"
"v_mfma_f32_16x16x32_bf16 v[148:151], v[68:71], a[76:79], v[148:151]\n"
"v_mul_f32 v75, v75, v95\n"
"v_cvt_pk_bf16_f32 v92, v72, v73\n"
"s_waitcnt lgkmcnt(2)\n"
"v_mfma_f32_16x16x32_bf16 v[112:115], v[56:59], a[80:83], v[112:115]\n"
"ds_read_b64_tr_b16 v[64:65], v7 offset:51200\n"
"v_add_u32 v13, v13, v54\n"
"v_mfma_f32_16x16x32_bf16 v[116:119], v[56:59], a[84:87], v[116:119]\n"
"ds_read_b64_tr_b16 v[66:67], v7 offset:51712\n"
"v_cvt_pk_bf16_f32 v93, v74, v75\n"
"v_mfma_f32_16x16x32_bf16 v[120:123], v[56:59], a[88:91], v[120:123]\n"
"buffer_load_dwordx4 v2, s[48:51], s90 offen offset:2048 lds\n"
"v_mul_f32 v96, v76, v96\n"
"v_mfma_f32_16x16x32_bf16 v[124:127], v[56:59], a[92:95], v[124:127]\n"
"ds_read_b64_tr_b16 v[68:69], v7 offset:52224\n"
"v_exp_f32_e64 v76, -v76\n"
"v_mfma_f32_16x16x32_bf16 v[128:131], v[56:59], a[96:99], v[128:131]\n"
"ds_read_b64_tr_b16 v[70:71], v7 offset:52736\n"
"s_waitcnt lgkmcnt(4)\n"
"v_mfma_f32_16x16x32_bf16 v[132:135], v[60:63], a[80:83], v[132:135]\n"
"v_mul_f32 v97, v77, v97\n"
"v_lshrrev_b32 v54, 4, v0\n"
"v_mfma_f32_16x16x32_bf16 v[136:139], v[60:63], a[84:87], v[136:139]\n"
"buffer_load_dwordx4 v2, s[52:55], s90 offen offset:3072 lds\n"
"v_exp_f32_e64 v77, -v77\n"
"v_mfma_f32_16x16x32_bf16 v[140:143], v[60:63], a[88:91], v[140:143]\n"
"s_add_u32 m0, s97, 49152\n"
"v_mfma_f32_16x16x32_bf16 v[144:147], v[60:63], a[92:95], v[144:147]\n"
"v_mul_f32 v98, v78, v98\n"
"v_mfma_f32_16x16x32_bf16 v[148:151], v[60:63], a[96:99], v[148:151]\n"
"s_waitcnt lgkmcnt(0) vmcnt(9)\n"
"s_barrier\n"
"v_mfma_f32_16x16x32_bf16 v[112:115], v[64:67], a[100:103], v[112:115]\n"
"ds_read_b64_tr_b16 v[56:57], v7 offset:0\n"
"s_add_u32 s90, s90, 589824\n"
"v_mfma_f32_16x16x32_bf16 v[116:119], v[64:67], a[104:107], v[116:119]\n"
"ds_read_b64_tr_b16 v[58:59], v7 offset:512\n"
"ds_write_b64 v11, v[92:93] offset:51200\n"
"v_exp_f32_e64 v78, -v78\n"
"v_mfma_f32_16x16x32_bf16 v[120:123], v[64:67], a[108:111], v[120:123]\n"
"v_mul_f32 v99, v79, v99\n"
"v_lshlrev_b32 v54, 3, v54\n"
"v_mfma_f32_16x16x32_bf16 v[124:127], v[64:67], a[112:115], v[124:127]\n"
"buffer_load_dwordx4 v1, s[48:51], s90 offen lds\n"
"v_exp_f32_e64 v79, -v79\n"
"v_mfma_f32_16x16x32_bf16 v[128:131], v[64:67], a[116:119], v[128:131]\n"
"s_lshl_b32 s98, %[cs], 7\n"
"v_mfma_f32_16x16x32_bf16 v[132:135], v[68:71], a[100:103], v[132:135]\n"
"ds_read_b64_tr_b16 v[60:61], v7 offset:1024\n"
"v_add_f32 v76, 1.0, v76\n"
"v_mfma_f32_16x16x32_bf16 v[136:139], v[68:71], a[104:107], v[136:139]\n"
"ds_read_b64_tr_b16 v[62:63], v7 offset:1536\n"
"v_rcp_f32 v76, v76\n"
"v_mfma_f32_16x16x32_bf16 v[140:143], v[68:71], a[108:111], v[140:143]\n"
"s_lshl_b32 s99, s95, 5\n"
"v_mfma_f32_16x16x32_bf16 v[144:147], v[68:71], a[112:115], v[144:147]\n"
"buffer_load_dwordx4 v1, s[52:55], s90 offen offset:1024 lds\n"
"v_add_f32 v77, 1.0, v77\n"
"v_mfma_f32_16x16x32_bf16 v[148:151], v[68:71], a[116:119], v[148:151]\n"
"v_rcp_f32 v77, v77\n"
"s_waitcnt lgkmcnt(3)\n"
"v_mfma_f32_16x16x32_bf16 v[112:115], v[56:59], a[120:123], v[112:115]\n"
"ds_read_b64_tr_b16 v[64:65], v7 offset:2048\n"
"v_add_f32 v78, 1.0, v78\n"
"v_mfma_f32_16x16x32_bf16 v[116:119], v[56:59], a[124:127], v[116:119]\n"
"ds_read_b64_tr_b16 v[66:67], v7 offset:2560\n"
"v_mfma_f32_16x16x32_bf16 v[120:123], v[56:59], a[128:131], v[120:123]\n"
"buffer_load_dwordx4 v2, s[48:51], s90 offen offset:2048 lds\n"
"v_rcp_f32 v78, v78\n"
"v_mfma_f32_16x16x32_bf16 v[124:127], v[56:59], a[132:135], v[124:127]\n"
"ds_read_b64_tr_b16 v[68:69], v7 offset:3072\n"
"v_mfma_f32_16x16x32_bf16 v[128:131], v[56:59], a[136:139], v[128:131]\n"
"ds_read_b64_tr_b16 v[70:71], v7 offset:3584\n"
"v_add_f32 v79, 1.0, v79\n"
"s_waitcnt lgkmcnt(4)\n"
"v_mfma_f32_16x16x32_bf16 v[132:135], v[60:63], a[120:123], v[132:135]\n"
"v_rcp_f32 v79, v79\n"
"v_mfma_f32_16x16x32_bf16 v[136:139], v[60:63], a[124:127], v[136:139]\n"
"buffer_load_dwordx4 v2, s[52:55], s90 offen offset:3072 lds\n"
"s_add_u32 s98, s98, s99\n"
"v_mfma_f32_16x16x32_bf16 v[140:143], v[60:63], a[128:131], v[140:143]\n"
"v_add_u32 v34, 36864, v34\n"
"s_add_u32 m0, s39, 4096\n"
"v_mfma_f32_16x16x32_bf16 v[144:147], v[60:63], a[132:135], v[144:147]\n"
"buffer_load_dwordx4 v34, s[60:63], 0 offen lds\n"
"v_mul_f32 v76, v76, v96\n"
"v_mfma_f32_16x16x32_bf16 v[148:151], v[60:63], a[136:139], v[148:151]\n"
"s_waitcnt lgkmcnt(0) vmcnt(10)\n"
"s_barrier\n"
"v_mfma_f32_16x16x32_bf16 v[112:115], v[64:67], a[140:143], v[112:115]\n"
"ds_read_b64_tr_b16 v[56:57], v7 offset:16384\n"
"s_add_u32 m0, s97, 0\n"
"v_mfma_f32_16x16x32_bf16 v[116:119], v[64:67], a[144:147], v[116:119]\n"
"ds_read_b64_tr_b16 v[58:59], v7 offset:16896\n"
"v_mul_f32 v77, v77, v97\n"
"v_mfma_f32_16x16x32_bf16 v[120:123], v[64:67], a[148:151], v[120:123]\n"
"s_add_u32 s90, s90, 589824\n"
"v_mul_f32 v78, v78, v98\n"
"v_mfma_f32_16x16x32_bf16 v[124:127], v[64:67], a[152:155], v[124:127]\n"
"buffer_load_dwordx4 v1, s[48:51], s90 offen lds\n"
"v_mul_f32 v79, v79, v99\n"
"v_mfma_f32_16x16x32_bf16 v[128:131], v[64:67], a[156:159], v[128:131]\n"
"v_cvt_pk_bf16_f32 v96, v76, v77\n"
"v_mfma_f32_16x16x32_bf16 v[132:135], v[68:71], a[140:143], v[132:135]\n"
"ds_read_b64_tr_b16 v[60:61], v7 offset:17408\n"
"v_cvt_pk_bf16_f32 v97, v78, v79\n"
"v_mfma_f32_16x16x32_bf16 v[136:139], v[68:71], a[144:147], v[136:139]\n"
"ds_read_b64_tr_b16 v[62:63], v7 offset:17920\n"
"v_mul_f32 v100, v80, v100\n"
"v_mfma_f32_16x16x32_bf16 v[140:143], v[68:71], a[148:151], v[140:143]\n"
"v_exp_f32_e64 v80, -v80\n"
"v_mfma_f32_16x16x32_bf16 v[144:147], v[68:71], a[152:155], v[144:147]\n"
"buffer_load_dwordx4 v1, s[52:55], s90 offen offset:1024 lds\n"
"v_add_u32 v54, s98, v54\n"
"v_mfma_f32_16x16x32_bf16 v[148:151], v[68:71], a[156:159], v[148:151]\n"
"v_mul_f32 v101, v81, v101\n"
"v_exp_f32_e64 v81, -v81\n"
"s_waitcnt lgkmcnt(2)\n"
"v_mfma_f32_16x16x32_bf16 v[112:115], v[56:59], a[160:163], v[112:115]\n"
"ds_read_b64_tr_b16 v[64:65], v7 offset:18432\n"
"v_mfma_f32_16x16x32_bf16 v[116:119], v[56:59], a[164:167], v[116:119]\n"
"ds_read_b64_tr_b16 v[66:67], v7 offset:18944\n"
"v_mfma_f32_16x16x32_bf16 v[120:123], v[56:59], a[168:171], v[120:123]\n"
"buffer_load_dwordx4 v2, s[48:51], s90 offen offset:2048 lds\n"
"v_mul_f32 v102, v82, v102\n"
"v_mfma_f32_16x16x32_bf16 v[124:127], v[56:59], a[172:175], v[124:127]\n"
"ds_read_b64_tr_b16 v[68:69], v7 offset:19456\n"
"v_exp_f32_e64 v82, -v82\n"
"v_mfma_f32_16x16x32_bf16 v[128:131], v[56:59], a[176:179], v[128:131]\n"
"ds_read_b64_tr_b16 v[70:71], v7 offset:19968\n"
"global_load_dwordx2 v[44:45], v54, %[dsk]\n"
"s_waitcnt lgkmcnt(4)\n"
"v_mfma_f32_16x16x32_bf16 v[132:135], v[60:63], a[160:163], v[132:135]\n"
"v_mul_f32 v103, v83, v103\n"
"v_mfma_f32_16x16x32_bf16 v[136:139], v[60:63], a[164:167], v[136:139]\n"
"buffer_load_dwordx4 v2, s[52:55], s90 offen offset:3072 lds\n"
"v_exp_f32_e64 v83, -v83\n"
"v_mfma_f32_16x16x32_bf16 v[140:143], v[60:63], a[168:171], v[140:143]\n"
"s_add_u32 s39, s39, s38\n"
"v_mad_u32_u24 v34, s38, 9, v34\n"
"v_mfma_f32_16x16x32_bf16 v[144:147], v[60:63], a[172:175], v[144:147]\n"
"s_add_u32 m0, s39, 4096\n"
"v_mul_u32_u24 v55, 1152, v13\n"
"v_mfma_f32_16x16x32_bf16 v[148:151], v[60:63], a[176:179], v[148:151]\n"
"s_waitcnt lgkmcnt(0) vmcnt(10)\n"
"s_barrier\n"
"v_mfma_f32_16x16x32_bf16 v[112:115], v[64:67], a[180:183], v[112:115]\n"
"ds_read_b64_tr_b16 v[56:57], v7 offset:32768\n"
"buffer_load_dwordx4 v34, s[60:63], 0 offen lds\n"
"v_mfma_f32_16x16x32_bf16 v[116:119], v[64:67], a[184:187], v[116:119]\n"
"ds_read_b64_tr_b16 v[58:59], v7 offset:33280\n"
"s_add_u32 m0, s97, 16384\n"
"v_mfma_f32_16x16x32_bf16 v[120:123], v[64:67], a[188:191], v[120:123]\n"
"s_add_u32 s90, s90, 589824\n"
"ds_write_b64 v11, v[96:97] offset:52224\n"
"v_add_f32 v80, 1.0, v80\n"
"v_mfma_f32_16x16x32_bf16 v[124:127], v[64:67], a[192:195], v[124:127]\n"
"buffer_load_dwordx4 v1, s[48:51], s90 offen lds\n"
"v_rcp_f32 v80, v80\n"
"v_mfma_f32_16x16x32_bf16 v[128:131], v[64:67], a[196:199], v[128:131]\n"
"v_add_u32 v55, v55, v54\n"
"v_mfma_f32_16x16x32_bf16 v[132:135], v[68:71], a[180:183], v[132:135]\n"
"ds_read_b64_tr_b16 v[60:61], v7 offset:33792\n"
"v_add_f32 v81, 1.0, v81\n"
"v_mfma_f32_16x16x32_bf16 v[136:139], v[68:71], a[184:187], v[136:139]\n"
"ds_read_b64_tr_b16 v[62:63], v7 offset:34304\n"
"v_rcp_f32 v81, v81\n"
"v_mfma_f32_16x16x32_bf16 v[140:143], v[68:71], a[188:191], v[140:143]\n"
"v_mfma_f32_16x16x32_bf16 v[144:147], v[68:71], a[192:195], v[144:147]\n"
"buffer_load_dwordx4 v1, s[52:55], s90 offen offset:1024 lds\n"
"v_add_f32 v82, 1.0, v82\n"
"v_mfma_f32_16x16x32_bf16 v[148:151], v[68:71], a[196:199], v[148:151]\n"
"v_rcp_f32 v82, v82\n"
"s_waitcnt lgkmcnt(3)\n"
"v_mfma_f32_16x16x32_bf16 v[112:115], v[56:59], a[200:203], v[112:115]\n"
"ds_read_b64_tr_b16 v[64:65], v7 offset:34816\n"
"v_add_f32 v83, 1.0, v83\n"
"v_mfma_f32_16x16x32_bf16 v[116:119], v[56:59], a[204:207], v[116:119]\n"
"ds_read_b64_tr_b16 v[66:67], v7 offset:35328\n"
"v_lshrrev_b32 v41, 5, v0\n"
"v_mfma_f32_16x16x32_bf16 v[120:123], v[56:59], a[208:211], v[120:123]\n"
"buffer_load_dwordx4 v2, s[48:51], s90 offen offset:2048 lds\n"
"v_rcp_f32 v83, v83\n"
"v_mfma_f32_16x16x32_bf16 v[124:127], v[56:59], a[212:215], v[124:127]\n"
"ds_read_b64_tr_b16 v[68:69], v7 offset:35840\n"
"v_mfma_f32_16x16x32_bf16 v[128:131], v[56:59], a[216:219], v[128:131]\n"
"ds_read_b64_tr_b16 v[70:71], v7 offset:36352\n"
"v_mul_f32 v80, v80, v100\n"
"s_waitcnt lgkmcnt(4)\n"
"v_mfma_f32_16x16x32_bf16 v[132:135], v[60:63], a[200:203], v[132:135]\n"
"v_mul_f32 v81, v81, v101\n"
"s_lshl_b32 s98, s95, 1\n"
"v_mfma_f32_16x16x32_bf16 v[136:139], v[60:63], a[204:207], v[136:139]\n"
"buffer_load_dwordx4 v2, s[52:55], s90 offen offset:3072 lds\n"
"v_mul_f32 v82, v82, v102\n"
"v_mfma_f32_16x16x32_bf16 v[140:143], v[60:63], a[208:211], v[140:143]\n"
"s_add_u32 m0, s97, 32768\n"
"v_mul_f32 v83, v83, v103\n"
"v_mfma_f32_16x16x32_bf16 v[144:147], v[60:63], a[212:215], v[144:147]\n"
"v_cvt_pk_bf16_f32 v100, v80, v81\n"
"v_cvt_pk_bf16_f32 v101, v82, v83\n"
"v_mfma_f32_16x16x32_bf16 v[148:151], v[60:63], a[216:219], v[148:151]\n"
"s_waitcnt lgkmcnt(0) vmcnt(11)\n"
"s_barrier\n"
"v_mfma_f32_16x16x32_bf16 v[112:115], v[64:67], a[220:223], v[112:115]\n"
"ds_read_b64_tr_b16 v[56:57], v7 offset:49152\n"
"s_mov_b32 s90, s77\n"
"v_mfma_f32_16x16x32_bf16 v[116:119], v[64:67], a[224:227], v[116:119]\n"
"ds_read_b64_tr_b16 v[58:59], v7 offset:49664\n"
"v_mfma_f32_16x16x32_bf16 v[120:123], v[64:67], a[228:231], v[120:123]\n"
"ds_write_b64 v11, v[100:101] offset:53248\n"
"v_mul_f32 v104, v84, v104\n"
"v_exp_f32_e64 v84, -v84\n"
"v_mfma_f32_16x16x32_bf16 v[124:127], v[64:67], a[232:235], v[124:127]\n"
"buffer_load_dwordx4 v1, s[48:51], s90 offen lds\n"
"v_add_u32 v41, s98, v41\n"
"v_mfma_f32_16x16x32_bf16 v[128:131], v[64:67], a[236:239], v[128:131]\n"
"v_mul_f32 v105, v85, v105\n"
"v_mfma_f32_16x16x32_bf16 v[132:135], v[68:71], a[220:223], v[132:135]\n"
"ds_read_b64_tr_b16 v[60:61], v7 offset:50176\n"
"v_exp_f32_e64 v85, -v85\n"
"v_mfma_f32_16x16x32_bf16 v[136:139], v[68:71], a[224:227], v[136:139]\n"
"ds_read_b64_tr_b16 v[62:63], v7 offset:50688\n"
"v_mfma_f32_16x16x32_bf16 v[140:143], v[68:71], a[228:231], v[140:143]\n"
"v_mul_f32 v106, v86, v106\n"
"v_exp_f32_e64 v86, -v86\n"
"v_mfma_f32_16x16x32_bf16 v[144:147], v[68:71], a[232:235], v[144:147]\n"
"buffer_load_dwordx4 v1, s[52:55], s90 offen offset:1024 lds\n"
"v_and_b32 v43, 7, v13\n"
"v_mfma_f32_16x16x32_bf16 v[148:151], v[68:71], a[236:239], v[148:151]\n"
"v_mul_f32 v107, v87, v107\n"
"s_waitcnt lgkmcnt(3)\n"
"v_mfma_f32_16x16x32_bf16 v[112:115], v[56:59], a[240:243], v[112:115]\n"
"ds_read_b64_tr_b16 v[64:65], v7 offset:51200\n"
"v_exp_f32_e64 v87, -v87\n"
"v_mfma_f32_16x16x32_bf16 v[116:119], v[56:59], a[244:247], v[116:119]\n"
"ds_read_b64_tr_b16 v[66:67], v7 offset:51712\n"
"v_xor_b32 v41, v41, v43\n"
"v_mfma_f32_16x16x32_bf16 v[120:123], v[56:59], a[248:251], v[120:123]\n"
"buffer_load_dwordx4 v2, s[48:51], s90 offen offset:2048 lds\n"
"v_mfma_f32_16x16x32_bf16 v[124:127], v[56:59], a[252:255], v[124:127]\n"
"ds_read_b64_tr_b16 v[68:69], v7 offset:52224\n"
"v_add_f32 v84, 1.0, v84\n"
"v_mfma_f32_16x16x32_bf16 v[128:131], v[56:59], v[152:155], v[128:131]\n"
"ds_read_b64_tr_b16 v[70:71], v7 offset:52736\n"
"v_rcp_f32 v84, v84\n"
"s_waitcnt lgkmcnt(4)\n"
"v_mfma_f32_16x16x32_bf16 v[132:135], v[60:63], a[240:243], v[132:135]\n"
"v_add_f32 v85, 1.0, v85\n"
"v_mfma_f32_16x16x32_bf16 v[136:139], v[60:63], a[244:247], v[136:139]\n"
"buffer_load_dwordx4 v2, s[52:55], s90 offen offset:3072 lds\n"
"v_rcp_f32 v85, v85\n"
"v_mfma_f32_16x16x32_bf16 v[140:143], v[60:63], a[248:251], v[140:143]\n"
"s_add_u32 m0, s97, 49152\n"
"v_lshlrev_b32 v41, 4, v41\n"
"v_mfma_f32_16x16x32_bf16 v[144:147], v[60:63], a[252:255], v[144:147]\n"
"v_add_f32 v86, 1.0, v86\n"
"v_mfma_f32_16x16x32_bf16 v[148:151], v[60:63], v[152:155], v[148:151]\n"
"s_waitcnt lgkmcnt(0) vmcnt(9)\n"
"s_barrier\n"
"v_mfma_f32_16x16x32_bf16 v[112:115], v[64:67], v[156:159], v[112:115]\n"
"ds_read_b64_tr_b16 v[56:57], v7 offset:0\n"
"s_add_u32 s90, s90, 589824\n"
"v_mfma_f32_16x16x32_bf16 v[116:119], v[64:67], v[160:163], v[116:119]\n"
"ds_read_b64_tr_b16 v[58:59], v7 offset:512\n"
"v_rcp_f32 v86, v86\n"
"v_mfma_f32_16x16x32_bf16 v[120:123], v[64:67], v[164:167], v[120:123]\n"
"v_lshl_add_u32 v41, v13, 7, v41\n"
"v_mfma_f32_16x16x32_bf16 v[124:127], v[64:67], v[168:171], v[124:127]\n"
"buffer_load_dwordx4 v1, s[48:51], s90 offen lds\n"
"v_add_f32 v87, 1.0, v87\n"
"v_mfma_f32_16x16x32_bf16 v[128:131], v[64:67], v[172:175], v[128:131]\n"
"v_rcp_f32 v87, v87\n"
"v_mfma_f32_16x16x32_bf16 v[132:135], v[68:71], v[156:159], v[132:135]\n"
"ds_read_b64_tr_b16 v[60:61], v7 offset:1024\n"
"v_mul_f32 v84, v84, v104\n"
"v_mfma_f32_16x16x32_bf16 v[136:139], v[68:71], v[160:163], v[136:139]\n"
"ds_read_b64_tr_b16 v[62:63], v7 offset:1536\n"
"v_bfe_u32 v43, v0, 4, 1\n"
"v_mfma_f32_16x16x32_bf16 v[140:143], v[68:71], v[164:167], v[140:143]\n"
"v_mul_f32 v85, v85, v105\n"
"v_mul_f32 v86, v86, v106\n"
"v_mfma_f32_16x16x32_bf16 v[144:147], v[68:71], v[168:171], v[144:147]\n"
"buffer_load_dwordx4 v1, s[52:55], s90 offen offset:1024 lds\n"
"v_mul_f32 v87, v87, v107\n"
"v_mfma_f32_16x16x32_bf16 v[148:151], v[68:71], v[172:175], v[148:151]\n"
"v_cvt_pk_bf16_f32 v104, v84, v85\n"
"s_waitcnt lgkmcnt(2)\n"
"v_mfma_f32_16x16x32_bf16 v[112:115], v[56:59], v[176:179], v[112:115]\n"
"ds_read_b64_tr_b16 v[64:65], v7 offset:2048\n"
"v_cvt_pk_bf16_f32 v105, v86, v87\n"
"v_mfma_f32_16x16x32_bf16 v[116:119], v[56:59], v[180:183], v[116:119]\n"
"ds_read_b64_tr_b16 v[66:67], v7 offset:2560\n"
"v_mul_f32 v108, v88, v108\n"
"v_mfma_f32_16x16x32_bf16 v[120:123], v[56:59], v[184:187], v[120:123]\n"
"buffer_load_dwordx4 v2, s[48:51], s90 offen offset:2048 lds\n"
"v_exp_f32_e64 v88, -v88\n"
"v_mfma_f32_16x16x32_bf16 v[124:127], v[56:59], v[188:191], v[124:127]\n"
"ds_read_b64_tr_b16 v[68:69], v7 offset:3072\n"
"v_mfma_f32_16x16x32_bf16 v[128:131], v[56:59], v[192:195], v[128:131]\n"
"ds_read_b64_tr_b16 v[70:71], v7 offset:3584\n"
"v_lshl_add_u32 v43, v43, 3, v41\n"
"s_waitcnt lgkmcnt(4)\n"
"v_mfma_f32_16x16x32_bf16 v[132:135], v[60:63], v[176:179], v[132:135]\n"
"v_mul_f32 v109, v89, v109\n"
"v_exp_f32_e64 v89, -v89\n"
"v_mfma_f32_16x16x32_bf16 v[136:139], v[60:63], v[180:183], v[136:139]\n"
"buffer_load_dwordx4 v2, s[52:55], s90 offen offset:3072 lds\n"
"v_mfma_f32_16x16x32_bf16 v[140:143], v[60:63], v[184:187], v[140:143]\n"
"s_add_u32 m0, s97, 0\n"
"v_mul_f32 v110, v90, v110\n"
"v_mfma_f32_16x16x32_bf16 v[144:147], v[60:63], v[188:191], v[144:147]\n"
"v_exp_f32_e64 v90, -v90\n"
"v_mfma_f32_16x16x32_bf16 v[148:151], v[60:63], v[192:195], v[148:151]\n"
"s_waitcnt lgkmcnt(0) vmcnt(8)\n"
"s_barrier\n"
"v_mfma_f32_16x16x32_bf16 v[112:115], v[64:67], v[196:199], v[112:115]\n"
"ds_read_b64_tr_b16 v[56:57], v7 offset:16384\n"
"s_add_u32 s90, s90, 589824\n"
"v_mfma_f32_16x16x32_bf16 v[116:119], v[64:67], v[200:203], v[116:119]\n"
"ds_read_b64_tr_b16 v[58:59], v7 offset:16896\n"
"ds_write_b64 v11, v[104:105] offset:54272\n"
"v_mfma_f32_16x16x32_bf16 v[120:123], v[64:67], v[204:207], v[120:123]\n"
"v_mul_f32 v111, v91, v111\n"
"v_exp_f32_e64 v91, -v91\n"
"v_mfma_f32_16x16x32_bf16 v[124:127], v[64:67], v[208:211], v[124:127]\n"
"buffer_load_dwordx4 v1, s[48:51], s90 offen lds\n"
"v_add_u32 v43, 147456, v43\n"
"v_mfma_f32_16x16x32_bf16 v[128:131], v[64:67], v[212:215], v[128:131]\n"
"v_add_f32 v88, 1.0, v88\n"
"v_mfma_f32_16x16x32_bf16 v[132:135], v[68:71], v[196:199], v[132:135]\n"
"ds_read_b64_tr_b16 v[60:61], v7 offset:17408\n"
"v_rcp_f32 v88, v88\n"
"v_mfma_f32_16x16x32_bf16 v[136:139], v[68:71], v[200:203], v[136:139]\n"
"ds_read_b64_tr_b16 v[62:63], v7 offset:17920\n"
"v_xor_b32 v41, 64, v43\n"
"v_mfma_f32_16x16x32_bf16 v[140:143], v[68:71], v[204:207], v[140:143]\n"
"v_add_f32 v89, 1.0, v89\n"
"v_mfma_f32_16x16x32_bf16 v[144:147], v[68:71], v[208:211], v[144:147]\n"
"buffer_load_dwordx4 v1, s[52:55], s90 offen offset:1024 lds\n"
"v_rcp_f32 v89, v89\n"
"v_mfma_f32_16x16x32_bf16 v[148:151], v[68:71], v[212:215], v[148:151]\n"
"v_add_f32 v90, 1.0, v90\n"
"s_waitcnt lgkmcnt(3)\n"
"v_mfma_f32_16x16x32_bf16 v[112:115], v[56:59], v[216:219], v[112:115]\n"
"ds_read_b64_tr_b16 v[64:65], v7 offset:18432\n"
"v_lshlrev_b32 v13, 3, v13\n"
"v_mfma_f32_16x16x32_bf16 v[116:119], v[56:59], v[220:223], v[116:119]\n"
"ds_read_b64_tr_b16 v[66:67], v7 offset:18944\n"
"v_rcp_f32 v90, v90\n"
"v_mfma_f32_16x16x32_bf16 v[120:123], v[56:59], v[224:227], v[120:123]\n"
"buffer_load_dwordx4 v2, s[48:51], s90 offen offset:2048 lds\n"
"v_mfma_f32_16x16x32_bf16 v[124:127], v[56:59], v[228:231], v[124:127]\n"
"ds_read_b64_tr_b16 v[68:69], v7 offset:19456\n"
"v_add_f32 v91, 1.0, v91\n"
"v_mfma_f32_16x16x32_bf16 v[128:131], v[56:59], v[232:235], v[128:131]\n"
"ds_read_b64_tr_b16 v[70:71], v7 offset:19968\n"
"v_rcp_f32 v91, v91\n"
"s_waitcnt lgkmcnt(4)\n"
"v_mfma_f32_16x16x32_bf16 v[132:135], v[60:63], v[216:219], v[132:135]\n"
"v_add_u32 v13, 157696, v13\n"
"v_mfma_f32_16x16x32_bf16 v[136:139], v[60:63], v[220:223], v[136:139]\n"
"buffer_load_dwordx4 v2, s[52:55], s90 offen offset:3072 lds\n"
"v_mul_f32 v88, v88, v108\n"
"v_mfma_f32_16x16x32_bf16 v[140:143], v[60:63], v[224:227], v[140:143]\n"
"s_add_u32 m0, s97, 16384\n"
"v_mul_f32 v89, v89, v109\n"
"v_mfma_f32_16x16x32_bf16 v[144:147], v[60:63], v[228:231], v[144:147]\n"
"v_mul_f32 v90, v90, v110\n"
"v_mfma_f32_16x16x32_bf16 v[148:151], v[60:63], v[232:235], v[148:151]\n"
"s_waitcnt lgkmcnt(0) vmcnt(8)\n"
"s_barrier\n"
"v_mfma_f32_16x16x32_bf16 v[112:115], v[64:67], v[236:239], v[112:115]\n"
"ds_read_b64_tr_b16 v[56:57], v7 offset:32768\n"
"s_add_u32 s90, s90, 589824\n"
"v_mfma_f32_16x16x32_bf16 v[116:119], v[64:67], v[240:243], v[116:119]\n"
"ds_read_b64_tr_b16 v[58:59], v7 offset:33280\n"
"v_mul_f32 v91, v91, v111\n"
"v_mfma_f32_16x16x32_bf16 v[120:123], v[64:67], v[244:247], v[120:123]\n"
"v_cvt_pk_bf16_f32 v108, v88, v89\n"
"v_cvt_pk_bf16_f32 v109, v90, v91\n"
"v_mfma_f32_16x16x32_bf16 v[124:127], v[64:67], v[248:251], v[124:127]\n"
"buffer_load_dwordx4 v1, s[48:51], s90 offen lds\n"
"v_mfma_f32_16x16x32_bf16 v[128:131], v[64:67], v[252:255], v[128:131]\n"
"v_mfma_f32_16x16x32_bf16 v[132:135], v[68:71], v[236:239], v[132:135]\n"
"ds_read_b64_tr_b16 v[60:61], v7 offset:33792\n"
"v_mfma_f32_16x16x32_bf16 v[136:139], v[68:71], v[240:243], v[136:139]\n"
"ds_read_b64_tr_b16 v[62:63], v7 offset:34304\n"
"v_mfma_f32_16x16x32_bf16 v[140:143], v[68:71], v[244:247], v[140:143]\n"
"ds_write_b64 v11, v[108:109] offset:55296\n"
"v_mfma_f32_16x16x32_bf16 v[144:147], v[68:71], v[248:251], v[144:147]\n"
"buffer_load_dwordx4 v1, s[52:55], s90 offen offset:1024 lds\n"
"v_mfma_f32_16x16x32_bf16 v[148:151], v[68:71], v[252:255], v[148:151]\n"
"s_waitcnt lgkmcnt(3)\n"
"v_mfma_f32_16x16x32_bf16 v[72:75], v[56:59], a[0:3], 0\n"
"ds_read_b64_tr_b16 v[64:65], v7 offset:34816\n"
"v_mfma_f32_16x16x32_bf16 v[76:79], v[56:59], a[4:7], 0\n"
"ds_read_b64_tr_b16 v[66:67], v7 offset:35328\n"
"v_mfma_f32_16x16x32_bf16 v[80:83], v[56:59], a[8:11], 0\n"
"buffer_load_dwordx4 v2, s[48:51], s90 offen offset:2048 lds\n"
"v_mfma_f32_16x16x32_bf16 v[84:87], v[56:59], a[12:15], 0\n"
"ds_read_b64_tr_b16 v[68:69], v7 offset:35840\n"
"v_mfma_f32_16x16x32_bf16 v[88:91], v[56:59], a[16:19], 0\n"
"ds_read_b64_tr_b16 v[70:71], v7 offset:36352\n"
"s_waitcnt lgkmcnt(5)\n"
"v_mfma_f32_16x16x32_bf16 v[92:95], v[60:63], a[0:3], 0\n"
"v_mfma_f32_16x16x32_bf16 v[96:99], v[60:63], a[4:7], 0\n"
"buffer_load_dwordx4 v2, s[52:55], s90 offen offset:3072 lds\n"
"v_mfma_f32_16x16x32_bf16 v[100:103], v[60:63], a[8:11], 0\n"
"s_add_u32 m0, s97, 32768\n"
"v_mfma_f32_16x16x32_bf16 v[104:107], v[60:63], a[12:15], 0\n"
"v_mfma_f32_16x16x32_bf16 v[108:111], v[60:63], a[16:19], 0\n"
"s_waitcnt lgkmcnt(0) vmcnt(8)\n"
"s_barrier\n"
"v_mfma_f32_16x16x32_bf16 v[72:75], v[64:67], a[20:23], v[72:75]\n"
"ds_read_b64_tr_b16 v[56:57], v7 offset:49152\n"
"s_add_u32 s90, s90, 589824\n"
"v_mfma_f32_16x16x32_bf16 v[76:79], v[64:67], a[24:27], v[76:79]\n"
"ds_read_b64_tr_b16 v[58:59], v7 offset:49664\n"
"v_add_u32 v11, 61440, v11\n"
"v_mfma_f32_16x16x32_bf16 v[80:83], v[64:67], a[28:31], v[80:83]\n"
"v_mul_f32 v132, v112, v132\n"
"v_exp_f32_e64 v112, -v112\n"
"v_mfma_f32_16x16x32_bf16 v[84:87], v[64:67], a[32:35], v[84:87]\n"
"buffer_load_dwordx4 v1, s[48:51], s90 offen lds\n"
"ds_read_b64 v[34:35], v43 offset:0\n"
"v_mfma_f32_16x16x32_bf16 v[88:91], v[64:67], a[36:39], v[88:91]\n"
"v_mul_f32 v133, v113, v133\n"
"ds_read_b64 v[36:37], v41 offset:512\n"
"v_mfma_f32_16x16x32_bf16 v[92:95], v[68:71], a[20:23], v[92:95]\n"
"ds_read_b64_tr_b16 v[60:61], v7 offset:50176\n"
"v_exp_f32_e64 v113, -v113\n"
"v_mfma_f32_16x16x32_bf16 v[96:99], v[68:71], a[24:27], v[96:99]\n"
"ds_read_b64_tr_b16 v[62:63], v7 offset:50688\n"
"ds_read_b64 v[38:39], v43 offset:1024\n"
"v_mfma_f32_16x16x32_bf16 v[100:103], v[68:71], a[28:31], v[100:103]\n"
"v_mul_f32 v134, v114, v134\n"
"ds_read_b64 v[40:41], v41 offset:1536\n"
"v_mfma_f32_16x16x32_bf16 v[104:107], v[68:71], a[32:35], v[104:107]\n"
"buffer_load_dwordx4 v1, s[52:55], s90 offen offset:1024 lds\n"
"v_exp_f32_e64 v114, -v114\n"
"v_mfma_f32_16x16x32_bf16 v[108:111], v[68:71], a[36:39], v[108:111]\n"
"v_mul_f32 v135, v115, v135\n"
"s_waitcnt lgkmcnt(6)\n"
"v_mfma_f32_16x16x32_bf16 v[72:75], v[56:59], a[40:43], v[72:75]\n"
"ds_read_b64_tr_b16 v[64:65], v7 offset:51200\n"
"ds_read_b64 v[42:43], v43 offset:2048\n"
"v_mfma_f32_16x16x32_bf16 v[76:79], v[56:59], a[44:47], v[76:79]\n"
"ds_read_b64_tr_b16 v[66:67], v7 offset:51712\n"
"v_exp_f32_e64 v115, -v115\n"
"v_mfma_f32_16x16x32_bf16 v[80:83], v[56:59], a[48:51], v[80:83]\n"
"buffer_load_dwordx4 v2, s[48:51], s90 offen offset:2048 lds\n"
"v_lshlrev_b32 v46, 16, v44\n"
"v_mfma_f32_16x16x32_bf16 v[84:87], v[56:59], a[52:55], v[84:87]\n"
"ds_read_b64_tr_b16 v[68:69], v7 offset:52224\n"
"v_add_f32 v112, 1.0, v112\n"
"v_mfma_f32_16x16x32_bf16 v[88:91], v[56:59], a[56:59], v[88:91]\n"
"ds_read_b64_tr_b16 v[70:71], v7 offset:52736\n"
"v_and_b32 v47, 0xffff0000, v44\n"
"s_waitcnt lgkmcnt(7)\n"
"v_mfma_f32_16x16x32_bf16 v[92:95], v[60:63], a[40:43], v[92:95]\n"
"v_rcp_f32 v112, v112\n"
"v_mfma_f32_16x16x32_bf16 v[96:99], v[60:63], a[44:47], v[96:99]\n"
"buffer_load_dwordx4 v2, s[52:55], s90 offen offset:3072 lds\n"
"v_add_f32 v113, 1.0, v113\n"
"v_mfma_f32_16x16x32_bf16 v[100:103], v[60:63], a[48:51], v[100:103]\n"
"s_add_u32 m0, s97, 49152\n"
"v_lshlrev_b32 v48, 16, v45\n"
"v_mfma_f32_16x16x32_bf16 v[104:107], v[60:63], a[52:55], v[104:107]\n"
"v_rcp_f32 v113, v113\n"
"v_mfma_f32_16x16x32_bf16 v[108:111], v[60:63], a[56:59], v[108:111]\n"
"s_waitcnt lgkmcnt(0) vmcnt(8)\n"
"s_barrier\n"
"v_mfma_f32_16x16x32_bf16 v[72:75], v[64:67], a[60:63], v[72:75]\n"
"ds_read_b64_tr_b16 v[56:57], v7 offset:0\n"
"s_add_u32 s90, s90, 589824\n"
"v_mfma_f32_16x16x32_bf16 v[76:79], v[64:67], a[64:67], v[76:79]\n"
"ds_read_b64_tr_b16 v[58:59], v7 offset:512\n"
"v_add_f32 v114, 1.0, v114\n"
"v_mfma_f32_16x16x32_bf16 v[80:83], v[64:67], a[68:71], v[80:83]\n"
"v_rcp_f32 v114, v114\n"
"v_mfma_f32_16x16x32_bf16 v[84:87], v[64:67], a[72:75], v[84:87]\n"
"buffer_load_dwordx4 v1, s[48:51], s90 offen lds\n"
"v_mfma_f32_16x16x32_bf16 v[88:91], v[64:67], a[76:79], v[88:91]\n"
"v_add_f32 v115, 1.0, v115\n"
"v_rcp_f32 v115, v115\n"
"v_mfma_f32_16x16x32_bf16 v[92:95], v[68:71], a[60:63], v[92:95]\n"
"ds_read_b64_tr_b16 v[60:61], v7 offset:1024\n"
"v_and_b32 v49, 0xffff0000, v45\n"
"v_mfma_f32_16x16x32_bf16 v[96:99], v[68:71], a[64:67], v[96:99]\n"
"ds_read_b64_tr_b16 v[62:63], v7 offset:1536\n"
"ds_read_b64 v[50:51], v13\n"
"v_mfma_f32_16x16x32_bf16 v[100:103], v[68:71], a[68:71], v[100:103]\n"
"v_mul_f32 v112, v112, v132\n"
"v_mul_f32 v113, v113, v133\n"
"v_mfma_f32_16x16x32_bf16 v[104:107], v[68:71], a[72:75], v[104:107]\n"
"buffer_load_dwordx4 v1, s[52:55], s90 offen offset:1024 lds\n"
"v_mul_f32 v114, v114, v134\n"
"v_mfma_f32_16x16x32_bf16 v[108:111], v[68:71], a[76:79], v[108:111]\n"
"v_mul_f32 v115, v115, v135\n"
"v_mul_f32 v46, 0x40053526, v46\n"
"s_waitcnt lgkmcnt(3)\n"
"v_mfma_f32_16x16x32_bf16 v[72:75], v[56:59], a[80:83], v[72:75]\n"
"ds_read_b64_tr_b16 v[64:65], v7 offset:2048\n"
"v_cvt_pk_bf16_f32 v132, v112, v113\n"
"v_mfma_f32_16x16x32_bf16 v[76:79], v[56:59], a[84:87], v[76:79]\n"
"ds_read_b64_tr_b16 v[66:67], v7 offset:2560\n"
"v_cvt_pk_bf16_f32 v133, v114, v115\n"
"v_mfma_f32_16x16x32_bf16 v[80:83], v[56:59], a[88:91], v[80:83]\n"
"buffer_load_dwordx4 v2, s[48:51], s90 offen offset:2048 lds\n"
"v_mul_f32 v47, 0x40053526, v47\n"
"v_mfma_f32_16x16x32_bf16 v[84:87], v[56:59], a[92:95], v[84:87]\n"
"ds_read_b64_tr_b16 v[68:69], v7 offset:3072\n"
"v_mul_f32 v136, v116, v136\n"
"v_mfma_f32_16x16x32_bf16 v[88:91], v[56:59], a[96:99], v[88:91]\n"
"ds_read_b64_tr_b16 v[70:71], v7 offset:3584\n"
"v_exp_f32_e64 v116, -v116\n"
"s_waitcnt lgkmcnt(5)\n"
"v_mfma_f32_16x16x32_bf16 v[92:95], v[60:63], a[80:83], v[92:95]\n"
"v_mul_f32 v48, 0x40053526, v48\n"
"v_mfma_f32_16x16x32_bf16 v[96:99], v[60:63], a[84:87], v[96:99]\n"
"buffer_load_dwordx4 v2, s[52:55], s90 offen offset:3072 lds\n"
"v_mul_f32 v137, v117, v137\n"
"v_mfma_f32_16x16x32_bf16 v[100:103], v[60:63], a[88:91], v[100:103]\n"
"s_add_u32 m0, s97, 0\n"
"v_exp_f32_e64 v117, -v117\n"
"v_mfma_f32_16x16x32_bf16 v[104:107], v[60:63], a[92:95], v[104:107]\n"
"v_mul_f32 v138, v118, v138\n"
"v_mul_f32 v49, 0x40053526, v49\n"
"v_mfma_f32_16x16x32_bf16 v[108:111], v[60:63], a[96:99], v[108:111]\n"
"s_waitcnt lgkmcnt(0) vmcnt(8)\n"
"s_barrier\n"
"v_mfma_f32_16x16x32_bf16 v[72:75], v[64:67], a[100:103], v[72:75]\n"
"ds_read_b64_tr_b16 v[56:57], v7 offset:16384\n"
"s_add_u32 s90, s90, 589824\n"
"v_mfma_f32_16x16x32_bf16 v[76:79], v[64:67], a[104:107], v[76:79]\n"
"ds_read_b64_tr_b16 v[58:59], v7 offset:16896\n"
"ds_write_b64 v11, v[132:133] offset:0\n"
"v_mfma_f32_16x16x32_bf16 v[80:83], v[64:67], a[108:111], v[80:83]\n"
"v_exp_f32_e64 v118, -v118\n"
"v_mfma_f32_16x16x32_bf16 v[84:87], v[64:67], a[112:115], v[84:87]\n"
"buffer_load_dwordx4 v1, s[48:51], s90 offen lds\n"
"v_mul_f32 v139, v119, v139\n"
"v_mfma_f32_16x16x32_bf16 v[88:91], v[64:67], a[116:119], v[88:91]\n"
"v_exp_f32_e64 v119, -v119\n"
"v_mfma_f32_16x16x32_bf16 v[92:95], v[68:71], a[100:103], v[92:95]\n"
"ds_read_b64_tr_b16 v[60:61], v7 offset:17408\n"
"ds_read_b64 v[52:53], v13 offset:32\n"
"v_mfma_f32_16x16x32_bf16 v[96:99], v[68:71], a[104:107], v[96:99]\n"
"ds_read_b64_tr_b16 v[62:63], v7 offset:17920\n"
"v_add_f32 v116, 1.0, v116\n"
"v_mfma_f32_16x16x32_bf16 v[100:103], v[68:71], a[108:111], v[100:103]\n"
"v_rcp_f32 v116, v116\n"
"v_mfma_f32_16x16x32_bf16 v[104:107], v[68:71], a[112:115], v[104:107]\n"
"buffer_load_dwordx4 v1, s[52:55], s90 offen offset:1024 lds\n"
"v_lshlrev_b32 v14, 16, v34\n"
"v_mfma_f32_16x16x32_bf16 v[108:111], v[68:71], a[116:119], v[108:111]\n"
"v_add_f32 v117, 1.0, v117\n"
"v_rcp_f32 v117, v117\n"
"s_waitcnt lgkmcnt(4)\n"
"v_mfma_f32_16x16x32_bf16 v[72:75], v[56:59], a[120:123], v[72:75]\n"
"ds_read_b64_tr_b16 v[64:65], v7 offset:18432\n"
"v_mfma_f32_16x16x32_bf16 v[76:79], v[56:59], a[124:127], v[76:79]\n"
"ds_read_b64_tr_b16 v[66:67], v7 offset:18944\n"
"v_fma_f32 v54, v14, v50, -v51\n"
"v_mfma_f32_16x16x32_bf16 v[80:83], v[56:59], a[128:131], v[80:83]\n"
"buffer_load_dwordx4 v2, s[48:51], s90 offen offset:2048 lds\n"
"v_add_f32 v118, 1.0, v118\n"
"v_mfma_f32_16x16x32_bf16 v[84:87], v[56:59], a[132:135], v[84:87]\n"
"ds_read_b64_tr_b16 v[68:69], v7 offset:19456\n"
"v_rcp_f32 v118, v118\n"
"v_mfma_f32_16x16x32_bf16 v[88:91], v[56:59], a[136:139], v[88:91]\n"
"ds_read_b64_tr_b16 v[70:71], v7 offset:19968\n"
"v_mul_f32 v14, 0x40053526, v14\n"
"s_waitcnt lgkmcnt(4)\n"
"v_mfma_f32_16x16x32_bf16 v[92:95], v[60:63], a[120:123], v[92:95]\n"
"v_add_f32 v119, 1.0, v119\n"
"v_fma_f32 v14, v54, v46, v14\n"
"v_mfma_f32_16x16x32_bf16 v[96:99], v[60:63], a[124:127], v[96:99]\n"
"buffer_load_dwordx4 v2, s[52:55], s90 offen offset:3072 lds\n"
"v_rcp_f32 v119, v119\n"
"v_mfma_f32_16x16x32_bf16 v[100:103], v[60:63], a[128:131], v[100:103]\n"
"s_add_u32 m0, s97, 16384\n"
"v_and_b32 v15, 0xffff0000, v34\n"
"v_mfma_f32_16x16x32_bf16 v[104:107], v[60:63], a[132:135], v[104:107]\n"
"v_mul_f32 v116, v116, v136\n"
"v_mfma_f32_16x16x32_bf16 v[108:111], v[60:63], a[136:139], v[108:111]\n"
"s_waitcnt lgkmcnt(0) vmcnt(8)\n"
"s_barrier\n"
"v_mfma_f32_16x16x32_bf16 v[72:75], v[64:67], a[140:143], v[72:75]\n"
"ds_read_b64_tr_b16 v[56:57], v7 offset:32768\n"
"s_add_u32 s90, s90, 589824\n"
"v_mfma_f32_16x16x32_bf16 v[76:79], v[64:67], a[144:147], v[76:79]\n"
"ds_read_b64_tr_b16 v[58:59], v7 offset:33280\n"
"v_mul_f32 v117, v117, v137\n"
"v_mfma_f32_16x16x32_bf16 v[80:83], v[64:67], a[148:151], v[80:83]\n"
"v_mul_f32 v118, v118, v138\n"
"v_fma_f32 v54, v15, v50, -v51\n"
"v_mfma_f32_16x16x32_bf16 v[84:87], v[64:67], a[152:155], v[84:87]\n"
"buffer_load_dwordx4 v1, s[48:51], s90 offen lds\n"
"v_mul_f32 v119, v119, v139\n"
"v_mfma_f32_16x16x32_bf16 v[88:91], v[64:67], a[156:159], v[88:91]\n"
"v_cvt_pk_bf16_f32 v136, v116, v117\n"
"v_cvt_pk_bf16_f32 v137, v118, v119\n"
"v_mfma_f32_16x16x32_bf16 v[92:95], v[68:71], a[140:143], v[92:95]\n"
"ds_read_b64_tr_b16 v[60:61], v7 offset:33792\n"
"v_mul_f32 v140, v120, v140\n"
"v_mfma_f32_16x16x32_bf16 v[96:99], v[68:71], a[144:147], v[96:99]\n"
"ds_read_b64_tr_b16 v[62:63], v7 offset:34304\n"
"v_mul_f32 v15, 0x40053526, v15\n"
"v_mfma_f32_16x16x32_bf16 v[100:103], v[68:71], a[148:151], v[100:103]\n"
"v_exp_f32_e64 v120, -v120\n"
"v_mfma_f32_16x16x32_bf16 v[104:107], v[68:71], a[152:155], v[104:107]\n"
"buffer_load_dwordx4 v1, s[52:55], s90 offen offset:1024 lds\n"
"v_mul_f32 v141, v121, v141\n"
"v_mfma_f32_16x16x32_bf16 v[108:111], v[68:71], a[156:159], v[108:111]\n"
"v_exp_f32_e64 v121, -v121\n"
"s_waitcnt lgkmcnt(2)\n"
"v_mfma_f32_16x16x32_bf16 v[72:75], v[56:59], a[160:163], v[72:75]\n"
"ds_read_b64_tr_b16 v[64:65], v7 offset:34816\n"
"v_fma_f32 v15, v54, v47, v15\n"
"v_mfma_f32_16x16x32_bf16 v[76:79], v[56:59], a[164:167], v[76:79]\n"
"ds_read_b64_tr_b16 v[66:67], v7 offset:35328\n"
"v_mul_f32 v142, v122, v142\n"
"v_mfma_f32_16x16x32_bf16 v[80:83], v[56:59], a[168:171], v[80:83]\n"
"buffer_load_dwordx4 v2, s[48:51], s90 offen offset:2048 lds\n"
"v_lshlrev_b32 v16, 16, v35\n"
"v_mfma_f32_16x16x32_bf16 v[84:87], v[56:59], a[172:175], v[84:87]\n"
"ds_read_b64_tr_b16 v[68:69], v7 offset:35840\n"
"v_exp_f32_e64 v122, -v122\n"
"v_mfma_f32_16x16x32_bf16 v[88:91], v[56:59], a[176:179], v[88:91]\n"
"ds_read_b64_tr_b16 v[70:71], v7 offset:36352\n"
"s_waitcnt lgkmcnt(4)\n"
"v_mfma_f32_16x16x32_bf16 v[92:95], v[60:63], a[160:163], v[92:95]\n"
"v_mul_f32 v143, v123, v143\n"
"v_exp_f32_e64 v123, -v123\n"
"v_mfma_f32_16x16x32_bf16 v[96:99], v[60:63], a[164:167], v[96:99]\n"
"buffer_load_dwordx4 v2, s[52:55], s90 offen offset:3072 lds\n"
"v_fma_f32 v54, v16, v50, -v51\n"
"v_mfma_f32_16x16x32_bf16 v[100:103], v[60:63], a[168:171], v[100:103]\n"
"s_add_u32 m0, s97, 32768\n"
"v_mul_f32 v16, 0x40053526, v16\n"
"v_mfma_f32_16x16x32_bf16 v[104:107], v[60:63], a[172:175], v[104:107]\n"
"v_add_f32 v120, 1.0, v120\n"
"v_rcp_f32 v120, v120\n"
"v_mfma_f32_16x16x32_bf16 v[108:111], v[60:63], a[176:179], v[108:111]\n"
"s_waitcnt lgkmcnt(0) vmcnt(8)\n"
"s_barrier\n"
"v_mfma_f32_16x16x32_bf16 v[72:75], v[64:67], a[180:183], v[72:75]\n"
"ds_read_b64_tr_b16 v[56:57], v7 offset:49152\n"
"s_add_u32 s90, s90, 589824\n"
"v_mfma_f32_16x16x32_bf16 v[76:79], v[64:67], a[184:187], v[76:79]\n"
"ds_read_b64_tr_b16 v[58:59], v7 offset:49664\n"
"ds_write_b64 v11, v[136:137] offset:1024\n"
"v_mfma_f32_16x16x32_bf16 v[80:83], v[64:67], a[188:191], v[80:83]\n"
"v_add_f32 v121, 1.0, v121\n"
"v_fma_f32 v16, v54, v48, v16\n"
"v_mfma_f32_16x16x32_bf16 v[84:87], v[64:67], a[192:195], v[84:87]\n"
"buffer_load_dwordx4 v1, s[48:51], s90 offen lds\n"
"v_rcp_f32 v121, v121\n"
"v_mfma_f32_16x16x32_bf16 v[88:91], v[64:67], a[196:199], v[88:91]\n"
"v_and_b32 v17, 0xffff0000, v35\n"
"v_mfma_f32_16x16x32_bf16 v[92:95], v[68:71], a[180:183], v[92:95]\n"
"ds_read_b64_tr_b16 v[60:61], v7 offset:50176\n"
"v_add_f32 v122, 1.0, v122\n"
"v_mfma_f32_16x16x32_bf16 v[96:99], v[68:71], a[184:187], v[96:99]\n"
"ds_read_b64_tr_b16 v[62:63], v7 offset:50688\n"
"v_rcp_f32 v122, v122\n"
"v_mfma_f32_16x16x32_bf16 v[100:103], v[68:71], a[188:191], v[100:103]\n"
"v_fma_f32 v54, v17, v50, -v51\n"
"v_mul_f32 v17, 0x40053526, v17\n"
"v_mfma_f32_16x16x32_bf16 v[104:107], v[68:71], a[192:195], v[104:107]\n"
"buffer_load_dwordx4 v1, s[52:55], s90 offen offset:1024 lds\n"
"v_add_f32 v123, 1.0, v123\n"
"v_mfma_f32_16x16x32_bf16 v[108:111], v[68:71], a[196:199], v[108:111]\n"
"v_rcp_f32 v123, v123\n"
"s_waitcnt lgkmcnt(3)\n"
"v_mfma_f32_16x16x32_bf16 v[72:75], v[56:59], a[200:203], v[72:75]\n"
"ds_read_b64_tr_b16 v[64:65], v7 offset:51200\n"
"v_fma_f32 v17, v54, v49, v17\n"
"v_mfma_f32_16x16x32_bf16 v[76:79], v[56:59], a[204:207], v[76:79]\n"
"ds_read_b64_tr_b16 v[66:67], v7 offset:51712\n"
"v_mul_f32 v120, v120, v140\n"
"v_mfma_f32_16x16x32_bf16 v[80:83], v[56:59], a[208:211], v[80:83]\n"
"buffer_load_dwordx4 v2, s[48:51], s90 offen offset:2048 lds\n"
"v_mul_f32 v121, v121, v141\n"
"v_mfma_f32_16x16x32_bf16 v[84:87], v[56:59], a[212:215], v[84:87]\n"
"ds_read_b64_tr_b16 v[68:69], v7 offset:52224\n"
"v_mul_f32 v122, v122, v142\n"
"v_mfma_f32_16x16x32_bf16 v[88:91], v[56:59], a[216:219], v[88:91]\n"
"ds_read_b64_tr_b16 v[70:71], v7 offset:52736\n"
"v_mul_f32 v123, v123, v143\n"
"s_waitcnt lgkmcnt(4)\n"
"v_mfma_f32_16x16x32_bf16 v[92:95], v[60:63], a[200:203], v[92:95]\n"
"v_cvt_pk_bf16_f32 v140, v120, v121\n"
"ds_read_b64 v[50:51], v13 offset:64\n"
"v_mfma_f32_16x16x32_bf16 v[96:99], v[60:63], a[204:207], v[96:99]\n"
"buffer_load_dwordx4 v2, s[52:55], s90 offen offset:3072 lds\n"
"v_cvt_pk_bf16_f32 v141, v122, v123\n"
"v_mfma_f32_16x16x32_bf16 v[100:103], v[60:63], a[208:211], v[100:103]\n"
"s_add_u32 m0, s97, 49152\n"
"v_mul_f32 v144, v124, v144\n"
"v_mfma_f32_16x16x32_bf16 v[104:107], v[60:63], a[212:215], v[104:107]\n"
"v_exp_f32_e64 v124, -v124\n"
"v_mfma_f32_16x16x32_bf16 v[108:111], v[60:63], a[216:219], v[108:111]\n"
"s_waitcnt lgkmcnt(1) vmcnt(8)\n"
"s_barrier\n"
"v_mfma_f32_16x16x32_bf16 v[72:75], v[64:67], a[220:223], v[72:75]\n"
"ds_read_b64_tr_b16 v[56:57], v7 offset:0\n"
"s_mov_b32 s90, s78\n"
"v_mfma_f32_16x16x32_bf16 v[76:79], v[64:67], a[224:227], v[76:79]\n"
"ds_read_b64_tr_b16 v[58:59], v7 offset:512\n"
"ds_write_b64 v11, v[140:141] offset:2048\n"
"v_mfma_f32_16x16x32_bf16 v[80:83], v[64:67], a[228:231], v[80:83]\n"
"v_mul_f32 v145, v125, v145\n"
"v_mfma_f32_16x16x32_bf16 v[84:87], v[64:67], a[232:235], v[84:87]\n"
"buffer_load_dwordx4 v1, s[48:51], s90 offen lds\n"
"v_exp_f32_e64 v125, -v125\n"
"v_mfma_f32_16x16x32_bf16 v[88:91], v[64:67], a[236:239], v[88:91]\n"
"v_mul_f32 v146, v126, v146\n"
"v_lshlrev_b32 v18, 16, v36\n"
"v_mfma_f32_16x16x32_bf16 v[92:95], v[68:71], a[220:223], v[92:95]\n"
"ds_read_b64_tr_b16 v[60:61], v7 offset:1024\n"
"v_exp_f32_e64 v126, -v126\n"
"v_mfma_f32_16x16x32_bf16 v[96:99], v[68:71], a[224:227], v[96:99]\n"
"ds_read_b64_tr_b16 v[62:63], v7 offset:1536\n"
"v_fma_f32 v54, v18, v52, -v53\n"
"v_mfma_f32_16x16x32_bf16 v[100:103], v[68:71], a[228:231], v[100:103]\n"
"v_mul_f32 v147, v127, v147\n"
"v_mul_f32 v18, 0x40053526, v18\n"
"v_mfma_f32_16x16x32_bf16 v[104:107], v[68:71], a[232:235], v[104:107]\n"
"buffer_load_dwordx4 v1, s[52:55], s90 offen offset:1024 lds\n"
"v_exp_f32_e64 v127, -v127\n"
"v_mfma_f32_16x16x32_bf16 v[108:111], v[68:71], a[236:239], v[108:111]\n"
"v_fma_f32 v18, v54, v46, v18\n"
"s_waitcnt lgkmcnt(3)\n"
"v_mfma_f32_16x16x32_bf16 v[72:75], v[56:59], a[240:243], v[72:75]\n"
"ds_read_b64_tr_b16 v[64:65], v7 offset:2048\n"
"v_add_f32 v124, 1.0, v124\n"
"v_mfma_f32_16x16x32_bf16 v[76:79], v[56:59], a[244:247], v[76:79]\n"
"ds_read_b64_tr_b16 v[66:67], v7 offset:2560\n"
"v_rcp_f32 v124, v124\n"
"v_mfma_f32_16x16x32_bf16 v[80:83], v[56:59], a[248:251], v[80:83]\n"
"buffer_load_dwordx4 v2, s[48:51], s90 offen offset:2048 lds\n"
"v_and_b32 v19, 0xffff0000, v36\n"
"v_mfma_f32_16x16x32_bf16 v[84:87], v[56:59], a[252:255], v[84:87]\n"
"ds_read_b64_tr_b16 v[68:69], v7 offset:3072\n"
"v_fma_f32 v54, v19, v52, -v53\n"
"v_mfma_f32_16x16x32_bf16 v[88:91], v[56:59], v[152:155], v[88:91]\n"
"ds_read_b64_tr_b16 v[70:71], v7 offset:3584\n"
"v_add_f32 v125, 1.0, v125\n"
"s_waitcnt lgkmcnt(4)\n"
"v_mfma_f32_16x16x32_bf16 v[92:95], v[60:63], a[240:243], v[92:95]\n"
"v_rcp_f32 v125, v125\n"
"v_mfma_f32_16x16x32_bf16 v[96:99], v[60:63], a[244:247], v[96:99]\n"
"buffer_load_dwordx4 v2, s[52:55], s90 offen offset:3072 lds\n"
"v_mul_f32 v19, 0x40053526, v19\n"
"v_mfma_f32_16x16x32_bf16 v[100:103], v[60:63], a[248:251], v[100:103]\n"
"s_add_u32 m0, s97, 0\n"
"v_add_f32 v126, 1.0, v126\n"
"v_mfma_f32_16x16x32_bf16 v[104:107], v[60:63], a[252:255], v[104:107]\n"
"v_rcp_f32 v126, v126\n"
"v_mfma_f32_16x16x32_bf16 v[108:111], v[60:63], v[152:155], v[108:111]\n"
"s_waitcnt lgkmcnt(0) vmcnt(8)\n"
"s_barrier\n"
"v_mfma_f32_16x16x32_bf16 v[72:75], v[64:67], v[156:159], v[72:75]\n"
"ds_read_b64_tr_b16 v[56:57], v7 offset:16384\n"
"s_add_u32 s90, s90, 589824\n"
"v_mfma_f32_16x16x32_bf16 v[76:79], v[64:67], v[160:163], v[76:79]\n"
"ds_read_b64_tr_b16 v[58:59], v7 offset:16896\n"
"v_fma_f32 v19, v54, v47, v19\n"
"v_mfma_f32_16x16x32_bf16 v[80:83], v[64:67], v[164:167], v[80:83]\n"
"v_add_f32 v127, 1.0, v127\n"
"v_rcp_f32 v127, v127\n"
"v_mfma_f32_16x16x32_bf16 v[84:87], v[64:67], v[168:171], v[84:87]\n"
"buffer_load_dwordx4 v1, s[48:51], s90 offen lds\n"
"v_mfma_f32_16x16x32_bf16 v[88:91], v[64:67], v[172:175], v[88:91]\n"
"v_mul_f32 v124, v124, v144\n"
"v_lshlrev_b32 v20, 16, v37\n"
"v_mfma_f32_16x16x32_bf16 v[92:95], v[68:71], v[156:159], v[92:95]\n"
"ds_read_b64_tr_b16 v[60:61], v7 offset:17408\n"
"v_mul_f32 v125, v125, v145\n"
"v_mfma_f32_16x16x32_bf16 v[96:99], v[68:71], v[160:163], v[96:99]\n"
"ds_read_b64_tr_b16 v[62:63], v7 offset:17920\n"
"v_mul_f32 v126, v126, v146\n"
"v_mfma_f32_16x16x32_bf16 v[100:103], v[68:71], v[164:167], v[100:103]\n"
"v_mul_f32 v127, v127, v147\n"
"v_fma_f32 v54, v20, v52, -v53\n"
"v_mfma_f32_16x16x32_bf16 v[104:107], v[68:71], v[168:171], v[104:107]\n"
"buffer_load_dwordx4 v1, s[52:55], s90 offen offset:1024 lds\n"
"v_cvt_pk_bf16_f32 v144, v124, v125\n"
"v_mfma_f32_16x16x32_bf16 v[108:111], v[68:71], v[172:175], v[108:111]\n"
"v_cvt_pk_bf16_f32 v145, v126, v127\n"
"v_mul_f32 v148, v128, v148\n"
"s_waitcnt lgkmcnt(2)\n"
"v_mfma_f32_16x16x32_bf16 v[72:75], v[56:59], v[176:179], v[72:75]\n"
"ds_read_b64_tr_b16 v[64:65], v7 offset:18432\n"
"v_exp_f32_e64 v128, -v128\n"
"v_mfma_f32_16x16x32_bf16 v[76:79], v[56:59], v[180:183], v[76:79]\n"
"ds_read_b64_tr_b16 v[66:67], v7 offset:18944\n"
"v_mul_f32 v20, 0x40053526, v20\n"
"v_mfma_f32_16x16x32_bf16 v[80:83], v[56:59], v[184:187], v[80:83]\n"
"buffer_load_dwordx4 v2, s[48:51], s90 offen offset:2048 lds\n"
"v_fma_f32 v20, v54, v48, v20\n"
"v_mfma_f32_16x16x32_bf16 v[84:87], v[56:59], v[188:191], v[84:87]\n"
"ds_read_b64_tr_b16 v[68:69], v7 offset:19456\n"
"v_mul_f32 v149, v129, v149\n"
"v_mfma_f32_16x16x32_bf16 v[88:91], v[56:59], v[192:195], v[88:91]\n"
"ds_read_b64_tr_b16 v[70:71], v7 offset:19968\n"
"v_exp_f32_e64 v129, -v129\n"
"s_waitcnt lgkmcnt(4)\n"
"v_mfma_f32_16x16x32_bf16 v[92:95], v[60:63], v[176:179], v[92:95]\n"
"v_and_b32 v21, 0xffff0000, v37\n"
"v_mfma_f32_16x16x32_bf16 v[96:99], v[60:63], v[180:183], v[96:99]\n"
"buffer_load_dwordx4 v2, s[52:55], s90 offen offset:3072 lds\n"
"v_mul_f32 v150, v130, v150\n"
"v_mfma_f32_16x16x32_bf16 v[100:103], v[60:63], v[184:187], v[100:103]\n"
"s_add_u32 m0, s97, 16384\n"
"v_exp_f32_e64 v130, -v130\n"
"v_mfma_f32_16x16x32_bf16 v[104:107], v[60:63], v[188:191], v[104:107]\n"
"v_fma_f32 v54, v21, v52, -v53\n"
"v_mul_f32 v21, 0x40053526, v21\n"
"v_mfma_f32_16x16x32_bf16 v[108:111], v[60:63], v[192:195], v[108:111]\n"
"s_waitcnt lgkmcnt(0) vmcnt(8)\n"
"s_barrier\n"
"v_mfma_f32_16x16x32_bf16 v[72:75], v[64:67], v[196:199], v[72:75]\n"
"ds_read_b64_tr_b16 v[56:57], v7 offset:32768\n"
"s_add_u32 s90, s90, 589824\n"
"v_mfma_f32_16x16x32_bf16 v[76:79], v[64:67], v[200:203], v[76:79]\n"
"ds_read_b64_tr_b16 v[58:59], v7 offset:33280\n"
"ds_write_b64 v11, v[144:145] offset:3072\n"
"v_mul_f32 v151, v131, v151\n"
"v_mfma_f32_16x16x32_bf16 v[80:83], v[64:67], v[204:207], v[80:83]\n"
"v_exp_f32_e64 v131, -v131\n"
"v_mfma_f32_16x16x32_bf16 v[84:87], v[64:67], v[208:211], v[84:87]\n"
"buffer_load_dwordx4 v1, s[48:51], s90 offen lds\n"
"v_fma_f32 v21, v54, v49, v21\n"
"v_mfma_f32_16x16x32_bf16 v[88:91], v[64:67], v[212:215], v[88:91]\n"
"v_add_f32 v128, 1.0, v128\n"
"v_rcp_f32 v128, v128\n"
"v_mfma_f32_16x16x32_bf16 v[92:95], v[68:71], v[196:199], v[92:95]\n"
"ds_read_b64_tr_b16 v[60:61], v7 offset:33792\n"
"v_mfma_f32_16x16x32_bf16 v[96:99], v[68:71], v[200:203], v[96:99]\n"
"ds_read_b64_tr_b16 v[62:63], v7 offset:34304\n"
"ds_read_b64 v[52:53], v13 offset:96\n"
"v_mfma_f32_16x16x32_bf16 v[100:103], v[68:71], v[204:207], v[100:103]\n"
"v_add_f32 v129, 1.0, v129\n"
"v_rcp_f32 v129, v129\n"
"v_mfma_f32_16x16x32_bf16 v[104:107], v[68:71], v[208:211], v[104:107]\n"
"buffer_load_dwordx4 v1, s[52:55], s90 offen offset:1024 lds\n"
"v_lshlrev_b32 v22, 16, v38\n"
"v_mfma_f32_16x16x32_bf16 v[108:111], v[68:71], v[212:215], v[108:111]\n"
"v_add_f32 v130, 1.0, v130\n"
"v_fma_f32 v54, v22, v50, -v51\n"
"s_waitcnt lgkmcnt(4)\n"
"v_mfma_f32_16x16x32_bf16 v[72:75], v[56:59], v[216:219], v[72:75]\n"
"ds_read_b64_tr_b16 v[64:65], v7 offset:34816\n"
"v_rcp_f32 v130, v130\n"
"v_mfma_f32_16x16x32_bf16 v[76:79], v[56:59], v[220:223], v[76:79]\n"
"ds_read_b64_tr_b16 v[66:67], v7 offset:35328\n"
"v_mul_f32 v22, 0x40053526, v22\n"
"v_mfma_f32_16x16x32_bf16 v[80:83], v[56:59], v[224:227], v[80:83]\n"
"buffer_load_dwordx4 v2, s[48:51], s90 offen offset:2048 lds\n"
"v_add_f32 v131, 1.0, v131\n"
"v_mfma_f32_16x16x32_bf16 v[84:87], v[56:59], v[228:231], v[84:87]\n"
"ds_read_b64_tr_b16 v[68:69], v7 offset:35840\n"
"v_mfma_f32_16x16x32_bf16 v[88:91], v[56:59], v[232:235], v[88:91]\n"
"ds_read_b64_tr_b16 v[70:71], v7 offset:36352\n"
"v_rcp_f32 v131, v131\n"
"s_waitcnt lgkmcnt(5)\n"
"v_mfma_f32_16x16x32_bf16 v[92:95], v[60:63], v[216:219], v[92:95]\n"
"v_mul_f32 v128, v128, v148\n"
"v_fma_f32 v22, v54, v46, v22\n"
"v_mfma_f32_16x16x32_bf16 v[96:99], v[60:63], v[220:223], v[96:99]\n"
"buffer_load_dwordx4 v2, s[52:55], s90 offen offset:3072 lds\n"
"v_and_b32 v23, 0xffff0000, v38\n"
"v_mfma_f32_16x16x32_bf16 v[100:103], v[60:63], v[224:227], v[100:103]\n"
"s_add_u32 m0, s97, 32768\n"
"v_mul_f32 v129, v129, v149\n"
"v_mfma_f32_16x16x32_bf16 v[104:107], v[60:63], v[228:231], v[104:107]\n"
"v_mul_f32 v130, v130, v150\n"
"v_mul_f32 v131, v131, v151\n"
"v_mfma_f32_16x16x32_bf16 v[108:111], v[60:63], v[232:235], v[108:111]\n"
"s_waitcnt lgkmcnt(0) vmcnt(8)\n"
"s_barrier\n"
"v_mfma_f32_16x16x32_bf16 v[72:75], v[64:67], v[236:239], v[72:75]\n"
"ds_read_b64_tr_b16 v[56:57], v7 offset:49152\n"
"s_add_u32 s90, s90, 589824\n"
"v_mfma_f32_16x16x32_bf16 v[76:79], v[64:67], v[240:243], v[76:79]\n"
"ds_read_b64_tr_b16 v[58:59], v7 offset:49664\n"
"v_cvt_pk_bf16_f32 v148, v128, v129\n"
"v_mfma_f32_16x16x32_bf16 v[80:83], v[64:67], v[244:247], v[80:83]\n"
"v_cvt_pk_bf16_f32 v149, v130, v131\n"
"v_fma_f32 v54, v23, v50, -v51\n"
"v_mfma_f32_16x16x32_bf16 v[84:87], v[64:67], v[248:251], v[84:87]\n"
"buffer_load_dwordx4 v1, s[48:51], s90 offen lds\n"
"v_mul_f32 v23, 0x40053526, v23\n"
"v_mfma_f32_16x16x32_bf16 v[88:91], v[64:67], v[252:255], v[88:91]\n"
"v_fma_f32 v23, v54, v47, v23\n"
"v_mfma_f32_16x16x32_bf16 v[92:95], v[68:71], v[236:239], v[92:95]\n"
"ds_read_b64_tr_b16 v[60:61], v7 offset:50176\n"
"v_lshlrev_b32 v24, 16, v39\n"
"v_mfma_f32_16x16x32_bf16 v[96:99], v[68:71], v[240:243], v[96:99]\n"
"ds_read_b64_tr_b16 v[62:63], v7 offset:50688\n"
"v_fma_f32 v54, v24, v50, -v51\n"
"v_mfma_f32_16x16x32_bf16 v[100:103], v[68:71], v[244:247], v[100:103]\n"
"ds_write_b64 v11, v[148:149] offset:4096\n"
"v_mul_f32 v24, 0x40053526, v24\n"
"v_mfma_f32_16x16x32_bf16 v[104:107], v[68:71], v[248:251], v[104:107]\n"
"buffer_load_dwordx4 v1, s[52:55], s90 offen offset:1024 lds\n"
"v_mfma_f32_16x16x32_bf16 v[108:111], v[68:71], v[252:255], v[108:111]\n"
"v_fma_f32 v24, v54, v48, v24\n"
"v_and_b32 v25, 0xffff0000, v39\n"
"s_waitcnt lgkmcnt(3)\n"
"v_mfma_f32_16x16x32_bf16 v[112:115], v[56:59], a[0:3], 0\n"
"ds_read_b64_tr_b16 v[64:65], v7 offset:51200\n"
"v_fma_f32 v54, v25, v50, -v51\n"
"v_mfma_f32_16x16x32_bf16 v[116:119], v[56:59], a[4:7], 0\n"
"ds_read_b64_tr_b16 v[66:67], v7 offset:51712\n"
"v_mul_f32 v25, 0x40053526, v25\n"
"v_mfma_f32_16x16x32_bf16 v[120:123], v[56:59], a[8:11], 0\n"
"buffer_load_dwordx4 v2, s[48:51], s90 offen offset:2048 lds\n"
"v_mfma_f32_16x16x32_bf16 v[124:127], v[56:59], a[12:15], 0\n"
"ds_read_b64_tr_b16 v[68:69], v7 offset:52224\n"
"v_fma_f32 v25, v54, v49, v25\n"
"v_mfma_f32_16x16x32_bf16 v[128:131], v[56:59], a[16:19], 0\n"
"ds_read_b64_tr_b16 v[70:71], v7 offset:52736\n"
"ds_read_b64 v[50:51], v13 offset:128\n"
"s_waitcnt lgkmcnt(6)\n"
"v_mfma_f32_16x16x32_bf16 v[132:135], v[60:63], a[0:3], 0\n"
"v_lshlrev_b32 v26, 16, v40\n"
"v_fma_f32 v54, v26, v52, -v53\n"
"v_mfma_f32_16x16x32_bf16 v[136:139], v[60:63], a[4:7], 0\n"
"buffer_load_dwordx4 v2, s[52:55], s90 offen offset:3072 lds\n"
"v_mfma_f32_16x16x32_bf16 v[140:143], v[60:63], a[8:11], 0\n"
"s_add_u32 m0, s97, 49152\n"
"v_mul_f32 v26, 0x40053526, v26\n"
"v_mfma_f32_16x16x32_bf16 v[144:147], v[60:63], a[12:15], 0\n"
"v_fma_f32 v26, v54, v46, v26\n"
"v_and_b32 v27, 0xffff0000, v40\n"
"v_mfma_f32_16x16x32_bf16 v[148:151], v[60:63], a[16:19], 0\n"
"s_waitcnt lgkmcnt(1) vmcnt(8)\n"
"s_barrier\n"
"v_mfma_f32_16x16x32_bf16 v[112:115], v[64:67], a[20:23], v[112:115]\n"
"ds_read_b64_tr_b16 v[56:57], v7 offset:0\n"
"s_add_u32 s90, s90, 589824\n"
"v_mfma_f32_16x16x32_bf16 v[116:119], v[64:67], a[24:27], v[116:119]\n"
"ds_read_b64_tr_b16 v[58:59], v7 offset:512\n"
"v_mul_f32 v92, v72, v92\n"
"v_mfma_f32_16x16x32_bf16 v[120:123], v[64:67], a[28:31], v[120:123]\n"
"v_exp_f32_e64 v72, -v72\n"
"v_mfma_f32_16x16x32_bf16 v[124:127], v[64:67], a[32:35], v[124:127]\n"
"buffer_load_dwordx4 v1, s[48:51], s90 offen lds\n"
"v_mul_f32 v93, v73, v93\n"
"v_mfma_f32_16x16x32_bf16 v[128:131], v[64:67], a[36:39], v[128:131]\n"
"v_exp_f32_e64 v73, -v73\n"
"v_mfma_f32_16x16x32_bf16 v[132:135], v[68:71], a[20:23], v[132:135]\n"
"ds_read_b64_tr_b16 v[60:61], v7 offset:1024\n"
"v_fma_f32 v54, v27, v52, -v53\n"
"v_mfma_f32_16x16x32_bf16 v[136:139], v[68:71], a[24:27], v[136:139]\n"
"ds_read_b64_tr_b16 v[62:63], v7 offset:1536\n"
"v_mul_f32 v94, v74, v94\n"
"v_mfma_f32_16x16x32_bf16 v[140:143], v[68:71], a[28:31], v[140:143]\n"
"v_exp_f32_e64 v74, -v74\n"
"v_mfma_f32_16x16x32_bf16 v[144:147], v[68:71], a[32:35], v[144:147]\n"
"buffer_load_dwordx4 v1, s[52:55], s90 offen offset:1024 lds\n"
"v_mfma_f32_16x16x32_bf16 v[148:151], v[68:71], a[36:39], v[148:151]\n"
"v_mul_f32 v95, v75, v95\n"
"v_mul_f32 v27, 0x40053526, v27\n"
"s_waitcnt lgkmcnt(2)\n"
"v_mfma_f32_16x16x32_bf16 v[112:115], v[56:59], a[40:43], v[112:115]\n"
"ds_read_b64_tr_b16 v[64:65], v7 offset:2048\n"
"v_exp_f32_e64 v75, -v75\n"
"v_mfma_f32_16x16x32_bf16 v[116:119], v[56:59], a[44:47], v[116:119]\n"
"ds_read_b64_tr_b16 v[66:67], v7 offset:2560\n"
"v_fma_f32 v27, v54, v47, v27\n"
"v_mfma_f32_16x16x32_bf16 v[120:123], v[56:59], a[48:51], v[120:123]\n"
"buffer_load_dwordx4 v2, s[48:51], s90 offen offset:2048 lds\n"
"v_add_f32 v72, 1.0, v72\n"
"v_mfma_f32_16x16x32_bf16 v[124:127], v[56:59], a[52:55], v[124:127]\n"
"ds_read_b64_tr_b16 v[68:69], v7 offset:3072\n"
"v_rcp_f32 v72, v72\n"
"v_mfma_f32_16x16x32_bf16 v[128:131], v[56:59], a[56:59], v[128:131]\n"
"ds_read_b64_tr_b16 v[70:71], v7 offset:3584\n"
"v_lshlrev_b32 v28, 16, v41\n"
"s_waitcnt lgkmcnt(4)\n"
"v_mfma_f32_16x16x32_bf16 v[132:135], v[60:63], a[40:43], v[132:135]\n"
"v_add_f32 v73, 1.0, v73\n"
"v_fma_f32 v54, v28, v52, -v53\n"
"v_mfma_f32_16x16x32_bf16 v[136:139], v[60:63], a[44:47], v[136:139]\n"
"buffer_load_dwordx4 v2, s[52:55], s90 offen offset:3072 lds\n"
"v_rcp_f32 v73, v73\n"
"v_mfma_f32_16x16x32_bf16 v[140:143], v[60:63], a[48:51], v[140:143]\n"
"s_add_u32 m0, s97, 0\n"
"v_mfma_f32_16x16x32_bf16 v[144:147], v[60:63], a[52:55], v[144:147]\n"
"v_add_f32 v74, 1.0, v74\n"
"v_mul_f32 v28, 0x40053526, v28\n"
"v_mfma_f32_16x16x32_bf16 v[148:151], v[60:63], a[56:59], v[148:151]\n"
"s_waitcnt lgkmcnt(0) vmcnt(8)\n"
"s_barrier\n"
"v_mfma_f32_16x16x32_bf16 v[112:115], v[64:67], a[60:63], v[112:115]\n"
"ds_read_b64_tr_b16 v[56:57], v7 offset:16384\n"
"s_add_u32 s90, s90, 589824\n"
"v_mfma_f32_16x16x32_bf16 v[116:119], v[64:67], a[64:67], v[116:119]\n"
"ds_read_b64_tr_b16 v[58:59], v7 offset:16896\n"
"v_rcp_f32 v74, v74\n"
"v_mfma_f32_16x16x32_bf16 v[120:123], v[64:67], a[68:71], v[120:123]\n"
"v_fma_f32 v28, v54, v48, v28\n"
"v_and_b32 v29, 0xffff0000, v41\n"
"v_mfma_f32_16x16x32_bf16 v[124:127], v[64:67], a[72:75], v[124:127]\n"
"buffer_load_dwordx4 v1, s[48:51], s90 offen lds\n"
"v_add_f32 v75, 1.0, v75\n"
"v_mfma_f32_16x16x32_bf16 v[128:131], v[64:67], a[76:79], v[128:131]\n"
"v_rcp_f32 v75, v75\n"
"v_mfma_f32_16x16x32_bf16 v[132:135], v[68:71], a[60:63], v[132:135]\n"
"ds_read_b64_tr_b16 v[60:61], v7 offset:17408\n"
"v_fma_f32 v54, v29, v52, -v53\n"
"v_mfma_f32_16x16x32_bf16 v[136:139], v[68:71], a[64:67], v[136:139]\n"
"ds_read_b64_tr_b16 v[62:63], v7 offset:17920\n"
"v_mul_f32 v72, v72, v92\n"
"v_mfma_f32_16x16x32_bf16 v[140:143], v[68:71], a[68:71], v[140:143]\n"
"v_mul_f32 v73, v73, v93\n"
"v_mul_f32 v74, v74, v94\n"
"v_mfma_f32_16x16x32_bf16 v[144:147], v[68:71], a[72:75], v[144:147]\n"
"buffer_load_dwordx4 v1, s[52:55], s90 offen offset:1024 lds\n"
"v_mfma_f32_16x16x32_bf16 v[148:151], v[68:71], a[76:79], v[148:151]\n"
"v_mul_f32 v75, v75, v95\n"
"v_cvt_pk_bf16_f32 v92, v72, v73\n"
"s_waitcnt lgkmcnt(2)\n"
"v_mfma_f32_16x16x32_bf16 v[112:115], v[56:59], a[80:83], v[112:115]\n"
"ds_read_b64_tr_b16 v[64:65], v7 offset:18432\n"
"v_cvt_pk_bf16_f32 v93, v74, v75\n"
"v_mfma_f32_16x16x32_bf16 v[116:119], v[56:59], a[84:87], v[116:119]\n"
"ds_read_b64_tr_b16 v[66:67], v7 offset:18944\n"
"v_mul_f32 v29, 0x40053526, v29\n"
"v_mfma_f32_16x16x32_bf16 v[120:123], v[56:59], a[88:91], v[120:123]\n"
"buffer_load_dwordx4 v2, s[48:51], s90 offen offset:2048 lds\n"
"v_mul_f32 v96, v76, v96\n"
"v_mfma_f32_16x16x32_bf16 v[124:127], v[56:59], a[92:95], v[124:127]\n"
"ds_read_b64_tr_b16 v[68:69], v7 offset:19456\n"
"v_exp_f32_e64 v76, -v76\n"
"v_mfma_f32_16x16x32_bf16 v[128:131], v[56:59], a[96:99], v[128:131]\n"
"ds_read_b64_tr_b16 v[70:71], v7 offset:19968\n"
"v_fma_f32 v29, v54, v49, v29\n"
"s_waitcnt lgkmcnt(4)\n"
"v_mfma_f32_16x16x32_bf16 v[132:135], v[60:63], a[80:83], v[132:135]\n"
"v_mul_f32 v97, v77, v97\n"
"v_lshlrev_b32 v30, 16, v42\n"
"v_mfma_f32_16x16x32_bf16 v[136:139], v[60:63], a[84:87], v[136:139]\n"
"buffer_load_dwordx4 v2, s[52:55], s90 offen offset:3072 lds\n"
"v_exp_f32_e64 v77, -v77\n"
"v_mfma_f32_16x16x32_bf16 v[140:143], v[60:63], a[88:91], v[140:143]\n"
"s_add_u32 m0, s97, 16384\n"
"v_fma_f32 v54, v30, v50, -v51\n"
"v_mfma_f32_16x16x32_bf16 v[144:147], v[60:63], a[92:95], v[144:147]\n"
"v_mul_f32 v98, v78, v98\n"
"v_mfma_f32_16x16x32_bf16 v[148:151], v[60:63], a[96:99], v[148:151]\n"
"s_waitcnt lgkmcnt(0) vmcnt(8)\n"
"s_barrier\n"
"v_mfma_f32_16x16x32_bf16 v[112:115], v[64:67], a[100:103], v[112:115]\n"
"ds_read_b64_tr_b16 v[56:57], v7 offset:32768\n"
"s_add_u32 s90, s90, 589824\n"
"v_mfma_f32_16x16x32_bf16 v[116:119], v[64:67], a[104:107], v[116:119]\n"
"ds_read_b64_tr_b16 v[58:59], v7 offset:33280\n"
"ds_write_b64 v11, v[92:93] offset:10240\n"
"v_exp_f32_e64 v78, -v78\n"
"v_mfma_f32_16x16x32_bf16 v[120:123], v[64:67], a[108:111], v[120:123]\n"
"v_mul_f32 v99, v79, v99\n"
"v_mul_f32 v30, 0x40053526, v30\n"
"v_mfma_f32_16x16x32_bf16 v[124:127], v[64:67], a[112:115], v[124:127]\n"
"buffer_load_dwordx4 v1, s[48:51], s90 offen lds\n"
"v_fma_f32 v30, v54, v46, v30\n"
"v_mfma_f32_16x16x32_bf16 v[128:131], v[64:67], a[116:119], v[128:131]\n"
"v_exp_f32_e64 v79, -v79\n"
"v_mfma_f32_16x16x32_bf16 v[132:135], v[68:71], a[100:103], v[132:135]\n"
"ds_read_b64_tr_b16 v[60:61], v7 offset:33792\n"
"v_add_f32 v76, 1.0, v76\n"
"v_mfma_f32_16x16x32_bf16 v[136:139], v[68:71], a[104:107], v[136:139]\n"
"ds_read_b64_tr_b16 v[62:63], v7 offset:34304\n"
"v_and_b32 v31, 0xffff0000, v42\n"
"v_mfma_f32_16x16x32_bf16 v[140:143], v[68:71], a[108:111], v[140:143]\n"
"v_rcp_f32 v76, v76\n"
"v_mfma_f32_16x16x32_bf16 v[144:147], v[68:71], a[112:115], v[144:147]\n"
"buffer_load_dwordx4 v1, s[52:55], s90 offen offset:1024 lds\n"
"v_add_f32 v77, 1.0, v77\n"
"v_mfma_f32_16x16x32_bf16 v[148:151], v[68:71], a[116:119], v[148:151]\n"
"v_rcp_f32 v77, v77\n"
"s_waitcnt lgkmcnt(3)\n"
"v_mfma_f32_16x16x32_bf16 v[112:115], v[56:59], a[120:123], v[112:115]\n"
"ds_read_b64_tr_b16 v[64:65], v7 offset:34816\n"
"v_fma_f32 v54, v31, v50, -v51\n"
"v_mfma_f32_16x16x32_bf16 v[116:119], v[56:59], a[124:127], v[116:119]\n"
"ds_read_b64_tr_b16 v[66:67], v7 offset:35328\n"
"v_add_f32 v78, 1.0, v78\n"
"v_mfma_f32_16x16x32_bf16 v[120:123], v[56:59], a[128:131], v[120:123]\n"
"buffer_load_dwordx4 v2, s[48:51], s90 offen offset:2048 lds\n"
"v_rcp_f32 v78, v78\n"
"v_mfma_f32_16x16x32_bf16 v[124:127], v[56:59], a[132:135], v[124:127]\n"
"ds_read_b64_tr_b16 v[68:69], v7 offset:35840\n"
"v_mfma_f32_16x16x32_bf16 v[128:131], v[56:59], a[136:139], v[128:131]\n"
"ds_read_b64_tr_b16 v[70:71], v7 offset:36352\n"
"v_mul_f32 v31, 0x40053526, v31\n"
"s_waitcnt lgkmcnt(4)\n"
"v_mfma_f32_16x16x32_bf16 v[132:135], v[60:63], a[120:123], v[132:135]\n"
"v_add_f32 v79, 1.0, v79\n"
"v_rcp_f32 v79, v79\n"
"v_mfma_f32_16x16x32_bf16 v[136:139], v[60:63], a[124:127], v[136:139]\n"
"buffer_load_dwordx4 v2, s[52:55], s90 offen offset:3072 lds\n"
"v_fma_f32 v31, v54, v47, v31\n"
"v_mfma_f32_16x16x32_bf16 v[140:143], v[60:63], a[128:131], v[140:143]\n"
"s_add_u32 m0, s97, 32768\n"
"v_lshlrev_b32 v32, 16, v43\n"
"v_mfma_f32_16x16x32_bf16 v[144:147], v[60:63], a[132:135], v[144:147]\n"
"v_mul_f32 v76, v76, v96\n"
"v_mul_f32 v77, v77, v97\n"
"v_mfma_f32_16x16x32_bf16 v[148:151], v[60:63], a[136:139], v[148:151]\n"
"s_waitcnt lgkmcnt(0) vmcnt(8)\n"
"s_barrier\n"
"v_mfma_f32_16x16x32_bf16 v[112:115], v[64:67], a[140:143], v[112:115]\n"
"ds_read_b64_tr_b16 v[56:57], v7 offset:49152\n"
"s_add_u32 s90, s90, 589824\n"
"v_mfma_f32_16x16x32_bf16 v[116:119], v[64:67], a[144:147], v[116:119]\n"
"ds_read_b64_tr_b16 v[58:59], v7 offset:49664\n"
"v_mul_f32 v78, v78, v98\n"
"v_mfma_f32_16x16x32_bf16 v[120:123], v[64:67], a[148:151], v[120:123]\n"
"v_mul_f32 v79, v79, v99\n"
"v_fma_f32 v54, v32, v50, -v51\n"
"v_mfma_f32_16x16x32_bf16 v[124:127], v[64:67], a[152:155], v[124:127]\n"
"buffer_load_dwordx4 v1, s[48:51], s90 offen lds\n"
"v_cvt_pk_bf16_f32 v96, v76, v77\n"
"v_mfma_f32_16x16x32_bf16 v[128:131], v[64:67], a[156:159], v[128:131]\n"
"v_cvt_pk_bf16_f32 v97, v78, v79\n"
"v_mfma_f32_16x16x32_bf16 v[132:135], v[68:71], a[140:143], v[132:135]\n"
"ds_read_b64_tr_b16 v[60:61], v7 offset:50176\n"
"v_mul_f32 v100, v80, v100\n"
"v_mfma_f32_16x16x32_bf16 v[136:139], v[68:71], a[144:147], v[136:139]\n"
"ds_read_b64_tr_b16 v[62:63], v7 offset:50688\n"
"v_exp_f32_e64 v80, -v80\n"
"v_mfma_f32_16x16x32_bf16 v[140:143], v[68:71], a[148:151], v[140:143]\n"
"v_mul_f32 v32, 0x40053526, v32\n"
"v_fma_f32 v32, v54, v48, v32\n"
"v_mfma_f32_16x16x32_bf16 v[144:147], v[68:71], a[152:155], v[144:147]\n"
"buffer_load_dwordx4 v1, s[52:55], s90 offen offset:1024 lds\n"
"v_mul_f32 v101, v81, v101\n"
"v_mfma_f32_16x16x32_bf16 v[148:151], v[68:71], a[156:159], v[148:151]\n"
"v_exp_f32_e64 v81, -v81\n"
"s_waitcnt lgkmcnt(2)\n"
"v_mfma_f32_16x16x32_bf16 v[112:115], v[56:59], a[160:163], v[112:115]\n"
"ds_read_b64_tr_b16 v[64:65], v7 offset:51200\n"
"v_and_b32 v33, 0xffff0000, v43\n"
"v_mfma_f32_16x16x32_bf16 v[116:119], v[56:59], a[164:167], v[116:119]\n"
"ds_read_b64_tr_b16 v[66:67], v7 offset:51712\n"
"v_mul_f32 v102, v82, v102\n"
"v_mfma_f32_16x16x32_bf16 v[120:123], v[56:59], a[168:171], v[120:123]\n"
"buffer_load_dwordx4 v2, s[48:51], s90 offen offset:2048 lds\n"
"v_exp_f32_e64 v82, -v82\n"
"v_mfma_f32_16x16x32_bf16 v[124:127], v[56:59], a[172:175], v[124:127]\n"
"ds_read_b64_tr_b16 v[68:69], v7 offset:52224\n"
"v_fma_f32 v54, v33, v50, -v51\n"
"v_mfma_f32_16x16x32_bf16 v[128:131], v[56:59], a[176:179], v[128:131]\n"
"ds_read_b64_tr_b16 v[70:71], v7 offset:52736\n"
"v_mul_f32 v103, v83, v103\n"
"s_waitcnt lgkmcnt(4)\n"
"v_mfma_f32_16x16x32_bf16 v[132:135], v[60:63], a[160:163], v[132:135]\n"
"v_exp_f32_e64 v83, -v83\n"
"v_mfma_f32_16x16x32_bf16 v[136:139], v[60:63], a[164:167], v[136:139]\n"
"buffer_load_dwordx4 v2, s[52:55], s90 offen offset:3072 lds\n"
"v_mfma_f32_16x16x32_bf16 v[140:143], v[60:63], a[168:171], v[140:143]\n"
"s_add_u32 m0, s97, 49152\n"
"v_add_f32 v80, 1.0, v80\n"
"v_mfma_f32_16x16x32_bf16 v[144:147], v[60:63], a[172:175], v[144:147]\n"
"v_rcp_f32 v80, v80\n"
"v_mfma_f32_16x16x32_bf16 v[148:151], v[60:63], a[176:179], v[148:151]\n"
"s_waitcnt lgkmcnt(0) vmcnt(8)\n"
"s_barrier\n"
"v_mfma_f32_16x16x32_bf16 v[112:115], v[64:67], a[180:183], v[112:115]\n"
"ds_read_b64_tr_b16 v[56:57], v7 offset:0\n"
"s_add_u32 s90, s90, 589824\n"
"v_mfma_f32_16x16x32_bf16 v[116:119], v[64:67], a[184:187], v[116:119]\n"
"ds_read_b64_tr_b16 v[58:59], v7 offset:512\n"
"ds_write_b64 v11, v[96:97] offset:11264\n"
"v_mfma_f32_16x16x32_bf16 v[120:123], v[64:67], a[188:191], v[120:123]\n"
"v_add_f32 v81, 1.0, v81\n"
"v_mul_f32 v33, 0x40053526, v33\n"
"v_mfma_f32_16x16x32_bf16 v[124:127], v[64:67], a[192:195], v[124:127]\n"
"buffer_load_dwordx4 v1, s[48:51], s90 offen lds\n"
"v_rcp_f32 v81, v81\n"
"v_mfma_f32_16x16x32_bf16 v[128:131], v[64:67], a[196:199], v[128:131]\n"
"v_add_f32 v82, 1.0, v82\n"
"v_fma_f32 v33, v54, v49, v33\n"
"v_mfma_f32_16x16x32_bf16 v[132:135], v[68:71], a[180:183], v[132:135]\n"
"ds_read_b64_tr_b16 v[60:61], v7 offset:1024\n"
"v_rcp_f32 v82, v82\n"
"v_mfma_f32_16x16x32_bf16 v[136:139], v[68:71], a[184:187], v[136:139]\n"
"ds_read_b64_tr_b16 v[62:63], v7 offset:1536\n"
"v_lshlrev_b32 v54, 1, v55\n"
"v_mfma_f32_16x16x32_bf16 v[140:143], v[68:71], a[188:191], v[140:143]\n"
"v_add_f32 v83, 1.0, v83\n"
"v_mov_b32 v46, s88\n"
"v_mfma_f32_16x16x32_bf16 v[144:147], v[68:71], a[192:195], v[144:147]\n"
"buffer_load_dwordx4 v1, s[52:55], s90 offen offset:1024 lds\n"
"v_rcp_f32 v83, v83\n"
"v_mfma_f32_16x16x32_bf16 v[148:151], v[68:71], a[196:199], v[148:151]\n"
"v_mov_b32 v47, s88\n"
"s_waitcnt lgkmcnt(3)\n"
"v_mfma_f32_16x16x32_bf16 v[112:115], v[56:59], a[200:203], v[112:115]\n"
"ds_read_b64_tr_b16 v[64:65], v7 offset:2048\n"
"v_mul_f32 v80, v80, v100\n"
"v_mfma_f32_16x16x32_bf16 v[116:119], v[56:59], a[204:207], v[116:119]\n"
"ds_read_b64_tr_b16 v[66:67], v7 offset:2560\n"
"v_mul_f32 v81, v81, v101\n"
"v_mfma_f32_16x16x32_bf16 v[120:123], v[56:59], a[208:211], v[120:123]\n"
"buffer_load_dwordx4 v2, s[48:51], s90 offen offset:2048 lds\n"
"s_mul_i32 s98, s95, 2560\n"
"v_mfma_f32_16x16x32_bf16 v[124:127], v[56:59], a[212:215], v[124:127]\n"
"ds_read_b64_tr_b16 v[68:69], v7 offset:3072\n"
"v_mul_f32 v82, v82, v102\n"
"v_mfma_f32_16x16x32_bf16 v[128:131], v[56:59], a[216:219], v[128:131]\n"
"ds_read_b64_tr_b16 v[70:71], v7 offset:3584\n"
"v_mul_f32 v83, v83, v103\n"
"s_waitcnt lgkmcnt(4)\n"
"v_mfma_f32_16x16x32_bf16 v[132:135], v[60:63], a[200:203], v[132:135]\n"
"v_cvt_pk_bf16_f32 v100, v80, v81\n"
"v_cvt_pk_bf16_f32 v101, v82, v83\n"
"v_mfma_f32_16x16x32_bf16 v[136:139], v[60:63], a[204:207], v[136:139]\n"
"buffer_load_dwordx4 v2, s[52:55], s90 offen offset:3072 lds\n"
"v_lshlrev_b32 v48, 4, v0\n"
"v_mfma_f32_16x16x32_bf16 v[140:143], v[60:63], a[208:211], v[140:143]\n"
"s_add_u32 m0, s97, 0\n"
"v_mul_f32 v104, v84, v104\n"
"v_mfma_f32_16x16x32_bf16 v[144:147], v[60:63], a[212:215], v[144:147]\n"
"v_exp_f32_e64 v84, -v84\n"
"v_mfma_f32_16x16x32_bf16 v[148:151], v[60:63], a[216:219], v[148:151]\n"
"s_waitcnt lgkmcnt(0) vmcnt(8)\n"
"s_barrier\n"
"v_mfma_f32_16x16x32_bf16 v[112:115], v[64:67], a[220:223], v[112:115]\n"
"ds_read_b64_tr_b16 v[56:57], v7 offset:16384\n"
"s_mov_b32 s90, s79\n"
"v_mfma_f32_16x16x32_bf16 v[116:119], v[64:67], a[224:227], v[116:119]\n"
"ds_read_b64_tr_b16 v[58:59], v7 offset:16896\n"
"ds_write_b64 v11, v[100:101] offset:12288\n"
"v_mfma_f32_16x16x32_bf16 v[120:123], v[64:67], a[228:231], v[120:123]\n"
"v_mul_f32 v105, v85, v105\n"
"v_exp_f32_e64 v85, -v85\n"
"v_mfma_f32_16x16x32_bf16 v[124:127], v[64:67], a[232:235], v[124:127]\n"
"buffer_load_dwordx4 v1, s[48:51], s90 offen lds\n"
"v_lshlrev_b32 v49, 3, v0\n"
"v_mfma_f32_16x16x32_bf16 v[128:131], v[64:67], a[236:239], v[128:131]\n"
"v_mul_f32 v106, v86, v106\n"
"v_mfma_f32_16x16x32_bf16 v[132:135], v[68:71], a[220:223], v[132:135]\n"
"ds_read_b64_tr_b16 v[60:61], v7 offset:17408\n"
"v_exp_f32_e64 v86, -v86\n"
"v_mfma_f32_16x16x32_bf16 v[136:139], v[68:71], a[224:227], v[136:139]\n"
"ds_read_b64_tr_b16 v[62:63], v7 offset:17920\n"
"v_add_u32 v48, s98, v48\n"
"v_mfma_f32_16x16x32_bf16 v[140:143], v[68:71], a[228:231], v[140:143]\n"
"v_mul_f32 v107, v87, v107\n"
"s_add_u32 s98, s98, 2048\n"
"v_mfma_f32_16x16x32_bf16 v[144:147], v[68:71], a[232:235], v[144:147]\n"
"buffer_load_dwordx4 v1, s[52:55], s90 offen offset:1024 lds\n"
"v_exp_f32_e64 v87, -v87\n"
"v_mfma_f32_16x16x32_bf16 v[148:151], v[68:71], a[236:239], v[148:151]\n"
"v_add_f32 v84, 1.0, v84\n"
"v_add_u32 v49, s98, v49\n"
"s_waitcnt lgkmcnt(3)\n"
"v_mfma_f32_16x16x32_bf16 v[112:115], v[56:59], a[240:243], v[112:115]\n"
"ds_read_b64_tr_b16 v[64:65], v7 offset:18432\n"
"v_bfe_u32 v50, v0, 2, 2\n"
"v_mfma_f32_16x16x32_bf16 v[116:119], v[56:59], a[244:247], v[116:119]\n"
"ds_read_b64_tr_b16 v[66:67], v7 offset:18944\n"
"v_rcp_f32 v84, v84\n"
"v_mfma_f32_16x16x32_bf16 v[120:123], v[56:59], a[248:251], v[120:123]\n"
"buffer_load_dwordx4 v2, s[48:51], s90 offen offset:2048 lds\n"
"v_mfma_f32_16x16x32_bf16 v[124:127], v[56:59], a[252:255], v[124:127]\n"
"ds_read_b64_tr_b16 v[68:69], v7 offset:19456\n"
"v_add_f32 v85, 1.0, v85\n"
"v_mfma_f32_16x16x32_bf16 v[128:131], v[56:59], v[152:155], v[128:131]\n"
"ds_read_b64_tr_b16 v[70:71], v7 offset:19968\n"
"v_lshrrev_b32 v51, 4, v0\n"
"s_waitcnt lgkmcnt(4)\n"
"v_mfma_f32_16x16x32_bf16 v[132:135], v[60:63], a[240:243], v[132:135]\n"
"v_rcp_f32 v85, v85\n"
"v_mfma_f32_16x16x32_bf16 v[136:139], v[60:63], a[244:247], v[136:139]\n"
"buffer_load_dwordx4 v2, s[52:55], s90 offen offset:3072 lds\n"
"v_add_f32 v86, 1.0, v86\n"
"v_mfma_f32_16x16x32_bf16 v[140:143], v[60:63], a[248:251], v[140:143]\n"
"s_add_u32 m0, s97, 16384\n"
"v_and_b32 v52, 1, v51\n"
"v_mfma_f32_16x16x32_bf16 v[144:147], v[60:63], a[252:255], v[144:147]\n"
"v_rcp_f32 v86, v86\n"
"v_mfma_f32_16x16x32_bf16 v[148:151], v[60:63], v[152:155], v[148:151]\n"
"s_waitcnt lgkmcnt(0) vmcnt(8)\n"
"s_barrier\n"
"v_mfma_f32_16x16x32_bf16 v[112:115], v[64:67], v[156:159], v[112:115]\n"
"ds_read_b64_tr_b16 v[56:57], v7 offset:32768\n"
"s_add_u32 s90, s90, 589824\n"
"v_mfma_f32_16x16x32_bf16 v[116:119], v[64:67], v[160:163], v[116:119]\n"
"ds_read_b64_tr_b16 v[58:59], v7 offset:33280\n"
"v_add_f32 v87, 1.0, v87\n"
"v_mfma_f32_16x16x32_bf16 v[120:123], v[64:67], v[164:167], v[120:123]\n"
"v_rcp_f32 v87, v87\n"
"v_mfma_f32_16x16x32_bf16 v[124:127], v[64:67], v[168:171], v[124:127]\n"
"buffer_load_dwordx4 v1, s[48:51], s90 offen lds\n"
"v_lshlrev_b32 v52, 2, v52\n"
"v_mfma_f32_16x16x32_bf16 v[128:131], v[64:67], v[172:175], v[128:131]\n"
"v_mul_f32 v84, v84, v104\n"
"v_mul_f32 v85, v85, v105\n"
"v_mfma_f32_16x16x32_bf16 v[132:135], v[68:71], v[156:159], v[132:135]\n"
"ds_read_b64_tr_b16 v[60:61], v7 offset:33792\n"
"v_xor_b32 v52, v52, v50\n"
"v_mfma_f32_16x16x32_bf16 v[136:139], v[68:71], v[160:163], v[136:139]\n"
"ds_read_b64_tr_b16 v[62:63], v7 offset:34304\n"
"v_mul_f32 v86, v86, v106\n"
"v_mfma_f32_16x16x32_bf16 v[140:143], v[68:71], v[164:167], v[140:143]\n"
"v_mul_f32 v87, v87, v107\n"
"v_cvt_pk_bf16_f32 v104, v84, v85\n"
"v_mfma_f32_16x16x32_bf16 v[144:147], v[68:71], v[168:171], v[144:147]\n"
"buffer_load_dwordx4 v1, s[52:55], s90 offen offset:1024 lds\n"
"v_mfma_f32_16x16x32_bf16 v[148:151], v[68:71], v[172:175], v[148:151]\n"
"v_cvt_pk_bf16_f32 v105, v86, v87\n"
"v_mul_f32 v108, v88, v108\n"
"s_waitcnt lgkmcnt(2)\n"
"v_mfma_f32_16x16x32_bf16 v[112:115], v[56:59], v[176:179], v[112:115]\n"
"ds_read_b64_tr_b16 v[64:65], v7 offset:34816\n"
"v_exp_f32_e64 v88, -v88\n"
"v_mfma_f32_16x16x32_bf16 v[116:119], v[56:59], v[180:183], v[116:119]\n"
"ds_read_b64_tr_b16 v[66:67], v7 offset:35328\n"
"v_lshl_add_u32 v52, v51, 3, v52\n"
"v_mfma_f32_16x16x32_bf16 v[120:123], v[56:59], v[184:187], v[120:123]\n"
"buffer_load_dwordx4 v2, s[48:51], s90 offen offset:2048 lds\n"
"v_and_b32 v50, 3, v0\n"
"v_mfma_f32_16x16x32_bf16 v[124:127], v[56:59], v[188:191], v[124:127]\n"
"ds_read_b64_tr_b16 v[68:69], v7 offset:35840\n"
"v_mul_f32 v109, v89, v109\n"
"v_mfma_f32_16x16x32_bf16 v[128:131], v[56:59], v[192:195], v[128:131]\n"
"ds_read_b64_tr_b16 v[70:71], v7 offset:36352\n"
"v_exp_f32_e64 v89, -v89\n"
"s_waitcnt lgkmcnt(4)\n"
"v_mfma_f32_16x16x32_bf16 v[132:135], v[60:63], v[176:179], v[132:135]\n"
"v_bfe_u32 v51, v52, 2, 2\n"
"v_xor_b32 v50, v50, v51\n"
"v_mfma_f32_16x16x32_bf16 v[136:139], v[60:63], v[180:183], v[136:139]\n"
"buffer_load_dwordx4 v2, s[52:55], s90 offen offset:3072 lds\n"
"v_mul_f32 v110, v90, v110\n"
"v_mfma_f32_16x16x32_bf16 v[140:143], v[60:63], v[184:187], v[140:143]\n"
"s_add_u32 m0, s97, 32768\n"
"v_exp_f32_e64 v90, -v90\n"
"v_mfma_f32_16x16x32_bf16 v[144:147], v[60:63], v[188:191], v[144:147]\n"
"v_lshlrev_b32 v50, 3, v50\n"
"v_mfma_f32_16x16x32_bf16 v[148:151], v[60:63], v[192:195], v[148:151]\n"
"s_waitcnt lgkmcnt(0) vmcnt(8)\n"
"s_barrier\n"
"v_mfma_f32_16x16x32_bf16 v[112:115], v[64:67], v[196:199], v[112:115]\n"
"ds_read_b64_tr_b16 v[56:57], v7 offset:49152\n"
"s_add_u32 s90, s90, 589824\n"
"v_mfma_f32_16x16x32_bf16 v[116:119], v[64:67], v[200:203], v[116:119]\n"
"ds_read_b64_tr_b16 v[58:59], v7 offset:49664\n"
"ds_write_b64 v11, v[104:105] offset:13312\n"
"v_mul_f32 v111, v91, v111\n"
"v_mfma_f32_16x16x32_bf16 v[120:123], v[64:67], v[204:207], v[120:123]\n"
"v_exp_f32_e64 v91, -v91\n"
"v_mfma_f32_16x16x32_bf16 v[124:127], v[64:67], v[208:211], v[124:127]\n"
"buffer_load_dwordx4 v1, s[48:51], s90 offen lds\n"
"v_lshl_add_u32 v52, v52, 5, v50\n"
"v_mfma_f32_16x16x32_bf16 v[128:131], v[64:67], v[212:215], v[128:131]\n"
"v_add_f32 v88, 1.0, v88\n"
"v_rcp_f32 v88, v88\n"
"v_mfma_f32_16x16x32_bf16 v[132:135], v[68:71], v[196:199], v[132:135]\n"
"ds_read_b64_tr_b16 v[60:61], v7 offset:50176\n"
"v_add_u32 v52, 65536, v52\n"
"v_mfma_f32_16x16x32_bf16 v[136:139], v[68:71], v[200:203], v[136:139]\n"
"ds_read_b64_tr_b16 v[62:63], v7 offset:50688\n"
"v_add_f32 v89, 1.0, v89\n"
"v_mfma_f32_16x16x32_bf16 v[140:143], v[68:71], v[204:207], v[140:143]\n"
"v_rcp_f32 v89, v89\n"
"v_mfma_f32_16x16x32_bf16 v[144:147], v[68:71], v[208:211], v[144:147]\n"
"buffer_load_dwordx4 v1, s[52:55], s90 offen offset:1024 lds\n"
"v_xor_b32 v53, 136, v52\n"
"v_mfma_f32_16x16x32_bf16 v[148:151], v[68:71], v[212:215], v[148:151]\n"
"v_add_f32 v90, 1.0, v90\n"
"s_waitcnt lgkmcnt(3)\n"
"v_mfma_f32_16x16x32_bf16 v[112:115], v[56:59], v[216:219], v[112:115]\n"
"ds_read_b64_tr_b16 v[64:65], v7 offset:51200\n"
"v_rcp_f32 v90, v90\n"
"v_mfma_f32_16x16x32_bf16 v[116:119], v[56:59], v[220:223], v[116:119]\n"
"ds_read_b64_tr_b16 v[66:67], v7 offset:51712\n"
"v_mfma_f32_16x16x32_bf16 v[120:123], v[56:59], v[224:227], v[120:123]\n"
"buffer_load_dwordx4 v2, s[48:51], s90 offen offset:2048 lds\n"
"v_add_f32 v91, 1.0, v91\n"
"v_mfma_f32_16x16x32_bf16 v[124:127], v[56:59], v[228:231], v[124:127]\n"
"ds_read_b64_tr_b16 v[68:69], v7 offset:52224\n"
"v_mfma_f32_16x16x32_bf16 v[128:131], v[56:59], v[232:235], v[128:131]\n"
"ds_read_b64_tr_b16 v[70:71], v7 offset:52736\n"
"v_rcp_f32 v91, v91\n"
"s_waitcnt lgkmcnt(4)\n"
"v_mfma_f32_16x16x32_bf16 v[132:135], v[60:63], v[216:219], v[132:135]\n"
"v_mul_f32 v88, v88, v108\n"
"v_mfma_f32_16x16x32_bf16 v[136:139], v[60:63], v[220:223], v[136:139]\n"
"buffer_load_dwordx4 v2, s[52:55], s90 offen offset:3072 lds\n"
"v_mul_f32 v89, v89, v109\n"
"v_mfma_f32_16x16x32_bf16 v[140:143], v[60:63], v[224:227], v[140:143]\n"
"s_add_u32 m0, s97, 49152\n"
"v_mfma_f32_16x16x32_bf16 v[144:147], v[60:63], v[228:231], v[144:147]\n"
"v_mul_f32 v90, v90, v110\n"
"v_mul_f32 v91, v91, v111\n"
"v_mfma_f32_16x16x32_bf16 v[148:151], v[60:63], v[232:235], v[148:151]\n"
"s_waitcnt lgkmcnt(0) vmcnt(8)\n"
"s_barrier\n"
"v_mfma_f32_16x16x32_bf16 v[112:115], v[64:67], v[236:239], v[112:115]\n"
"ds_read_b64_tr_b16 v[56:57], v7 offset:0\n"
"s_add_u32 s90, s90, 589824\n"
"v_mfma_f32_16x16x32_bf16 v[116:119], v[64:67], v[240:243], v[116:119]\n"
"ds_read_b64_tr_b16 v[58:59], v7 offset:512\n"
"v_cvt_pk_bf16_f32 v108, v88, v89\n"
"v_mfma_f32_16x16x32_bf16 v[120:123], v[64:67], v[244:247], v[120:123]\n"
"v_cvt_pk_bf16_f32 v109, v90, v91\n"
"v_mfma_f32_16x16x32_bf16 v[124:127], v[64:67], v[248:251], v[124:127]\n"
"buffer_load_dwordx4 v1, s[48:51], s90 offen lds\n"
"v_mfma_f32_16x16x32_bf16 v[128:131], v[64:67], v[252:255], v[128:131]\n"
"v_mfma_f32_16x16x32_bf16 v[132:135], v[68:71], v[236:239], v[132:135]\n"
"ds_read_b64_tr_b16 v[60:61], v7 offset:1024\n"
"v_mfma_f32_16x16x32_bf16 v[136:139], v[68:71], v[240:243], v[136:139]\n"
"ds_read_b64_tr_b16 v[62:63], v7 offset:1536\n"
"v_mfma_f32_16x16x32_bf16 v[140:143], v[68:71], v[244:247], v[140:143]\n"
"ds_write_b64 v11, v[108:109] offset:14336\n"
"v_mfma_f32_16x16x32_bf16 v[144:147], v[68:71], v[248:251], v[144:147]\n"
"buffer_load_dwordx4 v1, s[52:55], s90 offen offset:1024 lds\n"
"v_mfma_f32_16x16x32_bf16 v[148:151], v[68:71], v[252:255], v[148:151]\n"
"s_waitcnt lgkmcnt(3)\n"
"v_mfma_f32_16x16x32_bf16 v[72:75], v[56:59], a[0:3], 0\n"
"ds_read_b64_tr_b16 v[64:65], v7 offset:2048\n"
"v_mfma_f32_16x16x32_bf16 v[76:79], v[56:59], a[4:7], 0\n"
"ds_read_b64_tr_b16 v[66:67], v7 offset:2560\n"
"v_mfma_f32_16x16x32_bf16 v[80:83], v[56:59], a[8:11], 0\n"
"buffer_load_dwordx4 v2, s[48:51], s90 offen offset:2048 lds\n"
"v_mfma_f32_16x16x32_bf16 v[84:87], v[56:59], a[12:15], 0\n"
"ds_read_b64_tr_b16 v[68:69], v7 offset:3072\n"
"v_mfma_f32_16x16x32_bf16 v[88:91], v[56:59], a[16:19], 0\n"
"ds_read_b64_tr_b16 v[70:71], v7 offset:3584\n"
"s_waitcnt lgkmcnt(5)\n"
"v_mfma_f32_16x16x32_bf16 v[92:95], v[60:63], a[0:3], 0\n"
"v_mul_f32 v132, v112, v132\n"
"v_exp_f32_e64 v112, -v112\n"
"v_mfma_f32_16x16x32_bf16 v[96:99], v[60:63], a[4:7], 0\n"
"buffer_load_dwordx4 v2, s[52:55], s90 offen offset:3072 lds\n"
"v_mfma_f32_16x16x32_bf16 v[100:103], v[60:63], a[8:11], 0\n"
"s_add_u32 m0, s97, 0\n"
"v_mul_f32 v133, v113, v133\n"
"v_mfma_f32_16x16x32_bf16 v[104:107], v[60:63], a[12:15], 0\n"
"v_exp_f32_e64 v113, -v113\n"
"v_mfma_f32_16x16x32_bf16 v[108:111], v[60:63], a[16:19], 0\n"
"v_mul_f32 v134, v114, v134\n"
"s_waitcnt lgkmcnt(0) vmcnt(8)\n"
"s_barrier\n"
"v_mfma_f32_16x16x32_bf16 v[72:75], v[64:67], a[20:23], v[72:75]\n"
"ds_read_b64_tr_b16 v[56:57], v7 offset:16384\n"
"s_add_u32 s90, s90, 589824\n"
"v_mfma_f32_16x16x32_bf16 v[76:79], v[64:67], a[24:27], v[76:79]\n"
"ds_read_b64_tr_b16 v[58:59], v7 offset:16896\n"
"ds_read_b64_tr_b16 a[0:1], v52 offset:0\n"
"v_exp_f32_e64 v114, -v114\n"
"v_mfma_f32_16x16x32_bf16 v[80:83], v[64:67], a[28:31], v[80:83]\n"
"ds_read_b64_tr_b16 a[2:3], v53 offset:0\n"
"ds_read_b64_tr_b16 a[4:5], v52 offset:2048\n"
"v_mfma_f32_16x16x32_bf16 v[84:87], v[64:67], a[32:35], v[84:87]\n"
"buffer_load_dwordx4 v1, s[48:51], s90 offen lds\n"
"ds_read_b64_tr_b16 a[6:7], v53 offset:2048\n"
"v_mfma_f32_16x16x32_bf16 v[88:91], v[64:67], a[36:39], v[88:91]\n"
"ds_read_b64_tr_b16 a[8:9], v52 offset:4096\n"
"v_mul_f32 v135, v115, v135\n"
"v_mfma_f32_16x16x32_bf16 v[92:95], v[68:71], a[20:23], v[92:95]\n"
"ds_read_b64_tr_b16 v[60:61], v7 offset:17408\n"
"ds_read_b64_tr_b16 a[10:11], v53 offset:4096\n"
"v_exp_f32_e64 v115, -v115\n"
"v_mfma_f32_16x16x32_bf16 v[96:99], v[68:71], a[24:27], v[96:99]\n"
"ds_read_b64_tr_b16 v[62:63], v7 offset:17920\n"
"ds_read_b64_tr_b16 a[12:13], v52 offset:6144\n"
"v_mfma_f32_16x16x32_bf16 v[100:103], v[68:71], a[28:31], v[100:103]\n"
"ds_read_b64_tr_b16 a[14:15], v53 offset:6144\n"
"v_mfma_f32_16x16x32_bf16 v[104:107], v[68:71], a[32:35], v[104:107]\n"
"buffer_load_dwordx4 v1, s[52:55], s90 offen offset:1024 lds\n"
"ds_read_b64_tr_b16 a[16:17], v52 offset:8192\n"
"v_add_f32 v112, 1.0, v112\n"
"v_mfma_f32_16x16x32_bf16 v[108:111], v[68:71], a[36:39], v[108:111]\n"
"ds_read_b64_tr_b16 a[18:19], v53 offset:8192\n"
"v_rcp_f32 v112, v112\n"
"s_waitcnt lgkmcnt(12)\n"
"v_mfma_f32_16x16x32_bf16 v[72:75], v[56:59], a[40:43], v[72:75]\n"
"ds_read_b64_tr_b16 v[64:65], v7 offset:18432\n"
"ds_read_b64_tr_b16 a[20:21], v52 offset:10240\n"
"ds_read_b64_tr_b16 a[22:23], v53 offset:10240\n"
"v_mfma_f32_16x16x32_bf16 v[76:79], v[56:59], a[44:47], v[76:79]\n"
"ds_read_b64_tr_b16 v[66:67], v7 offset:18944\n"
"ds_read_b64_tr_b16 a[24:25], v52 offset:12288\n"
"ds_read_b64_tr_b16 a[26:27], v53 offset:12288\n"
"v_mfma_f32_16x16x32_bf16 v[80:83], v[56:59], a[48:51], v[80:83]\n"
"buffer_load_dwordx4 v2, s[48:51], s90 offen offset:2048 lds\n"
"ds_read_b64_tr_b16 a[28:29], v52 offset:14336\n"
"v_mfma_f32_16x16x32_bf16 v[84:87], v[56:59], a[52:55], v[84:87]\n"
"ds_read_b64_tr_b16 v[68:69], v7 offset:19456\n"
"ds_read_b64_tr_b16 a[30:31], v53 offset:14336\n"
"ds_read_b64_tr_b16 a[32:33], v52 offset:16384\n"
"v_mfma_f32_16x16x32_bf16 v[88:91], v[56:59], a[56:59], v[88:91]\n"
"ds_read_b64_tr_b16 v[70:71], v7 offset:19968\n"
"ds_read_b64_tr_b16 a[34:35], v53 offset:16384\n"
"s_waitcnt lgkmcnt(15)\n"
"v_mfma_f32_16x16x32_bf16 v[92:95], v[60:63], a[40:43], v[92:95]\n"
"ds_read_b64_tr_b16 a[36:37], v52 offset:18432\n"
"ds_read_b64_tr_b16 a[38:39], v53 offset:18432\n"
"v_add_f32 v113, 1.0, v113\n"
"v_mfma_f32_16x16x32_bf16 v[96:99], v[60:63], a[44:47], v[96:99]\n"
"buffer_load_dwordx4 v2, s[52:55], s90 offen offset:3072 lds\n"
"v_rcp_f32 v113, v113\n"
"v_mfma_f32_16x16x32_bf16 v[100:103], v[60:63], a[48:51], v[100:103]\n"
"s_add_u32 m0, s97, 16384\n"
"v_mfma_f32_16x16x32_bf16 v[104:107], v[60:63], a[52:55], v[104:107]\n"
"v_add_f32 v114, 1.0, v114\n"
"v_rcp_f32 v114, v114\n"
"v_mfma_f32_16x16x32_bf16 v[108:111], v[60:63], a[56:59], v[108:111]\n"
"s_waitcnt lgkmcnt(3) vmcnt(8)\n"
"s_barrier\n"
"v_mfma_f32_16x16x32_bf16 v[72:75], v[64:67], a[60:63], v[72:75]\n"
"ds_read_b64_tr_b16 v[56:57], v7 offset:32768\n"
"s_add_u32 s90, s90, 589824\n"
"v_mfma_f32_16x16x32_bf16 v[76:79], v[64:67], a[64:67], v[76:79]\n"
"ds_read_b64_tr_b16 v[58:59], v7 offset:33280\n"
"ds_read_b64_tr_b16 a[40:41], v52 offset:1024\n"
"v_mfma_f32_16x16x32_bf16 v[80:83], v[64:67], a[68:71], v[80:83]\n"
"ds_read_b64_tr_b16 a[42:43], v53 offset:1024\n"
"ds_read_b64_tr_b16 a[44:45], v52 offset:3072\n"
"v_add_f32 v115, 1.0, v115\n"
"v_mfma_f32_16x16x32_bf16 v[84:87], v[64:67], a[72:75], v[84:87]\n"
"buffer_load_dwordx4 v1, s[48:51], s90 offen lds\n"
"ds_read_b64_tr_b16 a[46:47], v53 offset:3072\n"
"v_mfma_f32_16x16x32_bf16 v[88:91], v[64:67], a[76:79], v[88:91]\n"
"ds_read_b64_tr_b16 a[48:49], v52 offset:5120\n"
"v_rcp_f32 v115, v115\n"
"v_mfma_f32_16x16x32_bf16 v[92:95], v[68:71], a[60:63], v[92:95]\n"
"ds_read_b64_tr_b16 v[60:61], v7 offset:33792\n"
"ds_read_b64_tr_b16 a[50:51], v53 offset:5120\n"
"v_mfma_f32_16x16x32_bf16 v[96:99], v[68:71], a[64:67], v[96:99]\n"
"ds_read_b64_tr_b16 v[62:63], v7 offset:34304\n"
"ds_read_b64_tr_b16 a[52:53], v52 offset:7168\n"
"v_mul_f32 v112, v112, v132\n"
"v_mfma_f32_16x16x32_bf16 v[100:103], v[68:71], a[68:71], v[100:103]\n"
"ds_read_b64_tr_b16 a[54:55], v53 offset:7168\n"
"v_mul_f32 v113, v113, v133\n"
"v_mfma_f32_16x16x32_bf16 v[104:107], v[68:71], a[72:75], v[104:107]\n"
"buffer_load_dwordx4 v1, s[52:55], s90 offen offset:1024 lds\n"
"ds_read_b64_tr_b16 a[56:57], v52 offset:9216\n"
"v_mfma_f32_16x16x32_bf16 v[108:111], v[68:71], a[76:79], v[108:111]\n"
"ds_read_b64_tr_b16 a[58:59], v53 offset:9216\n"
"v_mul_f32 v114, v114, v134\n"
"v_mul_f32 v115, v115, v135\n"
"s_waitcnt lgkmcnt(12)\n"
"v_mfma_f32_16x16x32_bf16 v[72:75], v[56:59], a[80:83], v[72:75]\n"
"ds_read_b64_tr_b16 v[64:65], v7 offset:34816\n"
"ds_read_b64_tr_b16 a[60:61], v52 offset:11264\n"
"ds_read_b64_tr_b16 a[62:63], v53 offset:11264\n"
"v_mfma_f32_16x16x32_bf16 v[76:79], v[56:59], a[84:87], v[76:79]\n"
"ds_read_b64_tr_b16 v[66:67], v7 offset:35328\n"
"ds_read_b64_tr_b16 a[64:65], v52 offset:13312\n"
"ds_read_b64_tr_b16 a[66:67], v53 offset:13312\n"
"v_mfma_f32_16x16x32_bf16 v[80:83], v[56:59], a[88:91], v[80:83]\n"
"buffer_load_dwordx4 v2, s[48:51], s90 offen offset:2048 lds\n"
"ds_read_b64_tr_b16 a[68:69], v52 offset:15360\n"
"v_mfma_f32_16x16x32_bf16 v[84:87], v[56:59], a[92:95], v[84:87]\n"
"ds_read_b64_tr_b16 v[68:69], v7 offset:35840\n"
"ds_read_b64_tr_b16 a[70:71], v53 offset:15360\n"
"ds_read_b64_tr_b16 a[72:73], v52 offset:17408\n"
"v_mfma_f32_16x16x32_bf16 v[88:91], v[56:59], a[96:99], v[88:91]\n"
"ds_read_b64_tr_b16 v[70:71], v7 offset:36352\n"
"ds_read_b64_tr_b16 a[74:75], v53 offset:17408\n"
"v_cvt_pk_bf16_f32 v132, v112, v113\n"
"s_waitcnt lgkmcnt(15)\n"
"v_mfma_f32_16x16x32_bf16 v[92:95], v[60:63], a[80:83], v[92:95]\n"
"ds_read_b64_tr_b16 a[76:77], v52 offset:19456\n"
"ds_read_b64_tr_b16 a[78:79], v53 offset:19456\n"
"v_mfma_f32_16x16x32_bf16 v[96:99], v[60:63], a[84:87], v[96:99]\n"
"buffer_load_dwordx4 v2, s[52:55], s90 offen offset:3072 lds\n"
"v_cvt_pk_bf16_f32 v133, v114, v115\n"
"v_mfma_f32_16x16x32_bf16 v[100:103], v[60:63], a[88:91], v[100:103]\n"
"s_add_u32 m0, s97, 32768\n"
"v_mul_f32 v136, v116, v136\n"
"v_mfma_f32_16x16x32_bf16 v[104:107], v[60:63], a[92:95], v[104:107]\n"
"v_exp_f32_e64 v116, -v116\n"
"v_mfma_f32_16x16x32_bf16 v[108:111], v[60:63], a[96:99], v[108:111]\n"
"v_mul_f32 v137, v117, v137\n"
"s_waitcnt lgkmcnt(3) vmcnt(8)\n"
"s_barrier\n"
"v_mfma_f32_16x16x32_bf16 v[72:75], v[64:67], a[100:103], v[72:75]\n"
"ds_read_b64_tr_b16 v[56:57], v7 offset:49152\n"
"s_add_u32 s90, s90, 589824\n"
"v_mfma_f32_16x16x32_bf16 v[76:79], v[64:67], a[104:107], v[76:79]\n"
"ds_read_b64_tr_b16 v[58:59], v7 offset:49664\n"
"ds_read_b128 a[80:83], v12 offset:20480\n"
"ds_write_b64 v11, v[132:133] offset:20480\n"
"v_exp_f32_e64 v117, -v117\n"
"v_mfma_f32_16x16x32_bf16 v[80:83], v[64:67], a[108:111], v[80:83]\n"
"ds_read_b128 a[84:87], v12 offset:21504\n"
"v_mfma_f32_16x16x32_bf16 v[84:87], v[64:67], a[112:115], v[84:87]\n"
"buffer_load_dwordx4 v1, s[48:51], s90 offen lds\n"
"v_mul_f32 v138, v118, v138\n"
"v_mfma_f32_16x16x32_bf16 v[88:91], v[64:67], a[116:119], v[88:91]\n"
"ds_read_b128 a[88:91], v12 offset:22528\n"
"v_exp_f32_e64 v118, -v118\n"
"v_mfma_f32_16x16x32_bf16 v[92:95], v[68:71], a[100:103], v[92:95]\n"
"ds_read_b64_tr_b16 v[60:61], v7 offset:50176\n"
"v_mfma_f32_16x16x32_bf16 v[96:99], v[68:71], a[104:107], v[96:99]\n"
"ds_read_b64_tr_b16 v[62:63], v7 offset:50688\n"
"ds_read_b128 a[92:95], v12 offset:23552\n"
"v_mul_f32 v139, v119, v139\n"
"v_mfma_f32_16x16x32_bf16 v[100:103], v[68:71], a[108:111], v[100:103]\n"
"v_exp_f32_e64 v119, -v119\n"
"v_mfma_f32_16x16x32_bf16 v[104:107], v[68:71], a[112:115], v[104:107]\n"
"buffer_load_dwordx4 v1, s[52:55], s90 offen offset:1024 lds\n"
"ds_read_b128 a[96:99], v12 offset:24576\n"
"v_mfma_f32_16x16x32_bf16 v[108:111], v[68:71], a[116:119], v[108:111]\n"
"v_add_f32 v116, 1.0, v116\n"
"v_rcp_f32 v116, v116\n"
"s_waitcnt lgkmcnt(8)\n"
"v_mfma_f32_16x16x32_bf16 v[72:75], v[56:59], a[120:123], v[72:75]\n"
"ds_read_b64_tr_b16 v[64:65], v7 offset:51200\n"
"ds_read_b128 a[100:103], v12 offset:25600\n"
"v_mfma_f32_16x16x32_bf16 v[76:79], v[56:59], a[124:127], v[76:79]\n"
"ds_read_b64_tr_b16 v[66:67], v7 offset:51712\n"
"ds_read_b128 a[104:107], v12 offset:26624\n"
"v_mfma_f32_16x16x32_bf16 v[80:83], v[56:59], a[128:131], v[80:83]\n"
"buffer_load_dwordx4 v2, s[48:51], s90 offen offset:2048 lds\n"
"ds_read_b128 a[108:111], v12 offset:27648\n"
"v_add_f32 v117, 1.0, v117\n"
"v_mfma_f32_16x16x32_bf16 v[84:87], v[56:59], a[132:135], v[84:87]\n"
"ds_read_b64_tr_b16 v[68:69], v7 offset:52224\n"
"v_rcp_f32 v117, v117\n"
"v_mfma_f32_16x16x32_bf16 v[88:91], v[56:59], a[136:139], v[88:91]\n"
"ds_read_b64_tr_b16 v[70:71], v7 offset:52736\n"
"ds_read_b128 a[112:115], v12 offset:28672\n"
"s_waitcnt lgkmcnt(10)\n"
"v_mfma_f32_16x16x32_bf16 v[92:95], v[60:63], a[120:123], v[92:95]\n"
"ds_read_b128 a[116:119], v12 offset:29696\n"
"v_mfma_f32_16x16x32_bf16 v[96:99], v[60:63], a[124:127], v[96:99]\n"
"buffer_load_dwordx4 v2, s[52:55], s90 offen offset:3072 lds\n"
"v_add_f32 v118, 1.0, v118\n"
"v_rcp_f32 v118, v118\n"
"v_mfma_f32_16x16x32_bf16 v[100:103], v[60:63], a[128:131], v[100:103]\n"
"s_add_u32 m0, s97, 49152\n"
"v_mfma_f32_16x16x32_bf16 v[104:107], v[60:63], a[132:135], v[104:107]\n"
"v_add_f32 v119, 1.0, v119\n"
"v_mfma_f32_16x16x32_bf16 v[108:111], v[60:63], a[136:139], v[108:111]\n"
"v_rcp_f32 v119, v119\n"
"s_waitcnt lgkmcnt(2) vmcnt(8)\n"
"s_barrier\n"
"v_mfma_f32_16x16x32_bf16 v[72:75], v[64:67], a[140:143], v[72:75]\n"
"ds_read_b64_tr_b16 v[56:57], v7 offset:0\n"
"s_add_u32 s90, s90, 589824\n"
"v_mfma_f32_16x16x32_bf16 v[76:79], v[64:67], a[144:147], v[76:79]\n"
"ds_read_b64_tr_b16 v[58:59], v7 offset:512\n"
"ds_read_b128 a[120:123], v12 offset:30720\n"
"v_mfma_f32_16x16x32_bf16 v[80:83], v[64:67], a[148:151], v[80:83]\n"
"ds_read_b128 a[124:127], v12 offset:31744\n"
"v_mul_f32 v116, v116, v136\n"
"v_mfma_f32_16x16x32_bf16 v[84:87], v[64:67], a[152:155], v[84:87]\n"
"buffer_load_dwordx4 v1, s[48:51], s90 offen lds\n"
"v_mul_f32 v117, v117, v137\n"
"v_mfma_f32_16x16x32_bf16 v[88:91], v[64:67], a[156:159], v[88:91]\n"
"ds_read_b128 a[128:131], v12 offset:32768\n"
"v_mul_f32 v118, v118, v138\n"
"v_mfma_f32_16x16x32_bf16 v[92:95], v[68:71], a[140:143], v[92:95]\n"
"ds_read_b64_tr_b16 v[60:61], v7 offset:1024\n"
"v_mul_f32 v119, v119, v139\n"
"v_cvt_pk_bf16_f32 v136, v116, v117\n"
"v_mfma_f32_16x16x32_bf16 v[96:99], v[68:71], a[144:147], v[96:99]\n"
"ds_read_b64_tr_b16 v[62:63], v7 offset:1536\n"
"ds_read_b128 a[132:135], v12 offset:33792\n"
"v_mfma_f32_16x16x32_bf16 v[100:103], v[68:71], a[148:151], v[100:103]\n"
"v_cvt_pk_bf16_f32 v137, v118, v119\n"
"v_mul_f32 v140, v120, v140\n"
"v_mfma_f32_16x16x32_bf16 v[104:107], v[68:71], a[152:155], v[104:107]\n"
"buffer_load_dwordx4 v1, s[52:55], s90 offen offset:1024 lds\n"
"ds_read_b128 a[136:139], v12 offset:34816\n"
"v_mfma_f32_16x16x32_bf16 v[108:111], v[68:71], a[156:159], v[108:111]\n"
"v_exp_f32_e64 v120, -v120\n"
"s_waitcnt lgkmcnt(7)\n"
"v_mfma_f32_16x16x32_bf16 v[72:75], v[56:59], a[160:163], v[72:75]\n"
"ds_read_b64_tr_b16 v[64:65], v7 offset:2048\n"
"ds_read_b128 a[140:143], v12 offset:35840\n"
"v_mul_f32 v141, v121, v141\n"
"v_mfma_f32_16x16x32_bf16 v[76:79], v[56:59], a[164:167], v[76:79]\n"
"ds_read_b64_tr_b16 v[66:67], v7 offset:2560\n"
"ds_read_b128 a[144:147], v12 offset:36864\n"
"v_mfma_f32_16x16x32_bf16 v[80:83], v[56:59], a[168:171], v[80:83]\n"
"buffer_load_dwordx4 v2, s[48:51], s90 offen offset:2048 lds\n"
"ds_read_b128 a[148:151], v12 offset:37888\n"
"v_exp_f32_e64 v121, -v121\n"
"v_mfma_f32_16x16x32_bf16 v[84:87], v[56:59], a[172:175], v[84:87]\n"
"ds_read_b64_tr_b16 v[68:69], v7 offset:3072\n"
"v_mfma_f32_16x16x32_bf16 v[88:91], v[56:59], a[176:179], v[88:91]\n"
"ds_read_b64_tr_b16 v[70:71], v7 offset:3584\n"
"ds_read_b128 a[152:155], v12 offset:38912\n"
"s_waitcnt lgkmcnt(10)\n"
"v_mfma_f32_16x16x32_bf16 v[92:95], v[60:63], a[160:163], v[92:95]\n"
"ds_read_b128 a[156:159], v12 offset:39936\n"
"v_mul_f32 v142, v122, v142\n"
"v_exp_f32_e64 v122, -v122\n"
"v_mfma_f32_16x16x32_bf16 v[96:99], v[60:63], a[164:167], v[96:99]\n"
"buffer_load_dwordx4 v2, s[52:55], s90 offen offset:3072 lds\n"
"v_mfma_f32_16x16x32_bf16 v[100:103], v[60:63], a[168:171], v[100:103]\n"
"s_add_u32 m0, s97, 0\n"
"v_mul_f32 v143, v123, v143\n"
"v_mfma_f32_16x16x32_bf16 v[104:107], v[60:63], a[172:175], v[104:107]\n"
"v_exp_f32_e64 v123, -v123\n"
"v_mfma_f32_16x16x32_bf16 v[108:111], v[60:63], a[176:179], v[108:111]\n"
"s_waitcnt lgkmcnt(2) vmcnt(8)\n"
"s_barrier\n"
"v_mfma_f32_16x16x32_bf16 v[72:75], v[64:67], a[180:183], v[72:75]\n"
"ds_read_b64_tr_b16 v[56:57], v7 offset:16384\n"
"s_add_u32 s90, s90, 589824\n"
"v_add_f32 v120, 1.0, v120\n"
"v_mfma_f32_16x16x32_bf16 v[76:79], v[64:67], a[184:187], v[76:79]\n"
"ds_read_b64_tr_b16 v[58:59], v7 offset:16896\n"
"ds_read_b128 a[160:163], v12 offset:40960\n"
"ds_write_b64 v11, v[136:137] offset:21504\n"
"v_mfma_f32_16x16x32_bf16 v[80:83], v[64:67], a[188:191], v[80:83]\n"
"ds_read_b128 a[164:167], v12 offset:41984\n"
"v_rcp_f32 v120, v120\n"
"v_mfma_f32_16x16x32_bf16 v[84:87], v[64:67], a[192:195], v[84:87]\n"
"buffer_load_dwordx4 v1, s[48:51], s90 offen lds\n"
"v_mfma_f32_16x16x32_bf16 v[88:91], v[64:67], a[196:199], v[88:91]\n"
"ds_read_b128 a[168:171], v12 offset:43008\n"
"v_add_f32 v121, 1.0, v121\n"
"v_rcp_f32 v121, v121\n"
"v_mfma_f32_16x16x32_bf16 v[92:95], v[68:71], a[180:183], v[92:95]\n"
"ds_read_b64_tr_b16 v[60:61], v7 offset:17408\n"
"v_mfma_f32_16x16x32_bf16 v[96:99], v[68:71], a[184:187], v[96:99]\n"
"ds_read_b64_tr_b16 v[62:63], v7 offset:17920\n"
"ds_read_b128 a[172:175], v12 offset:44032\n"
"v_mfma_f32_16x16x32_bf16 v[100:103], v[68:71], a[188:191], v[100:103]\n"
"v_add_f32 v122, 1.0, v122\n"
"v_rcp_f32 v122, v122\n"
"v_mfma_f32_16x16x32_bf16 v[104:107], v[68:71], a[192:195], v[104:107]\n"
"buffer_load_dwordx4 v1, s[52:55], s90 offen offset:1024 lds\n"
"ds_read_b128 a[176:179], v12 offset:45056\n"
"v_mfma_f32_16x16x32_bf16 v[108:111], v[68:71], a[196:199], v[108:111]\n"
"v_add_f32 v123, 1.0, v123\n"
"s_waitcnt lgkmcnt(8)\n"
"v_mfma_f32_16x16x32_bf16 v[72:75], v[56:59], a[200:203], v[72:75]\n"
"ds_read_b64_tr_b16 v[64:65], v7 offset:18432\n"
"ds_read_b128 a[180:183], v12 offset:46080\n"
"v_rcp_f32 v123, v123\n"
"v_mfma_f32_16x16x32_bf16 v[76:79], v[56:59], a[204:207], v[76:79]\n"
"ds_read_b64_tr_b16 v[66:67], v7 offset:18944\n"
"ds_read_b128 a[184:187], v12 offset:47104\n"
"v_mfma_f32_16x16x32_bf16 v[80:83], v[56:59], a[208:211], v[80:83]\n"
"buffer_load_dwordx4 v2, s[48:51], s90 offen offset:2048 lds\n"
"ds_read_b128 a[188:191], v12 offset:48128\n"
"v_mfma_f32_16x16x32_bf16 v[84:87], v[56:59], a[212:215], v[84:87]\n"
"ds_read_b64_tr_b16 v[68:69], v7 offset:19456\n"
"v_mul_f32 v120, v120, v140\n"
"v_mfma_f32_16x16x32_bf16 v[88:91], v[56:59], a[216:219], v[88:91]\n"
"ds_read_b64_tr_b16 v[70:71], v7 offset:19968\n"
"ds_read_b128 a[192:195], v12 offset:49152\n"
"v_mul_f32 v121, v121, v141\n"
"s_waitcnt lgkmcnt(10)\n"
"v_mfma_f32_16x16x32_bf16 v[92:95], v[60:63], a[200:203], v[92:95]\n"
"ds_read_b128 a[196:199], v12 offset:50176\n"
"v_mul_f32 v122, v122, v142\n"
"v_mfma_f32_16x16x32_bf16 v[96:99], v[60:63], a[204:207], v[96:99]\n"
"buffer_load_dwordx4 v2, s[52:55], s90 offen offset:3072 lds\n"
"v_mul_f32 v123, v123, v143\n"
"v_mfma_f32_16x16x32_bf16 v[100:103], v[60:63], a[208:211], v[100:103]\n"
"s_add_u32 m0, s97, 16384\n"
"s_add_u32 s93, s93, 128\n"
"v_mfma_f32_16x16x32_bf16 v[104:107], v[60:63], a[212:215], v[104:107]\n"
"s_cmp_eq_u32 s93, 1152\n"
"s_cselect_b32 s93, 0, s93\n"
"v_cvt_pk_bf16_f32 v140, v120, v121\n"
"v_mfma_f32_16x16x32_bf16 v[108:111], v[60:63], a[216:219], v[108:111]\n"
"v_cvt_pk_bf16_f32 v141, v122, v123\n"
"s_waitcnt lgkmcnt(2) vmcnt(8)\n"
"s_barrier\n"
"v_mfma_f32_16x16x32_bf16 v[72:75], v[64:67], a[220:223], v[72:75]\n"
"ds_read_b64_tr_b16 v[56:57], v7 offset:32768\n"
"ds_read_b128 a[200:203], v12 offset:51200\n"
"v_mfma_f32_16x16x32_bf16 v[76:79], v[64:67], a[224:227], v[76:79]\n"
"ds_read_b64_tr_b16 v[58:59], v7 offset:33280\n"
"s_mul_i32 s90, s93, 576\n"
"ds_write_b64 v11, v[140:141] offset:22528\n"
"v_mul_f32 v144, v124, v144\n"
"v_mfma_f32_16x16x32_bf16 v[80:83], v[64:67], a[228:231], v[80:83]\n"
"s_add_u32 s90, s90, s92\n"
"v_exp_f32_e64 v124, -v124\n"
"v_mfma_f32_16x16x32_bf16 v[84:87], v[64:67], a[232:235], v[84:87]\n"
"buffer_load_dwordx4 v3, s[40:43], s90 offen lds\n"
"ds_read_b128 a[204:207], v12 offset:52224\n"
"v_mfma_f32_16x16x32_bf16 v[88:91], v[64:67], a[236:239], v[88:91]\n"
"ds_read_b128 a[208:211], v12 offset:53248\n"
"v_mfma_f32_16x16x32_bf16 v[92:95], v[68:71], a[220:223], v[92:95]\n"
"ds_read_b64_tr_b16 v[60:61], v7 offset:33792\n"
"v_mul_f32 v145, v125, v145\n"
"v_exp_f32_e64 v125, -v125\n"
"v_mfma_f32_16x16x32_bf16 v[96:99], v[68:71], a[224:227], v[96:99]\n"
"ds_read_b64_tr_b16 v[62:63], v7 offset:34304\n"
"v_mfma_f32_16x16x32_bf16 v[100:103], v[68:71], a[228:231], v[100:103]\n"
"ds_read_b128 a[212:215], v12 offset:54272\n"
"v_mul_f32 v146, v126, v146\n"
"v_mfma_f32_16x16x32_bf16 v[104:107], v[68:71], a[232:235], v[104:107]\n"
"buffer_load_dwordx4 v3, s[44:47], s90 offen offset:1024 lds\n"
"ds_read_b128 a[216:219], v12 offset:55296\n"
"v_mfma_f32_16x16x32_bf16 v[108:111], v[68:71], a[236:239], v[108:111]\n"
"v_exp_f32_e64 v126, -v126\n"
"s_waitcnt lgkmcnt(7)\n"
"v_mfma_f32_16x16x32_bf16 v[72:75], v[56:59], a[240:243], v[72:75]\n"
"ds_read_b64_tr_b16 v[64:65], v7 offset:34816\n"
"ds_read_b128 a[220:223], v12 offset:56320\n"
"v_mul_f32 v147, v127, v147\n"
"v_mfma_f32_16x16x32_bf16 v[76:79], v[56:59], a[244:247], v[76:79]\n"
"ds_read_b64_tr_b16 v[66:67], v7 offset:35328\n"
"ds_read_b128 a[224:227], v12 offset:57344\n"
"v_mfma_f32_16x16x32_bf16 v[80:83], v[56:59], a[248:251], v[80:83]\n"
"buffer_load_dwordx4 v4, s[40:43], s90 offen offset:2048 lds\n"
"ds_read_b128 a[228:231], v12 offset:58368\n"
"v_exp_f32_e64 v127, -v127\n"
"v_mfma_f32_16x16x32_bf16 v[84:87], v[56:59], a[252:255], v[84:87]\n"
"ds_read_b64_tr_b16 v[68:69], v7 offset:35840\n"
"v_mfma_f32_16x16x32_bf16 v[88:91], v[56:59], v[152:155], v[88:91]\n"
"ds_read_b64_tr_b16 v[70:71], v7 offset:36352\n"
"ds_read_b128 a[232:235], v12 offset:59392\n"
"v_add_f32 v124, 1.0, v124\n"
"s_waitcnt lgkmcnt(10)\n"
"v_mfma_f32_16x16x32_bf16 v[92:95], v[60:63], a[240:243], v[92:95]\n"
"ds_read_b128 a[236:239], v12 offset:60416\n"
"v_rcp_f32 v124, v124\n"
"v_mfma_f32_16x16x32_bf16 v[96:99], v[60:63], a[244:247], v[96:99]\n"
"buffer_load_dwordx4 v4, s[44:47], s90 offen offset:3072 lds\n"
"v_mfma_f32_16x16x32_bf16 v[100:103], v[60:63], a[248:251], v[100:103]\n"
"s_add_u32 m0, s97, 32768\n"
"v_add_f32 v125, 1.0, v125\n"
"v_mfma_f32_16x16x32_bf16 v[104:107], v[60:63], a[252:255], v[104:107]\n"
"s_add_u32 s90, s80, s93\n"
"v_rcp_f32 v125, v125\n"
"v_mfma_f32_16x16x32_bf16 v[108:111], v[60:63], v[152:155], v[108:111]\n"
"s_waitcnt lgkmcnt(2) vmcnt(8)\n"
"s_barrier\n"
"v_mfma_f32_16x16x32_bf16 v[72:75], v[64:67], v[156:159], v[72:75]\n"
"ds_read_b64_tr_b16 v[56:57], v7 offset:49152\n"
"v_add_u32 v12, 61440, v12\n"
"v_mfma_f32_16x16x32_bf16 v[76:79], v[64:67], v[160:163], v[76:79]\n"
"ds_read_b64_tr_b16 v[58:59], v7 offset:49664\n"
"s_add_u32 s91, s81, s93\n"
"v_add_f32 v126, 1.0, v126\n"
"v_mfma_f32_16x16x32_bf16 v[80:83], v[64:67], v[164:167], v[80:83]\n"
"ds_read_b128 a[240:243], v12 offset:0\n"
"v_rcp_f32 v126, v126\n"
"v_mfma_f32_16x16x32_bf16 v[84:87], v[64:67], v[168:171], v[84:87]\n"
"buffer_load_dwordx4 v5, s[56:59], s90 offen lds\n"
"v_mfma_f32_16x16x32_bf16 v[88:91], v[64:67], v[172:175], v[88:91]\n"
"ds_read_b128 a[244:247], v12 offset:1024\n"
"v_add_f32 v127, 1.0, v127\n"
"v_mfma_f32_16x16x32_bf16 v[92:95], v[68:71], v[156:159], v[92:95]\n"
"ds_read_b64_tr_b16 v[60:61], v7 offset:50176\n"
"ds_read_b128 a[248:251], v12 offset:2048\n"
"v_rcp_f32 v127, v127\n"
"v_mfma_f32_16x16x32_bf16 v[96:99], v[68:71], v[160:163], v[96:99]\n"
"ds_read_b64_tr_b16 v[62:63], v7 offset:50688\n"
"v_mfma_f32_16x16x32_bf16 v[100:103], v[68:71], v[164:167], v[100:103]\n"
"ds_read_b128 a[252:255], v12 offset:3072\n"
"v_mul_f32 v124, v124, v144\n"
"v_mfma_f32_16x16x32_bf16 v[104:107], v[68:71], v[168:171], v[104:107]\n"
"buffer_load_dwordx4 v6, s[56:59], s90 offen offset:1024 lds\n"
"ds_read_b128 v[152:155], v12 offset:4096\n"
"v_mfma_f32_16x16x32_bf16 v[108:111], v[68:71], v[172:175], v[108:111]\n"
"v_mul_f32 v125, v125, v145\n"
"v_mul_f32 v126, v126, v146\n"
"s_waitcnt lgkmcnt(7)\n"
"v_mfma_f32_16x16x32_bf16 v[72:75], v[56:59], v[176:179], v[72:75]\n"
"ds_read_b64_tr_b16 v[64:65], v7 offset:51200\n"
"ds_read_b128 v[156:159], v12 offset:5120\n"
"v_mul_f32 v127, v127, v147\n"
"v_mfma_f32_16x16x32_bf16 v[76:79], v[56:59], v[180:183], v[76:79]\n"
"ds_read_b64_tr_b16 v[66:67], v7 offset:51712\n"
"ds_read_b128 v[160:163], v12 offset:6144\n"
"v_mfma_f32_16x16x32_bf16 v[80:83], v[56:59], v[184:187], v[80:83]\n"
"buffer_load_dwordx4 v5, s[56:59], s91 offen offset:2048 lds\n"
"ds_read_b128 v[164:167], v12 offset:7168\n"
"v_cvt_pk_bf16_f32 v144, v124, v125\n"
"v_mfma_f32_16x16x32_bf16 v[84:87], v[56:59], v[188:191], v[84:87]\n"
"ds_read_b64_tr_b16 v[68:69], v7 offset:52224\n"
"v_cvt_pk_bf16_f32 v145, v126, v127\n"
"v_mfma_f32_16x16x32_bf16 v[88:91], v[56:59], v[192:195], v[88:91]\n"
"ds_read_b64_tr_b16 v[70:71], v7 offset:52736\n"
"ds_read_b128 v[168:171], v12 offset:8192\n"
"v_mul_f32 v148, v128, v148\n"
"s_waitcnt lgkmcnt(10)\n"
"v_mfma_f32_16x16x32_bf16 v[92:95], v[60:63], v[176:179], v[92:95]\n"
"ds_read_b128 v[172:175], v12 offset:9216\n"
"v_exp_f32_e64 v128, -v128\n"
"v_mfma_f32_16x16x32_bf16 v[96:99], v[60:63], v[180:183], v[96:99]\n"
"buffer_load_dwordx4 v6, s[56:59], s91 offen offset:3072 lds\n"
"v_mfma_f32_16x16x32_bf16 v[100:103], v[60:63], v[184:187], v[100:103]\n"
"s_add_u32 m0, s97, 49152\n"
"v_mul_f32 v149, v129, v149\n"
"v_mfma_f32_16x16x32_bf16 v[104:107], v[60:63], v[188:191], v[104:107]\n"
"s_add_u32 s90, s82, s93\n"
"v_exp_f32_e64 v129, -v129\n"
"v_mfma_f32_16x16x32_bf16 v[108:111], v[60:63], v[192:195], v[108:111]\n"
"s_waitcnt lgkmcnt(2) vmcnt(8)\n"
"s_barrier\n"
"v_mfma_f32_16x16x32_bf16 v[72:75], v[64:67], v[196:199], v[72:75]\n"
"ds_read_b64_tr_b16 v[56:57], v7 offset:0\n"
"ds_read_b128 v[176:179], v12 offset:10240\n"
"v_mfma_f32_16x16x32_bf16 v[76:79], v[64:67], v[200:203], v[76:79]\n"
"ds_read_b64_tr_b16 v[58:59], v7 offset:512\n"
"s_add_u32 s91, s83, s93\n"
"ds_write_b64 v11, v[144:145] offset:23552\n"
"v_mul_f32 v150, v130, v150\n"
"v_mfma_f32_16x16x32_bf16 v[80:83], v[64:67], v[204:207], v[80:83]\n"
"ds_read_b128 v[180:183], v12 offset:11264\n"
"v_exp_f32_e64 v130, -v130\n"
"v_mfma_f32_16x16x32_bf16 v[84:87], v[64:67], v[208:211], v[84:87]\n"
"buffer_load_dwordx4 v5, s[56:59], s90 offen lds\n"
"v_mfma_f32_16x16x32_bf16 v[88:91], v[64:67], v[212:215], v[88:91]\n"
"ds_read_b128 v[184:187], v12 offset:12288\n"
"v_mul_f32 v151, v131, v151\n"
"v_mfma_f32_16x16x32_bf16 v[92:95], v[68:71], v[196:199], v[92:95]\n"
"ds_read_b64_tr_b16 v[60:61], v7 offset:1024\n"
"v_exp_f32_e64 v131, -v131\n"
"v_mfma_f32_16x16x32_bf16 v[96:99], v[68:71], v[200:203], v[96:99]\n"
"ds_read_b64_tr_b16 v[62:63], v7 offset:1536\n"
"ds_read_b128 v[188:191], v12 offset:13312\n"
"v_mfma_f32_16x16x32_bf16 v[100:103], v[68:71], v[204:207], v[100:103]\n"
"v_add_f32 v128, 1.0, v128\n"
"v_rcp_f32 v128, v128\n"
"v_mfma_f32_16x16x32_bf16 v[104:107], v[68:71], v[208:211], v[104:107]\n"
"buffer_load_dwordx4 v6, s[56:59], s90 offen offset:1024 lds\n"
"ds_read_b128 v[192:195], v12 offset:14336\n"
"v_mfma_f32_16x16x32_bf16 v[108:111], v[68:71], v[212:215], v[108:111]\n"
"v_add_f32 v129, 1.0, v129\n"
"s_waitcnt lgkmcnt(7)\n"
"v_mfma_f32_16x16x32_bf16 v[72:75], v[56:59], v[216:219], v[72:75]\n"
"ds_read_b64_tr_b16 v[64:65], v7 offset:2048\n"
"ds_read_b128 v[196:199], v12 offset:15360\n"
"v_mfma_f32_16x16x32_bf16 v[76:79], v[56:59], v[220:223], v[76:79]\n"
"ds_read_b64_tr_b16 v[66:67], v7 offset:2560\n"
"ds_read_b128 v[200:203], v12 offset:16384\n"
"v_rcp_f32 v129, v129\n"
"v_mfma_f32_16x16x32_bf16 v[80:83], v[56:59], v[224:227], v[80:83]\n"
"buffer_load_dwordx4 v5, s[56:59], s91 offen offset:2048 lds\n"
"ds_read_b128 v[204:207], v12 offset:17408\n"
"v_mfma_f32_16x16x32_bf16 v[84:87], v[56:59], v[228:231], v[84:87]\n"
"ds_read_b64_tr_b16 v[68:69], v7 offset:3072\n"
"v_add_f32 v130, 1.0, v130\n"
"v_mfma_f32_16x16x32_bf16 v[88:91], v[56:59], v[232:235], v[88:91]\n"
"ds_read_b64_tr_b16 v[70:71], v7 offset:3584\n"
"ds_read_b128 v[208:211], v12 offset:18432\n"
"s_waitcnt lgkmcnt(10)\n"
"v_mfma_f32_16x16x32_bf16 v[92:95], v[60:63], v[216:219], v[92:95]\n"
"ds_read_b128 v[212:215], v12 offset:19456\n"
"v_rcp_f32 v130, v130\n"
"v_mfma_f32_16x16x32_bf16 v[96:99], v[60:63], v[220:223], v[96:99]\n"
"buffer_load_dwordx4 v6, s[56:59], s91 offen offset:3072 lds\n"
"v_mfma_f32_16x16x32_bf16 v[100:103], v[60:63], v[224:227], v[100:103]\n"
"s_add_u32 m0, s97, 0\n"
"v_add_f32 v131, 1.0, v131\n"
"v_rcp_f32 v131, v131\n"
"v_mfma_f32_16x16x32_bf16 v[104:107], v[60:63], v[228:231], v[104:107]\n"
"s_add_u32 s90, s84, s93\n"
"s_mov_b32 s72, %[sl]\n"
"v_mfma_f32_16x16x32_bf16 v[108:111], v[60:63], v[232:235], v[108:111]\n"
"s_waitcnt lgkmcnt(2) vmcnt(8)\n"
"s_barrier\n"
"v_mfma_f32_16x16x32_bf16 v[72:75], v[64:67], v[236:239], v[72:75]\n"
"ds_read_b128 v[56:59], v8 offset:16384\n"
"v_mul_f32 v128, v128, v148\n"
"v_mfma_f32_16x16x32_bf16 v[76:79], v[64:67], v[240:243], v[76:79]\n"
"s_add_u32 s91, s85, s93\n"
"v_mul_f32 v129, v129, v149\n"
"v_mfma_f32_16x16x32_bf16 v[80:83], v[64:67], v[244:247], v[80:83]\n"
"v_mul_f32 v130, v130, v150\n"
"v_mul_f32 v131, v131, v151\n"
"v_mfma_f32_16x16x32_bf16 v[84:87], v[64:67], v[248:251], v[84:87]\n"
"buffer_load_dwordx4 v5, s[56:59], s90 offen lds\n"
"v_cvt_pk_bf16_f32 v148, v128, v129\n"
"v_mfma_f32_16x16x32_bf16 v[88:91], v[64:67], v[252:255], v[88:91]\n"
"v_cvt_pk_bf16_f32 v149, v130, v131\n"
"v_mfma_f32_16x16x32_bf16 v[92:95], v[68:71], v[236:239], v[92:95]\n"
"ds_read_b128 v[60:63], v8 offset:17408\n"
"s_mul_i32 s98, %[ti], 9\n"
"v_mfma_f32_16x16x32_bf16 v[96:99], v[68:71], v[240:243], v[96:99]\n"
"s_add_u32 s98, s98, %[sl]\n"
"v_mfma_f32_16x16x32_bf16 v[100:103], v[68:71], v[244:247], v[100:103]\n"
"ds_write_b64 v11, v[148:149] offset:24576\n"
"v_mfma_f32_16x16x32_bf16 v[104:107], v[68:71], v[248:251], v[104:107]\n"
"buffer_load_dwordx4 v6, s[56:59], s90 offen offset:1024 lds\n"
"s_mul_i32 s75, s98, 92160\n"
"v_mfma_f32_16x16x32_bf16 v[108:111], v[68:71], v[252:255], v[108:111]\n"
"s_add_u32 s75, s75, 1048576\n"
"s_waitcnt lgkmcnt(2)\n"
"v_mfma_f32_16x16x32_bf16 v[112:115], v[56:59], a[0:3], 0\n"
"ds_read_b128 v[64:67], v9 offset:16384\n"
"v_mfma_f32_16x16x32_bf16 v[116:119], v[56:59], a[4:7], 0\n"
"s_lshl_b32 s76, s98, 11\n"
"v_mfma_f32_16x16x32_bf16 v[120:123], v[56:59], a[8:11], 0\n"
"buffer_load_dwordx4 v5, s[56:59], s91 offen offset:2048 lds\n"
"s_lshl_b32 s99, s95, 6\n"
"v_mfma_f32_16x16x32_bf16 v[124:127], v[56:59], a[12:15], 0\n"
"ds_read_b128 v[68:71], v9 offset:17408\n"
"v_mfma_f32_16x16x32_bf16 v[128:131], v[56:59], a[16:19], 0\n"
"s_add_u32 s76, s76, s99\n"
"s_waitcnt lgkmcnt(3)\n"
"v_mfma_f32_16x16x32_bf16 v[112:115], v[60:63], a[20:23], v[112:115]\n"
"v_subrev_u32 v11, 61440, v11\n"
"v_mul_f32 v92, v72, v92\n"
"v_exp_f32_e64 v72, -v72\n"
"v_mfma_f32_16x16x32_bf16 v[116:119], v[60:63], a[24:27], v[116:119]\n"
"buffer_load_dwordx4 v6, s[56:59], s91 offen offset:3072 lds\n"
"v_mul_f32 v93, v73, v93\n"
"v_exp_f32_e64 v73, -v73\n"
"v_mfma_f32_16x16x32_bf16 v[120:123], v[60:63], a[28:31], v[120:123]\n"
"s_add_u32 m0, s97, 16384\n"
"v_mul_f32 v94, v74, v94\n"
"v_mfma_f32_16x16x32_bf16 v[124:127], v[60:63], a[32:35], v[124:127]\n"
"s_add_u32 s90, s86, s93\n"
"v_exp_f32_e64 v74, -v74\n"
"v_mfma_f32_16x16x32_bf16 v[128:131], v[60:63], a[36:39], v[128:131]\n"
"v_mul_f32 v95, v75, v95\n"
"s_waitcnt lgkmcnt(0) vmcnt(8)\n"
"s_barrier\n"
"v_mfma_f32_16x16x32_bf16 v[112:115], v[64:67], a[40:43], v[112:115]\n"
"ds_read_b64_tr_b16 v[56:57], v10 offset:32768\n"
"ds_read_b128 v[216:219], v12 offset:20480\n"
"v_exp_f32_e64 v75, -v75\n"
"v_mfma_f32_16x16x32_bf16 v[116:119], v[64:67], a[44:47], v[116:119]\n"
"ds_read_b64_tr_b16 v[58:59], v10 offset:40960\n"
"s_add_u32 s91, s87, s93\n"
"v_add_f32 v72, 1.0, v72\n"
"v_mfma_f32_16x16x32_bf16 v[120:123], v[64:67], a[48:51], v[120:123]\n"
"ds_read_b128 v[220:223], v12 offset:21504\n"
"v_rcp_f32 v72, v72\n"
"v_add_f32 v73, 1.0, v73\n"
"v_mfma_f32_16x16x32_bf16 v[124:127], v[64:67], a[52:55], v[124:127]\n"
"buffer_load_dwordx4 v5, s[56:59], s90 offen lds\n"
"v_rcp_f32 v73, v73\n"
"v_mfma_f32_16x16x32_bf16 v[128:131], v[64:67], a[56:59], v[128:131]\n"
"ds_read_b128 v[224:227], v12 offset:22528\n"
"v_add_f32 v74, 1.0, v74\n"
"v_rcp_f32 v74, v74\n"
"v_mfma_f32_16x16x32_bf16 v[112:115], v[68:71], a[60:63], v[112:115]\n"
"ds_read_b64_tr_b16 v[60:61], v10 offset:33792\n"
"ds_read_b128 v[228:231], v12 offset:23552\n"
"v_mfma_f32_16x16x32_bf16 v[116:119], v[68:71], a[64:67], v[116:119]\n"
"ds_read_b64_tr_b16 v[62:63], v10 offset:41984\n"
"v_add_f32 v75, 1.0, v75\n"
"v_rcp_f32 v75, v75\n"
"v_mfma_f32_16x16x32_bf16 v[120:123], v[68:71], a[68:71], v[120:123]\n"
"ds_read_b128 v[232:235], v12 offset:24576\n"
"v_mul_f32 v72, v72, v92\n"
"v_mul_f32 v73, v73, v93\n"
"v_mfma_f32_16x16x32_bf16 v[124:127], v[68:71], a[72:75], v[124:127]\n"
"buffer_load_dwordx4 v6, s[56:59], s90 offen offset:1024 lds\n"
"ds_read_b128 v[236:239], v12 offset:25600\n"
"v_mul_f32 v74, v74, v94\n"
"v_mfma_f32_16x16x32_bf16 v[128:131], v[68:71], a[76:79], v[128:131]\n"
"ds_read_b128 v[240:243], v12 offset:26624\n"
"v_mul_f32 v75, v75, v95\n"
"v_cvt_pk_bf16_f32 v92, v72, v73\n"
"v_cvt_pk_bf16_f32 v93, v74, v75\n"
"s_waitcnt lgkmcnt(8)\n"
"v_mfma_f32_16x16x32_bf16 v[112:115], v[56:59], a[80:83], v[112:115]\n"
"ds_read_b64_tr_b16 v[64:65], v10 offset:34816\n"
"v_mul_f32 v96, v76, v96\n"
"v_exp_f32_e64 v76, -v76\n"
"v_mfma_f32_16x16x32_bf16 v[116:119], v[56:59], a[84:87], v[116:119]\n"
"ds_read_b64_tr_b16 v[66:67], v10 offset:43008\n"
"ds_read_b128 v[244:247], v12 offset:27648\n"
"v_mfma_f32_16x16x32_bf16 v[120:123], v[56:59], a[88:91], v[120:123]\n"
"buffer_load_dwordx4 v5, s[56:59], s91 offen offset:2048 lds\n"
"v_mul_f32 v97, v77, v97\n"
"v_exp_f32_e64 v77, -v77\n"
"v_mfma_f32_16x16x32_bf16 v[124:127], v[56:59], a[92:95], v[124:127]\n"
"ds_read_b64_tr_b16 v[68:69], v10 offset:35840\n"
"ds_read_b128 v[248:251], v12 offset:28672\n"
"v_mul_f32 v98, v78, v98\n"
"v_mfma_f32_16x16x32_bf16 v[128:131], v[56:59], a[96:99], v[128:131]\n"
"ds_read_b64_tr_b16 v[70:71], v10 offset:44032\n"
"v_exp_f32_e64 v78, -v78\n"
"s_waitcnt lgkmcnt(9)\n"
"v_mfma_f32_16x16x32_bf16 v[112:115], v[60:63], a[100:103], v[112:115]\n"
"ds_read_b128 v[252:255], v12 offset:29696\n"
"v_mul_f32 v99, v79, v99\n"
"v_exp_f32_e64 v79, -v79\n"
"v_mfma_f32_16x16x32_bf16 v[116:119], v[60:63], a[104:107], v[116:119]\n"
"buffer_load_dwordx4 v6, s[56:59], s91 offen offset:3072 lds\n"
"v_add_f32 v76, 1.0, v76\n"
"v_mfma_f32_16x16x32_bf16 v[120:123], v[60:63], a[108:111], v[120:123]\n"
"s_add_u32 m0, s97, 32768\n"
"s_add_u32 s93, s93, 128\n"
"v_rcp_f32 v76, v76\n"
"v_mfma_f32_16x16x32_bf16 v[124:127], v[60:63], a[112:115], v[124:127]\n"
"s_cmp_eq_u32 s93, 1152\n"
"s_cselect_b32 s93, 0, s93\n"
"v_add_f32 v77, 1.0, v77\n"
"v_mfma_f32_16x16x32_bf16 v[128:131], v[60:63], a[116:119], v[128:131]\n"
"v_rcp_f32 v77, v77\n"
"s_waitcnt lgkmcnt(1) vmcnt(8)\n"
"s_barrier\n"
"v_mfma_f32_16x16x32_bf16 v[112:115], v[64:67], a[120:123], v[112:115]\n"
"ds_read_b64_tr_b16 v[56:57], v10 offset:49152\n"
"v_add_f32 v78, 1.0, v78\n"
"v_rcp_f32 v78, v78\n"
"v_mfma_f32_16x16x32_bf16 v[116:119], v[64:67], a[124:127], v[116:119]\n"
"ds_read_b64_tr_b16 v[58:59], v10 offset:57344\n"
"s_mul_i32 s90, s93, 576\n"
"ds_write_b64 v11, v[92:93] offset:0\n"
"v_mfma_f32_16x16x32_bf16 v[120:123], v[64:67], a[128:131], v[120:123]\n"
"s_add_u32 s90, s90, s92\n"
"v_add_f32 v79, 1.0, v79\n"
"v_rcp_f32 v79, v79\n"
"v_mfma_f32_16x16x32_bf16 v[124:127], v[64:67], a[132:135], v[124:127]\n"
"buffer_load_dwordx4 v3, s[40:43], s90 offen lds\n"
"v_mul_f32 v76, v76, v96\n"
"v_mul_f32 v77, v77, v97\n"
"v_mfma_f32_16x16x32_bf16 v[128:131], v[64:67], a[136:139], v[128:131]\n"
"v_mul_f32 v78, v78, v98\n"
"v_mul_f32 v79, v79, v99\n"
"v_cvt_pk_bf16_f32 v96, v76, v77\n"
"v_mfma_f32_16x16x32_bf16 v[112:115], v[68:71], a[140:143], v[112:115]\n"
"ds_read_b64_tr_b16 v[60:61], v10 offset:50176\n"
"v_cvt_pk_bf16_f32 v97, v78, v79\n"
"v_mul_f32 v100, v80, v100\n"
"v_mfma_f32_16x16x32_bf16 v[116:119], v[68:71], a[144:147], v[116:119]\n"
"ds_read_b64_tr_b16 v[62:63], v10 offset:58368\n"
"v_exp_f32_e64 v80, -v80\n"
"v_mfma_f32_16x16x32_bf16 v[120:123], v[68:71], a[148:151], v[120:123]\n"
"v_mul_f32 v101, v81, v101\n"
"v_exp_f32_e64 v81, -v81\n"
"v_mfma_f32_16x16x32_bf16 v[124:127], v[68:71], a[152:155], v[124:127]\n"
"buffer_load_dwordx4 v3, s[44:47], s90 offen offset:1024 lds\n"
"v_mul_f32 v102, v82, v102\n"
"v_exp_f32_e64 v82, -v82\n"
"v_mfma_f32_16x16x32_bf16 v[128:131], v[68:71], a[156:159], v[128:131]\n"
"v_mul_f32 v103, v83, v103\n"
"v_exp_f32_e64 v83, -v83\n"
"s_waitcnt lgkmcnt(3)\n"
"v_mfma_f32_16x16x32_bf16 v[112:115], v[56:59], a[160:163], v[112:115]\n"
"ds_read_b64_tr_b16 v[64:65], v10 offset:51200\n"
"v_add_f32 v80, 1.0, v80\n"
"v_mfma_f32_16x16x32_bf16 v[116:119], v[56:59], a[164:167], v[116:119]\n"
"ds_read_b64_tr_b16 v[66:67], v10 offset:59392\n"
"v_rcp_f32 v80, v80\n"
"v_add_f32 v81, 1.0, v81\n"
"v_mfma_f32_16x16x32_bf16 v[120:123], v[56:59], a[168:171], v[120:123]\n"
"buffer_load_dwordx4 v4, s[40:43], s90 offen offset:2048 lds\n"
"v_rcp_f32 v81, v81\n"
"v_mfma_f32_16x16x32_bf16 v[124:127], v[56:59], a[172:175], v[124:127]\n"
"ds_read_b64_tr_b16 v[68:69], v10 offset:52224\n"
"v_add_f32 v82, 1.0, v82\n"
"v_rcp_f32 v82, v82\n"
"v_mfma_f32_16x16x32_bf16 v[128:131], v[56:59], a[176:179], v[128:131]\n"
"ds_read_b64_tr_b16 v[70:71], v10 offset:60416\n"
"v_add_f32 v83, 1.0, v83\n"
"s_waitcnt lgkmcnt(4)\n"
"v_mfma_f32_16x16x32_bf16 v[112:115], v[60:63], a[180:183], v[112:115]\n"
"v_rcp_f32 v83, v83\n"
"v_mul_f32 v80, v80, v100\n"
"v_mfma_f32_16x16x32_bf16 v[116:119], v[60:63], a[184:187], v[116:119]\n"
"buffer_load_dwordx4 v4, s[44:47], s90 offen offset:3072 lds\n"
"v_mul_f32 v81, v81, v101\n"
"v_mul_f32 v82, v82, v102\n"
"v_mfma_f32_16x16x32_bf16 v[120:123], v[60:63], a[188:191], v[120:123]\n"
"s_add_u32 m0, s97, 49152\n"
"v_mul_f32 v83, v83, v103\n"
"v_cvt_pk_bf16_f32 v100, v80, v81\n"
"v_cvt_pk_bf16_f32 v101, v82, v83\n"
"v_mfma_f32_16x16x32_bf16 v[124:127], v[60:63], a[192:195], v[124:127]\n"
"s_add_u32 s90, s80, s93\n"
"v_mul_f32 v104, v84, v104\n"
"v_exp_f32_e64 v84, -v84\n"
"v_mfma_f32_16x16x32_bf16 v[128:131], v[60:63], a[196:199], v[128:131]\n"
"s_waitcnt lgkmcnt(0) vmcnt(8)\n"
"s_barrier\n"
"v_mfma_f32_16x16x32_bf16 v[112:115], v[64:67], a[200:203], v[112:115]\n"
"ds_read_b64_tr_b16 v[56:57], v10 offset:0\n"
"v_mul_f32 v105, v85, v105\n"
"v_exp_f32_e64 v85, -v85\n"
"v_mfma_f32_16x16x32_bf16 v[116:119], v[64:67], a[204:207], v[116:119]\n"
"ds_read_b64_tr_b16 v[58:59], v10 offset:8192\n"
"s_add_u32 s91, s81, s93\n"
"ds_write_b64 v11, v[96:97] offset:1024\n"
"ds_write_b64 v11, v[100:101] offset:2048\n"
"v_mul_f32 v106, v86, v106\n"
"v_mfma_f32_16x16x32_bf16 v[120:123], v[64:67], a[208:211], v[120:123]\n"
"v_exp_f32_e64 v86, -v86\n"
"v_mul_f32 v107, v87, v107\n"
"v_mfma_f32_16x16x32_bf16 v[124:127], v[64:67], a[212:215], v[124:127]\n"
"buffer_load_dwordx4 v5, s[56:59], s90 offen lds\n"
"v_exp_f32_e64 v87, -v87\n"
"v_mfma_f32_16x16x32_bf16 v[128:131], v[64:67], a[216:219], v[128:131]\n"
"v_add_f32 v84, 1.0, v84\n"
"v_rcp_f32 v84, v84\n"
"v_mfma_f32_16x16x32_bf16 v[112:115], v[68:71], a[220:223], v[112:115]\n"
"ds_read_b64_tr_b16 v[60:61], v10 offset:1024\n"
"v_add_f32 v85, 1.0, v85\n"
"v_rcp_f32 v85, v85\n"
"v_mfma_f32_16x16x32_bf16 v[116:119], v[68:71], a[224:227], v[116:119]\n"
"ds_read_b64_tr_b16 v[62:63], v10 offset:9216\n"
"v_add_f32 v86, 1.0, v86\n"
"v_mfma_f32_16x16x32_bf16 v[120:123], v[68:71], a[228:231], v[120:123]\n"
"v_rcp_f32 v86, v86\n"
"v_add_f32 v87, 1.0, v87\n"
"v_mfma_f32_16x16x32_bf16 v[124:127], v[68:71], a[232:235], v[124:127]\n"
"buffer_load_dwordx4 v6, s[56:59], s90 offen offset:1024 lds\n"
"v_rcp_f32 v87, v87\n"
"v_mfma_f32_16x16x32_bf16 v[128:131], v[68:71], a[236:239], v[128:131]\n"
"v_mul_f32 v84, v84, v104\n"
"v_mul_f32 v85, v85, v105\n"
"v_mul_f32 v86, v86, v106\n"
"s_waitcnt lgkmcnt(4)\n"
"v_mfma_f32_16x16x32_bf16 v[112:115], v[56:59], a[240:243], v[112:115]\n"
"ds_read_b64_tr_b16 v[64:65], v10 offset:2048\n"
"v_mul_f32 v87, v87, v107\n"
"v_cvt_pk_bf16_f32 v104, v84, v85\n"
"v_cvt_pk_bf16_f32 v105, v86, v87\n"
"v_mfma_f32_16x16x32_bf16 v[116:119], v[56:59], a[244:247], v[116:119]\n"
"ds_read_b64_tr_b16 v[66:67], v10 offset:10240\n"
"v_mul_f32 v108, v88, v108\n"
"v_exp_f32_e64 v88, -v88\n"
"v_mfma_f32_16x16x32_bf16 v[120:123], v[56:59], a[248:251], v[120:123]\n"
"buffer_load_dwordx4 v5, s[56:59], s91 offen offset:2048 lds\n"
"v_mul_f32 v109, v89, v109\n"
"v_mfma_f32_16x16x32_bf16 v[124:127], v[56:59], a[252:255], v[124:127]\n"
"ds_read_b64_tr_b16 v[68:69], v10 offset:3072\n"
"v_exp_f32_e64 v89, -v89\n"
"v_mfma_f32_16x16x32_bf16 v[128:131], v[56:59], v[152:155], v[128:131]\n"
"ds_read_b64_tr_b16 v[70:71], v10 offset:11264\n"
"v_mul_f32 v110, v90, v110\n"
"v_exp_f32_e64 v90, -v90\n"
"s_waitcnt lgkmcnt(4)\n"
"v_mfma_f32_16x16x32_bf16 v[112:115], v[60:63], v[156:159], v[112:115]\n"
"v_mul_f32 v111, v91, v111\n"
"v_exp_f32_e64 v91, -v91\n"
"v_mfma_f32_16x16x32_bf16 v[116:119], v[60:63], v[160:163], v[116:119]\n"
"buffer_load_dwordx4 v6, s[56:59], s91 offen offset:3072 lds\n"
"v_add_f32 v88, 1.0, v88\n"
"v_rcp_f32 v88, v88\n"
"v_mfma_f32_16x16x32_bf16 v[120:123], v[60:63], v[164:167], v[120:123]\n"
"s_add_u32 m0, s97, 0\n"
"v_add_f32 v89, 1.0, v89\n"
"v_mfma_f32_16x16x32_bf16 v[124:127], v[60:63], v[168:171], v[124:127]\n"
"s_add_u32 s90, s82, s93\n"
"v_rcp_f32 v89, v89\n"
"v_mfma_f32_16x16x32_bf16 v[128:131], v[60:63], v[172:175], v[128:131]\n"
"v_add_f32 v90, 1.0, v90\n"
"s_waitcnt lgkmcnt(0) vmcnt(8)\n"
"s_barrier\n"
"v_mfma_f32_16x16x32_bf16 v[112:115], v[64:67], v[176:179], v[112:115]\n"
"ds_read_b64_tr_b16 v[56:57], v10 offset:16384\n"
"ds_write_b64 v11, v[104:105] offset:3072\n"
"v_rcp_f32 v90, v90\n"
"v_add_f32 v91, 1.0, v91\n"
"v_mfma_f32_16x16x32_bf16 v[116:119], v[64:67], v[180:183], v[116:119]\n"
"ds_read_b64_tr_b16 v[58:59], v10 offset:24576\n"
"s_add_u32 s91, s83, s93\n"
"v_rcp_f32 v91, v91\n"
"v_mfma_f32_16x16x32_bf16 v[120:123], v[64:67], v[184:187], v[120:123]\n"
"v_mul_f32 v88, v88, v108\n"
"v_mul_f32 v89, v89, v109\n"
"v_mfma_f32_16x16x32_bf16 v[124:127], v[64:67], v[188:191], v[124:127]\n"
"buffer_load_dwordx4 v5, s[56:59], s90 offen lds\n"
"v_mul_f32 v90, v90, v110\n"
"v_mul_f32 v91, v91, v111\n"
"v_mfma_f32_16x16x32_bf16 v[128:131], v[64:67], v[192:195], v[128:131]\n"
"v_cvt_pk_bf16_f32 v108, v88, v89\n"
"v_cvt_pk_bf16_f32 v109, v90, v91\n"
"ds_write_b64 v11, v[108:109] offset:4096\n"
"v_mfma_f32_16x16x32_bf16 v[112:115], v[68:71], v[196:199], v[112:115]\n"
"ds_read_b64_tr_b16 v[60:61], v10 offset:17408\n"
"s_add_u32 s76, s76, 393216\n"
"v_mfma_f32_16x16x32_bf16 v[116:119], v[68:71], v[200:203], v[116:119]\n"
"ds_read_b64_tr_b16 v[62:63], v10 offset:25600\n"
"v_mfma_f32_16x16x32_bf16 v[120:123], v[68:71], v[204:207], v[120:123]\n"
"s_mul_i32 s98, %[sl], 92160\n"
"v_mfma_f32_16x16x32_bf16 v[124:127], v[68:71], v[208:211], v[124:127]\n"
"buffer_load_dwordx4 v6, s[56:59], s90 offen offset:1024 lds\n"
"s_sub_u32 s98, s75, s98\n"
"v_mfma_f32_16x16x32_bf16 v[128:131], v[68:71], v[212:215], v[128:131]\n"
"s_lshl_b32 s99, %[sl], 11\n"
"s_waitcnt lgkmcnt(0)\n"
"s_barrier\n"
"v_mfma_f32_16x16x32_bf16 v[112:115], v[56:59], v[216:219], v[112:115]\n"
"ds_read_b64_tr_b16 v[64:65], v10 offset:18432\n"
"v_subrev_u32 v12, 61440, v12\n"
"ds_read_b128 v[72:75], v12 offset:0\n"
"v_mfma_f32_16x16x32_bf16 v[116:119], v[56:59], v[220:223], v[116:119]\n"
"ds_read_b64_tr_b16 v[66:67], v10 offset:26624\n"
"ds_read_b128 v[76:79], v12 offset:1024\n"
"ds_read_b128 v[80:83], v12 offset:2048\n"
"v_mfma_f32_16x16x32_bf16 v[120:123], v[56:59], v[224:227], v[120:123]\n"
"buffer_load_dwordx4 v5, s[56:59], s91 offen offset:2048 lds\n"
"ds_read_b128 v[84:87], v12 offset:3072\n"
"ds_read_b128 v[88:91], v12 offset:4096\n"
"v_mfma_f32_16x16x32_bf16 v[124:127], v[56:59], v[228:231], v[124:127]\n"
"ds_read_b64_tr_b16 v[68:69], v10 offset:19456\n"
"ds_read_b128 v[92:95], v12 offset:5120\n"
"ds_read_b128 v[96:99], v12 offset:6144\n"
"v_mfma_f32_16x16x32_bf16 v[128:131], v[56:59], v[232:235], v[128:131]\n"
"ds_read_b64_tr_b16 v[70:71], v10 offset:27648\n"
"ds_read_b128 v[100:103], v12 offset:7168\n"
"ds_read_b128 v[104:107], v12 offset:8192\n"
"v_mfma_f32_16x16x32_bf16 v[112:115], v[60:63], v[236:239], v[112:115]\n"
"ds_read_b128 v[108:111], v12 offset:9216\n"
"v_mfma_f32_16x16x32_bf16 v[116:119], v[60:63], v[240:243], v[116:119]\n"
"buffer_load_dwordx4 v6, s[56:59], s91 offen offset:3072 lds\n"
"s_sub_u32 s99, s76, s99\n"
"v_mfma_f32_16x16x32_bf16 v[120:123], v[60:63], v[244:247], v[120:123]\n"
"s_add_u32 m0, s97, 16384\n"
"v_mfma_f32_16x16x32_bf16 v[124:127], v[60:63], v[248:251], v[124:127]\n"
"s_add_u32 s90, s84, s93\n"
"v_mfma_f32_16x16x32_bf16 v[128:131], v[60:63], v[252:255], v[128:131]\n"
"s_waitcnt lgkmcnt(3) vmcnt(8)\n"
"s_barrier\n"
"v_mfma_f32_16x16x32_bf16 v[112:115], v[64:67], v[72:75], v[112:115]\n"
"ds_read_b128 v[56:59], v8 offset:32768\n"
"s_add_u32 s72, s72, 1\n"
"v_mfma_f32_16x16x32_bf16 v[116:119], v[64:67], v[76:79], v[116:119]\n"
"s_add_u32 s91, s85, s93\n"
"v_mfma_f32_16x16x32_bf16 v[120:123], v[64:67], v[80:83], v[120:123]\n"
"s_cmp_eq_u32 s72, 9\n"
"s_cselect_b32 s72, 0, s72\n"
"v_mfma_f32_16x16x32_bf16 v[124:127], v[64:67], v[84:87], v[124:127]\n"
"buffer_load_dwordx4 v5, s[56:59], s90 offen lds\n"
"v_mfma_f32_16x16x32_bf16 v[128:131], v[64:67], v[88:91], v[128:131]\n"
"v_mfma_f32_16x16x32_bf16 v[112:115], v[68:71], v[92:95], v[112:115]\n"
"ds_read_b128 v[60:63], v8 offset:33792\n"
"v_mfma_f32_16x16x32_bf16 v[116:119], v[68:71], v[96:99], v[116:119]\n"
"s_waitcnt lgkmcnt(4)\n"
"v_mfma_f32_16x16x32_bf16 v[120:123], v[68:71], v[100:103], v[120:123]\n"
"s_mul_i32 s73, s72, 92160\n"
"s_waitcnt lgkmcnt(3)\n"
"v_mfma_f32_16x16x32_bf16 v[124:127], v[68:71], v[104:107], v[124:127]\n"
"buffer_load_dwordx4 v6, s[56:59], s90 offen offset:1024 lds\n"
"s_waitcnt lgkmcnt(2)\n"
"v_mfma_f32_16x16x32_bf16 v[128:131], v[68:71], v[108:111], v[128:131]\n"
"s_add_u32 s73, s73, s98\n"
"s_waitcnt lgkmcnt(1)\n"
"v_mfma_f32_16x16x32_bf16 v[132:135], v[56:59], a[0:3], 0\n"
"ds_read_b128 v[64:67], v9 offset:32768\n"
"v_cvt_pk_bf16_f32 v112, v112, v113\n"
"v_mfma_f32_16x16x32_bf16 v[136:139], v[56:59], a[4:7], 0\n"
"v_cvt_pk_bf16_f32 v113, v114, v115\n"
"v_cvt_pk_bf16_f32 v114, v116, v117\n"
"v_mfma_f32_16x16x32_bf16 v[140:143], v[56:59], a[8:11], 0\n"
"buffer_load_dwordx4 v5, s[56:59], s91 offen offset:2048 lds\n"
"s_add_u32 s98, s98, 10240\n"
"v_mfma_f32_16x16x32_bf16 v[144:147], v[56:59], a[12:15], 0\n"
"ds_read_b128 v[68:71], v9 offset:33792\n"
"v_cvt_pk_bf16_f32 v115, v118, v119\n"
"v_mfma_f32_16x16x32_bf16 v[148:151], v[56:59], a[16:19], 0\n"
"v_cvt_pk_bf16_f32 v116, v120, v121\n"
"v_cvt_pk_bf16_f32 v117, v122, v123\n"
"s_waitcnt lgkmcnt(2)\n"
"v_mfma_f32_16x16x32_bf16 v[132:135], v[60:63], a[20:23], v[132:135]\n"
"v_cvt_pk_bf16_f32 v118, v124, v125\n"
"v_cvt_pk_bf16_f32 v119, v126, v127\n"
"v_mfma_f32_16x16x32_bf16 v[136:139], v[60:63], a[24:27], v[136:139]\n"
"buffer_load_dwordx4 v6, s[56:59], s91 offen offset:3072 lds\n"
"v_cvt_pk_bf16_f32 v120, v128, v129\n"
"v_mfma_f32_16x16x32_bf16 v[140:143], v[60:63], a[28:31], v[140:143]\n"
"s_add_u32 m0, s97, 32768\n"
"v_cvt_pk_bf16_f32 v121, v130, v131\n"
"v_mfma_f32_16x16x32_bf16 v[144:147], v[60:63], a[32:35], v[144:147]\n"
"s_add_u32 s90, s86, s93\n"
"v_mfma_f32_16x16x32_bf16 v[148:151], v[60:63], a[36:39], v[148:151]\n"
"buffer_store_dwordx4 v[112:115], v48, s[64:67], s73 offen sc1\n"
"s_waitcnt lgkmcnt(0) vmcnt(9)\n"
"s_barrier\n"
"v_mfma_f32_16x16x32_bf16 v[132:135], v[64:67], a[40:43], v[132:135]\n"
"ds_read_b64_tr_b16 v[56:57], v10 offset:49152\n"
"v_mfma_f32_16x16x32_bf16 v[136:139], v[64:67], a[44:47], v[136:139]\n"
"ds_read_b64_tr_b16 v[58:59], v10 offset:57344\n"
"s_add_u32 s91, s87, s93\n"
"v_mfma_f32_16x16x32_bf16 v[140:143], v[64:67], a[48:51], v[140:143]\n"
"v_mfma_f32_16x16x32_bf16 v[144:147], v[64:67], a[52:55], v[144:147]\n"
"buffer_load_dwordx4 v5, s[56:59], s90 offen lds\n"
"v_mfma_f32_16x16x32_bf16 v[148:151], v[64:67], a[56:59], v[148:151]\n"
"v_mfma_f32_16x16x32_bf16 v[132:135], v[68:71], a[60:63], v[132:135]\n"
"ds_read_b64_tr_b16 v[60:61], v10 offset:50176\n"
"v_mfma_f32_16x16x32_bf16 v[136:139], v[68:71], a[64:67], v[136:139]\n"
"ds_read_b64_tr_b16 v[62:63], v10 offset:58368\n"
"v_mfma_f32_16x16x32_bf16 v[140:143], v[68:71], a[68:71], v[140:143]\n"
"v_mfma_f32_16x16x32_bf16 v[144:147], v[68:71], a[72:75], v[144:147]\n"
"buffer_load_dwordx4 v6, s[56:59], s90 offen offset:1024 lds\n"
"v_mfma_f32_16x16x32_bf16 v[148:151], v[68:71], a[76:79], v[148:151]\n"
"buffer_store_dwordx4 v[116:119], v48, s[64:67], s73 offen offset:1024 sc1\n"
"s_waitcnt lgkmcnt(2)\n"
"v_mfma_f32_16x16x32_bf16 v[132:135], v[56:59], a[80:83], v[132:135]\n"
"ds_read_b64_tr_b16 v[64:65], v10 offset:51200\n"
"v_mfma_f32_16x16x32_bf16 v[136:139], v[56:59], a[84:87], v[136:139]\n"
"ds_read_b64_tr_b16 v[66:67], v10 offset:59392\n"
"v_mfma_f32_16x16x32_bf16 v[140:143], v[56:59], a[88:91], v[140:143]\n"
"buffer_load_dwordx4 v5, s[56:59], s91 offen offset:2048 lds\n"
"v_mfma_f32_16x16x32_bf16 v[144:147], v[56:59], a[92:95], v[144:147]\n"
"ds_read_b64_tr_b16 v[68:69], v10 offset:52224\n"
"v_mfma_f32_16x16x32_bf16 v[148:151], v[56:59], a[96:99], v[148:151]\n"
"ds_read_b64_tr_b16 v[70:71], v10 offset:60416\n"
"s_waitcnt lgkmcnt(4)\n"
"v_mfma_f32_16x16x32_bf16 v[132:135], v[60:63], a[100:103], v[132:135]\n"
"v_mfma_f32_16x16x32_bf16 v[136:139], v[60:63], a[104:107], v[136:139]\n"
"buffer_load_dwordx4 v6, s[56:59], s91 offen offset:3072 lds\n"
"v_mfma_f32_16x16x32_bf16 v[140:143], v[60:63], a[108:111], v[140:143]\n"
"s_add_u32 m0, s97, 49152\n"
"s_add_u32 s93, s93, 128\n"
"v_mfma_f32_16x16x32_bf16 v[144:147], v[60:63], a[112:115], v[144:147]\n"
"s_cmp_eq_u32 s93, 1152\n"
"s_cselect_b32 s93, 0, s93\n"
"v_mfma_f32_16x16x32_bf16 v[148:151], v[60:63], a[116:119], v[148:151]\n"
"buffer_store_dwordx2 v[120:121], v49, s[64:67], s73 offen sc1\n"
"s_waitcnt lgkmcnt(0) vmcnt(11)\n"
"s_barrier\n"
"v_mfma_f32_16x16x32_bf16 v[132:135], v[64:67], a[120:123], v[132:135]\n"
"ds_read_b64_tr_b16 v[56:57], v10 offset:0\n"
"s_lshl_b32 s74, s72, 11\n"
"v_mfma_f32_16x16x32_bf16 v[136:139], v[64:67], a[124:127], v[136:139]\n"
"ds_read_b64_tr_b16 v[58:59], v10 offset:8192\n"
"s_mul_i32 s90, s93, 576\n"
"v_mfma_f32_16x16x32_bf16 v[140:143], v[64:67], a[128:131], v[140:143]\n"
"s_add_u32 s90, s90, s92\n"
"v_mov_b32 v11, v54\n"
"v_mfma_f32_16x16x32_bf16 v[144:147], v[64:67], a[132:135], v[144:147]\n"
"buffer_load_dwordx4 v3, s[40:43], s90 offen lds\n"
"v_mfma_f32_16x16x32_bf16 v[148:151], v[64:67], a[136:139], v[148:151]\n"
"v_and_b32 v50, 1, v0\n"
"v_mfma_f32_16x16x32_bf16 v[132:135], v[68:71], a[140:143], v[132:135]\n"
"ds_read_b64_tr_b16 v[60:61], v10 offset:1024\n"
"v_mfma_f32_16x16x32_bf16 v[136:139], v[68:71], a[144:147], v[136:139]\n"
"ds_read_b64_tr_b16 v[62:63], v10 offset:9216\n"
"v_mfma_f32_16x16x32_bf16 v[140:143], v[68:71], a[148:151], v[140:143]\n"
"v_lshlrev_b32 v50, 4, v50\n"
"v_mfma_f32_16x16x32_bf16 v[144:147], v[68:71], a[152:155], v[144:147]\n"
"buffer_load_dwordx4 v3, s[44:47], s90 offen offset:1024 lds\n"
"v_mfma_f32_16x16x32_bf16 v[148:151], v[68:71], a[156:159], v[148:151]\n"
"s_add_u32 s74, s74, s99\n"
"v_mov_b32 v51, 0x3f80\n"
"s_waitcnt lgkmcnt(2)\n"
"v_mfma_f32_16x16x32_bf16 v[132:135], v[56:59], a[160:163], v[132:135]\n"
"ds_read_b64_tr_b16 v[64:65], v10 offset:2048\n"
"v_mfma_f32_16x16x32_bf16 v[136:139], v[56:59], a[164:167], v[136:139]\n"
"ds_read_b64_tr_b16 v[66:67], v10 offset:10240\n"
"v_mfma_f32_16x16x32_bf16 v[140:143], v[56:59], a[168:171], v[140:143]\n"
"buffer_load_dwordx4 v4, s[40:43], s90 offen offset:2048 lds\n"
"v_lshlrev_b32 v51, v50, v51\n"
"v_mfma_f32_16x16x32_bf16 v[144:147], v[56:59], a[172:175], v[144:147]\n"
"ds_read_b64_tr_b16 v[68:69], v10 offset:3072\n"
"v_mfma_f32_16x16x32_bf16 v[148:151], v[56:59], a[176:179], v[148:151]\n"
"ds_read_b64_tr_b16 v[70:71], v10 offset:11264\n"
"s_waitcnt lgkmcnt(4)\n"
"v_mfma_f32_16x16x32_bf16 v[132:135], v[60:63], a[180:183], v[132:135]\n"
"v_lshrrev_b32 v50, 4, v0\n"
"v_mfma_f32_16x16x32_bf16 v[136:139], v[60:63], a[184:187], v[136:139]\n"
"buffer_load_dwordx4 v4, s[44:47], s90 offen offset:3072 lds\n"
"v_mfma_f32_16x16x32_bf16 v[140:143], v[60:63], a[188:191], v[140:143]\n"
"s_add_u32 m0, s97, 0\n"
"v_mfma_f32_16x16x32_bf16 v[144:147], v[60:63], a[192:195], v[144:147]\n"
"s_add_u32 s90, s80, s93\n"
"v_lshrrev_b32 v54, 2, v0\n"
"v_mfma_f32_16x16x32_bf16 v[148:151], v[60:63], a[196:199], v[148:151]\n"
"s_waitcnt lgkmcnt(0) vmcnt(11)\n"
"s_barrier\n"
"v_mfma_f32_16x16x32_bf16 v[132:135], v[64:67], a[200:203], v[132:135]\n"
"ds_read_b64_tr_b16 v[56:57], v10 offset:16384\n"
"s_add_u32 s99, s99, 256\n"
"v_mfma_f32_16x16x32_bf16 v[136:139], v[64:67], a[204:207], v[136:139]\n"
"ds_read_b64_tr_b16 v[58:59], v10 offset:24576\n"
"s_add_u32 s91, s81, s93\n"
"v_mfma_f32_16x16x32_bf16 v[140:143], v[64:67], a[208:211], v[140:143]\n"
"v_mfma_f32_16x16x32_bf16 v[144:147], v[64:67], a[212:215], v[144:147]\n"
"buffer_load_dwordx4 v5, s[56:59], s90 offen lds\n"
"v_xor_b32 v50, v50, v54\n"
"v_mfma_f32_16x16x32_bf16 v[148:151], v[64:67], a[216:219], v[148:151]\n"
"v_mfma_f32_16x16x32_bf16 v[132:135], v[68:71], a[220:223], v[132:135]\n"
"ds_read_b64_tr_b16 v[60:61], v10 offset:17408\n"
"v_and_b32 v50, 3, v50\n"
"v_mfma_f32_16x16x32_bf16 v[136:139], v[68:71], a[224:227], v[136:139]\n"
"ds_read_b64_tr_b16 v[62:63], v10 offset:25600\n"
"v_mfma_f32_16x16x32_bf16 v[140:143], v[68:71], a[228:231], v[140:143]\n"
"v_and_b32 v54, 2, v0\n"
"v_mfma_f32_16x16x32_bf16 v[144:147], v[68:71], a[232:235], v[144:147]\n"
"buffer_load_dwordx4 v6, s[56:59], s90 offen offset:1024 lds\n"
"v_mfma_f32_16x16x32_bf16 v[148:151], v[68:71], a[236:239], v[148:151]\n"
"s_waitcnt lgkmcnt(2)\n"
"v_mfma_f32_16x16x32_bf16 v[132:135], v[56:59], a[240:243], v[132:135]\n"
"ds_read_b64_tr_b16 v[64:65], v10 offset:18432\n"
"v_lshl_add_u32 v50, v50, 2, v54\n"
"v_mfma_f32_16x16x32_bf16 v[136:139], v[56:59], a[244:247], v[136:139]\n"
"ds_read_b64_tr_b16 v[66:67], v10 offset:26624\n"
"v_mfma_f32_16x16x32_bf16 v[140:143], v[56:59], a[248:251], v[140:143]\n"
"buffer_load_dwordx4 v5, s[56:59], s91 offen offset:2048 lds\n"
"v_mfma_f32_16x16x32_bf16 v[144:147], v[56:59], a[252:255], v[144:147]\n"
"ds_read_b64_tr_b16 v[68:69], v10 offset:19456\n"
"v_cmp_eq_u32 vcc, 0, v50\n"
"v_mfma_f32_16x16x32_bf16 v[148:151], v[56:59], v[152:155], v[148:151]\n"
"ds_read_b64_tr_b16 v[70:71], v10 offset:27648\n"
"s_waitcnt lgkmcnt(4)\n"
"v_mfma_f32_16x16x32_bf16 v[132:135], v[60:63], v[156:159], v[132:135]\n"
"v_mov_b32 v54, 0\n"
"v_mfma_f32_16x16x32_bf16 v[136:139], v[60:63], v[160:163], v[136:139]\n"
"buffer_load_dwordx4 v6, s[56:59], s91 offen offset:3072 lds\n"
"v_mfma_f32_16x16x32_bf16 v[140:143], v[60:63], v[164:167], v[140:143]\n"
"s_add_u32 m0, s97, 16384\n"
"v_mfma_f32_16x16x32_bf16 v[144:147], v[60:63], v[168:171], v[144:147]\n"
"s_add_u32 s90, s82, s93\n"
"v_cndmask_b32 v52, v54, v51, vcc\n"
"v_mfma_f32_16x16x32_bf16 v[148:151], v[60:63], v[172:175], v[148:151]\n"
"s_waitcnt lgkmcnt(0) vmcnt(9)\n"
"s_barrier\n"
"v_mfma_f32_16x16x32_bf16 v[132:135], v[64:67], v[176:179], v[132:135]\n"
"ds_read_b64_tr_b16 v[56:57], v10 offset:32768\n"
"v_mfma_f32_16x16x32_bf16 v[136:139], v[64:67], v[180:183], v[136:139]\n"
"ds_read_b64_tr_b16 v[58:59], v10 offset:40960\n"
"s_add_u32 s91, s83, s93\n"
"v_mfma_f32_16x16x32_bf16 v[140:143], v[64:67], v[184:187], v[140:143]\n"
"s_add_u32 s72, s72, 1\n"
"v_mfma_f32_16x16x32_bf16 v[144:147], v[64:67], v[188:191], v[144:147]\n"
"buffer_load_dwordx4 v5, s[56:59], s90 offen lds\n"
"v_cmp_eq_u32 vcc, 2, v50\n"
"v_mfma_f32_16x16x32_bf16 v[148:151], v[64:67], v[192:195], v[148:151]\n"
"v_mfma_f32_16x16x32_bf16 v[132:135], v[68:71], v[196:199], v[132:135]\n"
"ds_read_b64_tr_b16 v[60:61], v10 offset:33792\n"
"v_mfma_f32_16x16x32_bf16 v[136:139], v[68:71], v[200:203], v[136:139]\n"
"ds_read_b64_tr_b16 v[62:63], v10 offset:41984\n"
"v_mov_b32 v55, 0\n"
"v_mfma_f32_16x16x32_bf16 v[140:143], v[68:71], v[204:207], v[140:143]\n"
"s_cmp_eq_u32 s72, 9\n"
"s_cselect_b32 s72, 0, s72\n"
"v_mfma_f32_16x16x32_bf16 v[144:147], v[68:71], v[208:211], v[144:147]\n"
"buffer_load_dwordx4 v6, s[56:59], s90 offen offset:1024 lds\n"
"v_mfma_f32_16x16x32_bf16 v[148:151], v[68:71], v[212:215], v[148:151]\n"
"v_cndmask_b32 v53, v54, v51, vcc\n"
"s_waitcnt lgkmcnt(2)\n"
"v_mfma_f32_16x16x32_bf16 v[132:135], v[56:59], v[216:219], v[132:135]\n"
"ds_read_b64_tr_b16 v[64:65], v10 offset:34816\n"
"v_mfma_f32_16x16x32_bf16 v[136:139], v[56:59], v[220:223], v[136:139]\n"
"ds_read_b64_tr_b16 v[66:67], v10 offset:43008\n"
"v_mfma_f32_16x16x32_bf16 v[140:143], v[56:59], v[224:227], v[140:143]\n"
"buffer_load_dwordx4 v5, s[56:59], s91 offen offset:2048 lds\n"
"v_mov_b32 v50, 0\n"
"v_mfma_f32_16x16x32_bf16 v[144:147], v[56:59], v[228:231], v[144:147]\n"
"ds_read_b64_tr_b16 v[68:69], v10 offset:35840\n"
"v_mfma_f32_16x16x32_bf16 v[148:151], v[56:59], v[232:235], v[148:151]\n"
"ds_read_b64_tr_b16 v[70:71], v10 offset:44032\n"
"s_waitcnt lgkmcnt(4)\n"
"v_mfma_f32_16x16x32_bf16 v[132:135], v[60:63], v[236:239], v[132:135]\n"
"v_mov_b32 v51, 0\n"
"v_mfma_f32_16x16x32_bf16 v[136:139], v[60:63], v[240:243], v[136:139]\n"
"buffer_load_dwordx4 v6, s[56:59], s91 offen offset:3072 lds\n"
"v_mfma_f32_16x16x32_bf16 v[140:143], v[60:63], v[244:247], v[140:143]\n"
"s_add_u32 m0, s97, 32768\n"
"v_mfma_f32_16x16x32_bf16 v[144:147], v[60:63], v[248:251], v[144:147]\n"
"s_add_u32 s90, s84, s93\n"
"s_mul_i32 s73, s72, 92160\n"
"v_mfma_f32_16x16x32_bf16 v[148:151], v[60:63], v[252:255], v[148:151]\n"
"s_waitcnt lgkmcnt(0) vmcnt(8)\n"
"s_barrier\n"
"v_mfma_f32_16x16x32_bf16 v[132:135], v[64:67], v[72:75], v[132:135]\n"
"ds_read_b128 v[56:59], v8 offset:49152\n"
"v_mfma_f32_16x16x32_bf16 v[136:139], v[64:67], v[76:79], v[136:139]\n"
"s_add_u32 s91, s85, s93\n"
"buffer_store_dwordx2 v[46:47], off, s[64:67], s74 sc1\n"
"v_mfma_f32_16x16x32_bf16 v[140:143], v[64:67], v[80:83], v[140:143]\n"
"v_mfma_f32_16x16x32_bf16 v[144:147], v[64:67], v[84:87], v[144:147]\n"
"buffer_load_dwordx4 v5, s[56:59], s90 offen lds\n"
"v_mfma_f32_16x16x32_bf16 v[148:151], v[64:67], v[88:91], v[148:151]\n"
"v_mfma_f32_16x16x32_bf16 v[132:135], v[68:71], v[92:95], v[132:135]\n"
"ds_read_b128 v[60:63], v8 offset:50176\n"
"s_add_u32 s73, s73, s98\n"
"v_mfma_f32_16x16x32_bf16 v[136:139], v[68:71], v[96:99], v[136:139]\n"
"v_mfma_f32_16x16x32_bf16 v[140:143], v[68:71], v[100:103], v[140:143]\n"
"v_mfma_f32_16x16x32_bf16 v[144:147], v[68:71], v[104:107], v[144:147]\n"
"buffer_load_dwordx4 v6, s[56:59], s90 offen offset:1024 lds\n"
"v_mfma_f32_16x16x32_bf16 v[148:151], v[68:71], v[108:111], v[148:151]\n"
"s_add_u32 s98, s98, 10240\n"
"s_waitcnt lgkmcnt(1)\n"
"v_mfma_f32_16x16x32_bf16 v[112:115], v[56:59], a[0:3], 0\n"
"ds_read_b128 v[64:67], v9 offset:49152\n"
"v_cvt_pk_bf16_f32 v132, v132, v133\n"
"v_mfma_f32_16x16x32_bf16 v[116:119], v[56:59], a[4:7], 0\n"
"v_cvt_pk_bf16_f32 v133, v134, v135\n"
"v_cvt_pk_bf16_f32 v134, v136, v137\n"
"v_mfma_f32_16x16x32_bf16 v[120:123], v[56:59], a[8:11], 0\n"
"buffer_load_dwordx4 v5, s[56:59], s91 offen offset:2048 lds\n"
"v_cvt_pk_bf16_f32 v135, v138, v139\n"
"v_mfma_f32_16x16x32_bf16 v[124:127], v[56:59], a[12:15], 0\n"
"ds_read_b128 v[68:71], v9 offset:50176\n"
"v_cvt_pk_bf16_f32 v136, v140, v141\n"
"v_mfma_f32_16x16x32_bf16 v[128:131], v[56:59], a[16:19], 0\n"
"v_cvt_pk_bf16_f32 v137, v142, v143\n"
"s_waitcnt lgkmcnt(2)\n"
"v_mfma_f32_16x16x32_bf16 v[112:115], v[60:63], a[20:23], v[112:115]\n"
"v_cvt_pk_bf16_f32 v138, v144, v145\n"
"v_cvt_pk_bf16_f32 v139, v146, v147\n"
"v_mfma_f32_16x16x32_bf16 v[116:119], v[60:63], a[24:27], v[116:119]\n"
"buffer_load_dwordx4 v6, s[56:59], s91 offen offset:3072 lds\n"
"v_cvt_pk_bf16_f32 v140, v148, v149\n"
"v_mfma_f32_16x16x32_bf16 v[120:123], v[60:63], a[28:31], v[120:123]\n"
"s_add_u32 m0, s97, 49152\n"
"v_cvt_pk_bf16_f32 v141, v150, v151\n"
"v_mfma_f32_16x16x32_bf16 v[124:127], v[60:63], a[32:35], v[124:127]\n"
"s_add_u32 s90, s86, s93\n"
"v_mfma_f32_16x16x32_bf16 v[128:131], v[60:63], a[36:39], v[128:131]\n"
"buffer_store_dwordx4 v[132:135], v48, s[64:67], s73 offen sc1\n"
"s_waitcnt lgkmcnt(0) vmcnt(10)\n"
"s_barrier\n"
"v_mfma_f32_16x16x32_bf16 v[112:115], v[64:67], a[40:43], v[112:115]\n"
"ds_read_b64_tr_b16 v[56:57], v10 offset:0\n"
"v_and_b32 v12, 15, v0\n"
"v_mfma_f32_16x16x32_bf16 v[116:119], v[64:67], a[44:47], v[116:119]\n"
"ds_read_b64_tr_b16 v[58:59], v10 offset:8192\n"
"s_add_u32 s91, s87, s93\n"
"v_mfma_f32_16x16x32_bf16 v[120:123], v[64:67], a[48:51], v[120:123]\n"
"v_mfma_f32_16x16x32_bf16 v[124:127], v[64:67], a[52:55], v[124:127]\n"
"buffer_load_dwordx4 v5, s[56:59], s90 offen lds\n"
"v_bfe_u32 v1, v0, 1, 2\n"
"v_mfma_f32_16x16x32_bf16 v[128:131], v[64:67], a[56:59], v[128:131]\n"
"v_mfma_f32_16x16x32_bf16 v[112:115], v[68:71], a[60:63], v[112:115]\n"
"ds_read_b64_tr_b16 v[60:61], v10 offset:1024\n"
"v_lshrrev_b32 v2, 4, v0\n"
"v_mfma_f32_16x16x32_bf16 v[116:119], v[68:71], a[64:67], v[116:119]\n"
"ds_read_b64_tr_b16 v[62:63], v10 offset:9216\n"
"v_mfma_f32_16x16x32_bf16 v[120:123], v[68:71], a[68:71], v[120:123]\n"
"v_mfma_f32_16x16x32_bf16 v[124:127], v[68:71], a[72:75], v[124:127]\n"
"buffer_load_dwordx4 v6, s[56:59], s90 offen offset:1024 lds\n"
"v_xor_b32 v1, v1, v2\n"
"v_mfma_f32_16x16x32_bf16 v[128:131], v[68:71], a[76:79], v[128:131]\n"
"buffer_store_dwordx4 v[136:139], v48, s[64:67], s73 offen offset:1024 sc1\n"
"s_waitcnt lgkmcnt(2)\n"
"v_mfma_f32_16x16x32_bf16 v[112:115], v[56:59], a[80:83], v[112:115]\n"
"ds_read_b64_tr_b16 v[64:65], v10 offset:2048\n"
"v_mfma_f32_16x16x32_bf16 v[116:119], v[56:59], a[84:87], v[116:119]\n"
"ds_read_b64_tr_b16 v[66:67], v10 offset:10240\n"
"v_lshlrev_b32 v1, 4, v1\n"
"v_mfma_f32_16x16x32_bf16 v[120:123], v[56:59], a[88:91], v[120:123]\n"
"buffer_load_dwordx4 v5, s[56:59], s91 offen offset:2048 lds\n"
"v_mfma_f32_16x16x32_bf16 v[124:127], v[56:59], a[92:95], v[124:127]\n"
"ds_read_b64_tr_b16 v[68:69], v10 offset:3072\n"
"v_mfma_f32_16x16x32_bf16 v[128:131], v[56:59], a[96:99], v[128:131]\n"
"ds_read_b64_tr_b16 v[70:71], v10 offset:11264\n"
"v_lshl_add_u32 v1, v12, 6, v1\n"
"s_waitcnt lgkmcnt(4)\n"
"v_mfma_f32_16x16x32_bf16 v[112:115], v[60:63], a[100:103], v[112:115]\n"
"v_mfma_f32_16x16x32_bf16 v[116:119], v[60:63], a[104:107], v[116:119]\n"
"buffer_load_dwordx4 v6, s[56:59], s91 offen offset:3072 lds\n"
"v_mfma_f32_16x16x32_bf16 v[120:123], v[60:63], a[108:111], v[120:123]\n"
"s_add_u32 m0, s97, 0\n"
"s_add_u32 s93, s93, 128\n"
"v_mfma_f32_16x16x32_bf16 v[124:127], v[60:63], a[112:115], v[124:127]\n"
"s_cmp_eq_u32 s93, 1152\n"
"s_cselect_b32 s93, 0, s93\n"
"v_mfma_f32_16x16x32_bf16 v[128:131], v[60:63], a[116:119], v[128:131]\n"
"buffer_store_dwordx2 v[140:141], v49, s[64:67], s73 offen sc1\n"
"s_waitcnt lgkmcnt(0) vmcnt(12)\n"
"s_barrier\n"
"v_mfma_f32_16x16x32_bf16 v[112:115], v[64:67], a[120:123], v[112:115]\n"
"ds_read_b64_tr_b16 v[56:57], v10 offset:16384\n"
"s_lshl_b32 s74, s72, 11\n"
"v_mfma_f32_16x16x32_bf16 v[116:119], v[64:67], a[124:127], v[116:119]\n"
"ds_read_b64_tr_b16 v[58:59], v10 offset:24576\n"
"s_mul_i32 s90, s93, 576\n"
"v_mfma_f32_16x16x32_bf16 v[120:123], v[64:67], a[128:131], v[120:123]\n"
"s_add_u32 s90, s90, s92\n"
"v_mul_u32_u24 v11, 20, v2\n"
"v_mfma_f32_16x16x32_bf16 v[124:127], v[64:67], a[132:135], v[124:127]\n"
"buffer_load_dwordx4 v3, s[40:43], s90 offen lds\n"
"v_mfma_f32_16x16x32_bf16 v[128:131], v[64:67], a[136:139], v[128:131]\n"
"v_bfe_u32 v12, v0, 2, 2\n"
"v_mfma_f32_16x16x32_bf16 v[112:115], v[68:71], a[140:143], v[112:115]\n"
"ds_read_b64_tr_b16 v[60:61], v10 offset:17408\n"
"v_mfma_f32_16x16x32_bf16 v[116:119], v[68:71], a[144:147], v[116:119]\n"
"ds_read_b64_tr_b16 v[62:63], v10 offset:25600\n"
"v_mfma_f32_16x16x32_bf16 v[120:123], v[68:71], a[148:151], v[120:123]\n"
"v_add_u32 v11, v11, v12\n"
"v_mfma_f32_16x16x32_bf16 v[124:127], v[68:71], a[152:155], v[124:127]\n"
"buffer_load_dwordx4 v3, s[44:47], s90 offen offset:1024 lds\n"
"v_mfma_f32_16x16x32_bf16 v[128:131], v[68:71], a[156:159], v[128:131]\n"
"s_add_u32 s74, s74, s99\n"
"s_waitcnt lgkmcnt(2)\n"
"v_mfma_f32_16x16x32_bf16 v[112:115], v[56:59], a[160:163], v[112:115]\n"
"ds_read_b64_tr_b16 v[64:65], v10 offset:18432\n"
"v_mul_u32_u24 v11, 2304, v11\n"
"v_mfma_f32_16x16x32_bf16 v[116:119], v[56:59], a[164:167], v[116:119]\n"
"ds_read_b64_tr_b16 v[66:67], v10 offset:26624\n"
"v_mfma_f32_16x16x32_bf16 v[120:123], v[56:59], a[168:171], v[120:123]\n"
"buffer_load_dwordx4 v4, s[40:43], s90 offen offset:2048 lds\n"
"v_mfma_f32_16x16x32_bf16 v[124:127], v[56:59], a[172:175], v[124:127]\n"
"ds_read_b64_tr_b16 v[68:69], v10 offset:19456\n"
"v_and_b32 v12, 3, v0\n"
"v_mfma_f32_16x16x32_bf16 v[128:131], v[56:59], a[176:179], v[128:131]\n"
"ds_read_b64_tr_b16 v[70:71], v10 offset:27648\n"
"s_waitcnt lgkmcnt(4)\n"
"v_mfma_f32_16x16x32_bf16 v[112:115], v[60:63], a[180:183], v[112:115]\n"
"v_lshl_add_u32 v11, v12, 4, v11\n"
"v_mfma_f32_16x16x32_bf16 v[116:119], v[60:63], a[184:187], v[116:119]\n"
"buffer_load_dwordx4 v4, s[44:47], s90 offen offset:3072 lds\n"
"v_mfma_f32_16x16x32_bf16 v[120:123], v[60:63], a[188:191], v[120:123]\n"
"s_add_u32 m0, s97, 16384\n"
"v_mfma_f32_16x16x32_bf16 v[124:127], v[60:63], a[192:195], v[124:127]\n"
"s_add_u32 s90, s80, s93\n"
"v_mfma_f32_16x16x32_bf16 v[128:131], v[60:63], a[196:199], v[128:131]\n"
"s_waitcnt lgkmcnt(0) vmcnt(11)\n"
"s_barrier\n"
"v_mfma_f32_16x16x32_bf16 v[112:115], v[64:67], a[200:203], v[112:115]\n"
"ds_read_b64_tr_b16 v[56:57], v10 offset:32768\n"
"s_add_u32 s99, s99, 256\n"
"v_mfma_f32_16x16x32_bf16 v[116:119], v[64:67], a[204:207], v[116:119]\n"
"ds_read_b64_tr_b16 v[58:59], v10 offset:40960\n"
"s_add_u32 s91, s81, s93\n"
"v_mfma_f32_16x16x32_bf16 v[120:123], v[64:67], a[208:211], v[120:123]\n"
"v_lshl_add_u32 v11, %[cs], 8, v11\n"
"v_mfma_f32_16x16x32_bf16 v[124:127], v[64:67], a[212:215], v[124:127]\n"
"buffer_load_dwordx4 v5, s[56:59], s90 offen lds\n"
"v_mfma_f32_16x16x32_bf16 v[128:131], v[64:67], a[216:219], v[128:131]\n"
"v_lshl_add_u32 v11, s95, 6, v11\n"
"v_mfma_f32_16x16x32_bf16 v[112:115], v[68:71], a[220:223], v[112:115]\n"
"ds_read_b64_tr_b16 v[60:61], v10 offset:33792\n"
"v_mfma_f32_16x16x32_bf16 v[116:119], v[68:71], a[224:227], v[116:119]\n"
"ds_read_b64_tr_b16 v[62:63], v10 offset:41984\n"
"v_bfe_u32 v2, v0, 3, 2\n"
"v_mfma_f32_16x16x32_bf16 v[120:123], v[68:71], a[228:231], v[120:123]\n"
"v_mfma_f32_16x16x32_bf16 v[124:127], v[68:71], a[232:235], v[124:127]\n"
"buffer_load_dwordx4 v6, s[56:59], s90 offen offset:1024 lds\n"
"v_mfma_f32_16x16x32_bf16 v[128:131], v[68:71], a[236:239], v[128:131]\n"
"v_xor_b32 v2, v2, v12\n"
"s_waitcnt lgkmcnt(2)\n"
"v_mfma_f32_16x16x32_bf16 v[112:115], v[56:59], a[240:243], v[112:115]\n"
"ds_read_b64_tr_b16 v[64:65], v10 offset:34816\n"
"v_mfma_f32_16x16x32_bf16 v[116:119], v[56:59], a[244:247], v[116:119]\n"
"ds_read_b64_tr_b16 v[66:67], v10 offset:43008\n"
"v_lshlrev_b32 v2, 4, v2\n"
"v_mfma_f32_16x16x32_bf16 v[120:123], v[56:59], a[248:251], v[120:123]\n"
"buffer_load_dwordx4 v5, s[56:59], s91 offen offset:2048 lds\n"
"v_mfma_f32_16x16x32_bf16 v[124:127], v[56:59], a[252:255], v[124:127]\n"
"ds_read_b64_tr_b16 v[68:69], v10 offset:35840\n"
"v_mfma_f32_16x16x32_bf16 v[128:131], v[56:59], v[152:155], v[128:131]\n"
"ds_read_b64_tr_b16 v[70:71], v10 offset:44032\n"
"v_lshrrev_b32 v12, 2, v0\n"
"s_waitcnt lgkmcnt(4)\n"
"v_mfma_f32_16x16x32_bf16 v[112:115], v[60:63], v[156:159], v[112:115]\n"
"v_mfma_f32_16x16x32_bf16 v[116:119], v[60:63], v[160:163], v[116:119]\n"
"buffer_load_dwordx4 v6, s[56:59], s91 offen offset:3072 lds\n"
"v_mfma_f32_16x16x32_bf16 v[120:123], v[60:63], v[164:167], v[120:123]\n"
"s_add_u32 m0, s97, 32768\n"
"v_lshl_add_u32 v2, v12, 6, v2\n"
"v_mfma_f32_16x16x32_bf16 v[124:127], v[60:63], v[168:171], v[124:127]\n"
"s_add_u32 s90, s82, s93\n"
"v_mfma_f32_16x16x32_bf16 v[128:131], v[60:63], v[172:175], v[128:131]\n"
"s_waitcnt lgkmcnt(0) vmcnt(9)\n"
"s_barrier\n"
"v_mfma_f32_16x16x32_bf16 v[112:115], v[64:67], v[176:179], v[112:115]\n"
"ds_read_b64_tr_b16 v[56:57], v10 offset:49152\n"
"v_mfma_f32_16x16x32_bf16 v[116:119], v[64:67], v[180:183], v[116:119]\n"
"ds_read_b64_tr_b16 v[58:59], v10 offset:57344\n"
"s_add_u32 s91, s83, s93\n"
"v_mfma_f32_16x16x32_bf16 v[120:123], v[64:67], v[184:187], v[120:123]\n"
"s_add_u32 s72, s72, 1\n"
"s_add_u32 s77, s76, 0\n"
"buffer_load_dwordx2 v[44:45], off, s[64:67], s77 sc1\n"
"v_mfma_f32_16x16x32_bf16 v[124:127], v[64:67], v[188:191], v[124:127]\n"
"buffer_load_dwordx4 v5, s[56:59], s90 offen lds\n"
"v_mov_b32 v12, 5120\n"
"v_mfma_f32_16x16x32_bf16 v[128:131], v[64:67], v[192:195], v[128:131]\n"
"v_mfma_f32_16x16x32_bf16 v[112:115], v[68:71], v[196:199], v[112:115]\n"
"ds_read_b64_tr_b16 v[60:61], v10 offset:50176\n"
"v_mul_u32_u24 v12, s95, v12\n"
"v_mfma_f32_16x16x32_bf16 v[116:119], v[68:71], v[200:203], v[116:119]\n"
"ds_read_b64_tr_b16 v[62:63], v10 offset:58368\n"
"v_mfma_f32_16x16x32_bf16 v[120:123], v[68:71], v[204:207], v[120:123]\n"
"s_cmp_eq_u32 s72, 9\n"
"s_cselect_b32 s72, 0, s72\n"
"v_mfma_f32_16x16x32_bf16 v[124:127], v[68:71], v[208:211], v[124:127]\n"
"buffer_load_dwordx4 v6, s[56:59], s90 offen offset:1024 lds\n"
"v_mfma_f32_16x16x32_bf16 v[128:131], v[68:71], v[212:215], v[128:131]\n"
"v_add_u32 v12, 65536, v12\n"
"s_waitcnt lgkmcnt(2)\n"
"v_mfma_f32_16x16x32_bf16 v[112:115], v[56:59], v[216:219], v[112:115]\n"
"ds_read_b64_tr_b16 v[64:65], v10 offset:51200\n"
"v_mfma_f32_16x16x32_bf16 v[116:119], v[56:59], v[220:223], v[116:119]\n"
"ds_read_b64_tr_b16 v[66:67], v10 offset:59392\n"
"v_mfma_f32_16x16x32_bf16 v[120:123], v[56:59], v[224:227], v[120:123]\n"
"buffer_load_dwordx4 v5, s[56:59], s91 offen offset:2048 lds\n"
"v_add_u32 v1, v1, v12\n"
"v_mfma_f32_16x16x32_bf16 v[124:127], v[56:59], v[228:231], v[124:127]\n"
"ds_read_b64_tr_b16 v[68:69], v10 offset:52224\n"
"v_mfma_f32_16x16x32_bf16 v[128:131], v[56:59], v[232:235], v[128:131]\n"
"ds_read_b64_tr_b16 v[70:71], v10 offset:60416\n"
"s_waitcnt lgkmcnt(4)\n"
"v_mfma_f32_16x16x32_bf16 v[112:115], v[60:63], v[236:239], v[112:115]\n"
"v_add_u32 v2, v2, v12\n"
"v_mfma_f32_16x16x32_bf16 v[116:119], v[60:63], v[240:243], v[116:119]\n"
"buffer_load_dwordx4 v6, s[56:59], s91 offen offset:3072 lds\n"
"v_mfma_f32_16x16x32_bf16 v[120:123], v[60:63], v[244:247], v[120:123]\n"
"s_add_u32 m0, s97, 49152\n"
"v_mfma_f32_16x16x32_bf16 v[124:127], v[60:63], v[248:251], v[124:127]\n"
"s_add_u32 s90, s84, s93\n"
"s_mul_i32 s73, s72, 92160\n"
"v_mfma_f32_16x16x32_bf16 v[128:131], v[60:63], v[252:255], v[128:131]\n"
"s_waitcnt lgkmcnt(0) vmcnt(9)\n"
"s_barrier\n"
"v_mfma_f32_16x16x32_bf16 v[112:115], v[64:67], v[72:75], v[112:115]\n"
"ds_read_b128 v[56:59], v8 offset:0\n"
"v_mfma_f32_16x16x32_bf16 v[116:119], v[64:67], v[76:79], v[116:119]\n"
"s_add_u32 s91, s85, s93\n"
"buffer_store_dwordx2 v[46:47], off, s[64:67], s74 sc1\n"
"v_mfma_f32_16x16x32_bf16 v[120:123], v[64:67], v[80:83], v[120:123]\n"
"v_mfma_f32_16x16x32_bf16 v[124:127], v[64:67], v[84:87], v[124:127]\n"
"buffer_load_dwordx4 v5, s[56:59], s90 offen lds\n"
"v_mfma_f32_16x16x32_bf16 v[128:131], v[64:67], v[88:91], v[128:131]\n"
"v_mfma_f32_16x16x32_bf16 v[112:115], v[68:71], v[92:95], v[112:115]\n"
"ds_read_b128 v[60:63], v8 offset:1024\n"
"s_add_u32 s73, s73, s98\n"
"v_mfma_f32_16x16x32_bf16 v[116:119], v[68:71], v[96:99], v[116:119]\n"
"v_mfma_f32_16x16x32_bf16 v[120:123], v[68:71], v[100:103], v[120:123]\n"
"v_mfma_f32_16x16x32_bf16 v[124:127], v[68:71], v[104:107], v[124:127]\n"
"buffer_load_dwordx4 v6, s[56:59], s90 offen offset:1024 lds\n"
"v_mfma_f32_16x16x32_bf16 v[128:131], v[68:71], v[108:111], v[128:131]\n"
"s_add_u32 s98, s98, 10240\n"
"s_waitcnt lgkmcnt(1)\n"
"v_mfma_f32_16x16x32_bf16 v[132:135], v[56:59], a[0:3], 0\n"
"ds_read_b128 v[64:67], v9 offset:0\n"
"v_cvt_pk_bf16_f32 v112, v112, v113\n"
"v_mfma_f32_16x16x32_bf16 v[136:139], v[56:59], a[4:7], 0\n"
"v_cvt_pk_bf16_f32 v113, v114, v115\n"
"v_cvt_pk_bf16_f32 v114, v116, v117\n"
"v_mfma_f32_16x16x32_bf16 v[140:143], v[56:59], a[8:11], 0\n"
"buffer_load_dwordx4 v5, s[56:59], s91 offen offset:2048 lds\n"
"v_cvt_pk_bf16_f32 v115, v118, v119\n"
"v_mfma_f32_16x16x32_bf16 v[144:147], v[56:59], a[12:15], 0\n"
"ds_read_b128 v[68:71], v9 offset:1024\n"
"v_cvt_pk_bf16_f32 v116, v120, v121\n"
"v_mfma_f32_16x16x32_bf16 v[148:151], v[56:59], a[16:19], 0\n"
"v_cvt_pk_bf16_f32 v117, v122, v123\n"
"s_waitcnt lgkmcnt(2)\n"
"v_mfma_f32_16x16x32_bf16 v[132:135], v[60:63], a[20:23], v[132:135]\n"
"v_cvt_pk_bf16_f32 v118, v124, v125\n"
"v_cvt_pk_bf16_f32 v119, v126, v127\n"
"v_mfma_f32_16x16x32_bf16 v[136:139], v[60:63], a[24:27], v[136:139]\n"
"buffer_load_dwordx4 v6, s[56:59], s91 offen offset:3072 lds\n"
"v_cvt_pk_bf16_f32 v120, v128, v129\n"
"v_mfma_f32_16x16x32_bf16 v[140:143], v[60:63], a[28:31], v[140:143]\n"
"s_add_u32 m0, s97, 0\n"
"v_cvt_pk_bf16_f32 v121, v130, v131\n"
"v_mfma_f32_16x16x32_bf16 v[144:147], v[60:63], a[32:35], v[144:147]\n"
"s_add_u32 s90, s86, s93\n"
"v_mfma_f32_16x16x32_bf16 v[148:151], v[60:63], a[36:39], v[148:151]\n"
"buffer_store_dwordx4 v[112:115], v48, s[64:67], s73 offen sc1\n"
"s_waitcnt lgkmcnt(0) vmcnt(11)\n"
"s_barrier\n"
"v_mfma_f32_16x16x32_bf16 v[132:135], v[64:67], a[40:43], v[132:135]\n"
"ds_read_b64_tr_b16 v[56:57], v10 offset:16384\n"
"v_mfma_f32_16x16x32_bf16 v[136:139], v[64:67], a[44:47], v[136:139]\n"
"ds_read_b64_tr_b16 v[58:59], v10 offset:24576\n"
"s_add_u32 s91, s87, s93\n"
"v_mfma_f32_16x16x32_bf16 v[140:143], v[64:67], a[48:51], v[140:143]\n"
"v_mfma_f32_16x16x32_bf16 v[144:147], v[64:67], a[52:55], v[144:147]\n"
"buffer_load_dwordx4 v5, s[56:59], s90 offen lds\n"
"v_mfma_f32_16x16x32_bf16 v[148:151], v[64:67], a[56:59], v[148:151]\n"
"v_mfma_f32_16x16x32_bf16 v[132:135], v[68:71], a[60:63], v[132:135]\n"
"ds_read_b64_tr_b16 v[60:61], v10 offset:17408\n"
"v_mfma_f32_16x16x32_bf16 v[136:139], v[68:71], a[64:67], v[136:139]\n"
"ds_read_b64_tr_b16 v[62:63], v10 offset:25600\n"
"v_mfma_f32_16x16x32_bf16 v[140:143], v[68:71], a[68:71], v[140:143]\n"
"v_mfma_f32_16x16x32_bf16 v[144:147], v[68:71], a[72:75], v[144:147]\n"
"buffer_load_dwordx4 v6, s[56:59], s90 offen offset:1024 lds\n"
"v_mfma_f32_16x16x32_bf16 v[148:151], v[68:71], a[76:79], v[148:151]\n"
"buffer_store_dwordx4 v[116:119], v48, s[64:67], s73 offen offset:1024 sc1\n"
"s_waitcnt lgkmcnt(2)\n"
"v_mfma_f32_16x16x32_bf16 v[132:135], v[56:59], a[80:83], v[132:135]\n"
"ds_read_b64_tr_b16 v[64:65], v10 offset:18432\n"
"v_mfma_f32_16x16x32_bf16 v[136:139], v[56:59], a[84:87], v[136:139]\n"
"ds_read_b64_tr_b16 v[66:67], v10 offset:26624\n"
"v_mfma_f32_16x16x32_bf16 v[140:143], v[56:59], a[88:91], v[140:143]\n"
"buffer_load_dwordx4 v5, s[56:59], s91 offen offset:2048 lds\n"
"v_mfma_f32_16x16x32_bf16 v[144:147], v[56:59], a[92:95], v[144:147]\n"
"ds_read_b64_tr_b16 v[68:69], v10 offset:19456\n"
"v_mfma_f32_16x16x32_bf16 v[148:151], v[56:59], a[96:99], v[148:151]\n"
"ds_read_b64_tr_b16 v[70:71], v10 offset:27648\n"
"s_waitcnt lgkmcnt(4)\n"
"v_mfma_f32_16x16x32_bf16 v[132:135], v[60:63], a[100:103], v[132:135]\n"
"v_mfma_f32_16x16x32_bf16 v[136:139], v[60:63], a[104:107], v[136:139]\n"
"buffer_load_dwordx4 v6, s[56:59], s91 offen offset:3072 lds\n"
"v_mfma_f32_16x16x32_bf16 v[140:143], v[60:63], a[108:111], v[140:143]\n"
"s_add_u32 m0, s97, 16384\n"
"s_add_u32 s93, s93, 128\n"
"v_mfma_f32_16x16x32_bf16 v[144:147], v[60:63], a[112:115], v[144:147]\n"
"s_cmp_eq_u32 s93, 1152\n"
"s_cselect_b32 s93, 0, s93\n"
"v_mfma_f32_16x16x32_bf16 v[148:151], v[60:63], a[116:119], v[148:151]\n"
"buffer_store_dwordx2 v[120:121], v49, s[64:67], s73 offen sc1\n"
"s_waitcnt lgkmcnt(0) vmcnt(12)\n"
"s_barrier\n"
"v_mfma_f32_16x16x32_bf16 v[132:135], v[64:67], a[120:123], v[132:135]\n"
"ds_read_b64_tr_b16 v[56:57], v10 offset:32768\n"
"s_lshl_b32 s74, s72, 11\n"
"v_mfma_f32_16x16x32_bf16 v[136:139], v[64:67], a[124:127], v[136:139]\n"
"ds_read_b64_tr_b16 v[58:59], v10 offset:40960\n"
"s_mul_i32 s90, s93, 576\n"
"v_mfma_f32_16x16x32_bf16 v[140:143], v[64:67], a[128:131], v[140:143]\n"
"s_add_u32 s90, s90, s92\n"
"v_cmp_ne_u64 vcc, s[88:89], v[44:45]\n"
"v_mfma_f32_16x16x32_bf16 v[144:147], v[64:67], a[132:135], v[144:147]\n"
"buffer_load_dwordx4 v3, s[40:43], s90 offen lds\n"
"s_cbranch_vccnz .Lfz_rf0\n"
".Lfz_cf0:\n"
"v_mfma_f32_16x16x32_bf16 v[148:151], v[64:67], a[136:139], v[148:151]\n"
"s_add_u32 s79, s75, 0\n"
"v_mfma_f32_16x16x32_bf16 v[132:135], v[68:71], a[140:143], v[132:135]\n"
"ds_read_b64_tr_b16 v[60:61], v10 offset:33792\n"
"v_mfma_f32_16x16x32_bf16 v[136:139], v[68:71], a[144:147], v[136:139]\n"
"ds_read_b64_tr_b16 v[62:63], v10 offset:41984\n"
"v_mfma_f32_16x16x32_bf16 v[140:143], v[68:71], a[148:151], v[140:143]\n"
"v_mfma_f32_16x16x32_bf16 v[144:147], v[68:71], a[152:155], v[144:147]\n"
"buffer_load_dwordx4 v3, s[44:47], s90 offen offset:1024 lds\n"
"v_mfma_f32_16x16x32_bf16 v[148:151], v[68:71], a[156:159], v[148:151]\n"
"s_add_u32 s74, s74, s99\n"
"buffer_load_dwordx4 v[34:37], v48, s[64:67], s79 offen sc1\n"
"s_waitcnt lgkmcnt(2)\n"
"v_mfma_f32_16x16x32_bf16 v[132:135], v[56:59], a[160:163], v[132:135]\n"
"ds_read_b64_tr_b16 v[64:65], v10 offset:34816\n"
"v_mfma_f32_16x16x32_bf16 v[136:139], v[56:59], a[164:167], v[136:139]\n"
"ds_read_b64_tr_b16 v[66:67], v10 offset:43008\n"
"v_mfma_f32_16x16x32_bf16 v[140:143], v[56:59], a[168:171], v[140:143]\n"
"buffer_load_dwordx4 v4, s[40:43], s90 offen offset:2048 lds\n"
"v_mfma_f32_16x16x32_bf16 v[144:147], v[56:59], a[172:175], v[144:147]\n"
"ds_read_b64_tr_b16 v[68:69], v10 offset:35840\n"
"v_mfma_f32_16x16x32_bf16 v[148:151], v[56:59], a[176:179], v[148:151]\n"
"ds_read_b64_tr_b16 v[70:71], v10 offset:44032\n"
"s_waitcnt lgkmcnt(4)\n"
"v_mfma_f32_16x16x32_bf16 v[132:135], v[60:63], a[180:183], v[132:135]\n"
"v_mfma_f32_16x16x32_bf16 v[136:139], v[60:63], a[184:187], v[136:139]\n"
"buffer_load_dwordx4 v4, s[44:47], s90 offen offset:3072 lds\n"
"v_mfma_f32_16x16x32_bf16 v[140:143], v[60:63], a[188:191], v[140:143]\n"
"s_add_u32 m0, s97, 32768\n"
"v_mfma_f32_16x16x32_bf16 v[144:147], v[60:63], a[192:195], v[144:147]\n"
"s_add_u32 s90, s80, s93\n"
"v_mfma_f32_16x16x32_bf16 v[148:151], v[60:63], a[196:199], v[148:151]\n"
"buffer_load_dwordx4 v[38:41], v48, s[64:67], s79 offen offset:1024 sc1\n"
"s_waitcnt lgkmcnt(0) vmcnt(13)\n"
"s_barrier\n"
"v_mfma_f32_16x16x32_bf16 v[132:135], v[64:67], a[200:203], v[132:135]\n"
"ds_read_b64_tr_b16 v[56:57], v10 offset:49152\n"
"s_add_u32 s99, s99, 256\n"
"v_mfma_f32_16x16x32_bf16 v[136:139], v[64:67], a[204:207], v[136:139]\n"
"ds_read_b64_tr_b16 v[58:59], v10 offset:57344\n"
"s_add_u32 s91, s81, s93\n"
"v_mfma_f32_16x16x32_bf16 v[140:143], v[64:67], a[208:211], v[140:143]\n"
"v_mfma_f32_16x16x32_bf16 v[144:147], v[64:67], a[212:215], v[144:147]\n"
"buffer_load_dwordx4 v5, s[56:59], s90 offen lds\n"
"v_mfma_f32_16x16x32_bf16 v[148:151], v[64:67], a[216:219], v[148:151]\n"
"v_mfma_f32_16x16x32_bf16 v[132:135], v[68:71], a[220:223], v[132:135]\n"
"ds_read_b64_tr_b16 v[60:61], v10 offset:50176\n"
"v_mfma_f32_16x16x32_bf16 v[136:139], v[68:71], a[224:227], v[136:139]\n"
"ds_read_b64_tr_b16 v[62:63], v10 offset:58368\n"
"v_mfma_f32_16x16x32_bf16 v[140:143], v[68:71], a[228:231], v[140:143]\n"
"v_mfma_f32_16x16x32_bf16 v[144:147], v[68:71], a[232:235], v[144:147]\n"
"buffer_load_dwordx4 v6, s[56:59], s90 offen offset:1024 lds\n"
"v_mfma_f32_16x16x32_bf16 v[148:151], v[68:71], a[236:239], v[148:151]\n"
"buffer_load_dwordx2 v[42:43], v49, s[64:67], s79 offen sc1\n"
"s_waitcnt lgkmcnt(2)\n"
"v_mfma_f32_16x16x32_bf16 v[132:135], v[56:59], a[240:243], v[132:135]\n"
"ds_read_b64_tr_b16 v[64:65], v10 offset:51200\n"
"v_mfma_f32_16x16x32_bf16 v[136:139], v[56:59], a[244:247], v[136:139]\n"
"ds_read_b64_tr_b16 v[66:67], v10 offset:59392\n"
"v_mfma_f32_16x16x32_bf16 v[140:143], v[56:59], a[248:251], v[140:143]\n"
"buffer_load_dwordx4 v5, s[56:59], s91 offen offset:2048 lds\n"
"v_mfma_f32_16x16x32_bf16 v[144:147], v[56:59], a[252:255], v[144:147]\n"
"ds_read_b64_tr_b16 v[68:69], v10 offset:52224\n"
"v_mfma_f32_16x16x32_bf16 v[148:151], v[56:59], v[152:155], v[148:151]\n"
"ds_read_b64_tr_b16 v[70:71], v10 offset:60416\n"
"s_waitcnt lgkmcnt(4)\n"
"v_mfma_f32_16x16x32_bf16 v[132:135], v[60:63], v[156:159], v[132:135]\n"
"v_mfma_f32_16x16x32_bf16 v[136:139], v[60:63], v[160:163], v[136:139]\n"
"buffer_load_dwordx4 v6, s[56:59], s91 offen offset:3072 lds\n"
"v_mfma_f32_16x16x32_bf16 v[140:143], v[60:63], v[164:167], v[140:143]\n"
"s_add_u32 m0, s97, 49152\n"
"v_mfma_f32_16x16x32_bf16 v[144:147], v[60:63], v[168:171], v[144:147]\n"
"s_add_u32 s90, s82, s93\n"
"v_mfma_f32_16x16x32_bf16 v[148:151], v[60:63], v[172:175], v[148:151]\n"
"s_waitcnt lgkmcnt(0) vmcnt(12)\n"
"s_barrier\n"
"v_mfma_f32_16x16x32_bf16 v[132:135], v[64:67], v[176:179], v[132:135]\n"
"ds_read_b64_tr_b16 v[56:57], v10 offset:0\n"
"v_mfma_f32_16x16x32_bf16 v[136:139], v[64:67], v[180:183], v[136:139]\n"
"ds_read_b64_tr_b16 v[58:59], v10 offset:8192\n"
"s_add_u32 s91, s83, s93\n"
"v_mfma_f32_16x16x32_bf16 v[140:143], v[64:67], v[184:187], v[140:143]\n"
"s_add_u32 s72, s72, 1\n"
"s_add_u32 s77, s76, 256\n"
"buffer_load_dwordx2 v[44:45], off, s[64:67], s77 sc1\n"
"v_mfma_f32_16x16x32_bf16 v[144:147], v[64:67], v[188:191], v[144:147]\n"
"buffer_load_dwordx4 v5, s[56:59], s90 offen lds\n"
"v_mfma_f32_16x16x32_bf16 v[148:151], v[64:67], v[192:195], v[148:151]\n"
"v_mfma_f32_16x16x32_bf16 v[132:135], v[68:71], v[196:199], v[132:135]\n"
"ds_read_b64_tr_b16 v[60:61], v10 offset:1024\n"
"v_mfma_f32_16x16x32_bf16 v[136:139], v[68:71], v[200:203], v[136:139]\n"
"ds_read_b64_tr_b16 v[62:63], v10 offset:9216\n"
"v_mfma_f32_16x16x32_bf16 v[140:143], v[68:71], v[204:207], v[140:143]\n"
"s_cmp_eq_u32 s72, 9\n"
"s_cselect_b32 s72, 0, s72\n"
"v_mfma_f32_16x16x32_bf16 v[144:147], v[68:71], v[208:211], v[144:147]\n"
"buffer_load_dwordx4 v6, s[56:59], s90 offen offset:1024 lds\n"
"v_mfma_f32_16x16x32_bf16 v[148:151], v[68:71], v[212:215], v[148:151]\n"
"s_waitcnt lgkmcnt(2)\n"
"v_mfma_f32_16x16x32_bf16 v[132:135], v[56:59], v[216:219], v[132:135]\n"
"ds_read_b64_tr_b16 v[64:65], v10 offset:2048\n"
"v_mfma_f32_16x16x32_bf16 v[136:139], v[56:59], v[220:223], v[136:139]\n"
"ds_read_b64_tr_b16 v[66:67], v10 offset:10240\n"
"v_mfma_f32_16x16x32_bf16 v[140:143], v[56:59], v[224:227], v[140:143]\n"
"buffer_load_dwordx4 v5, s[56:59], s91 offen offset:2048 lds\n"
"v_mfma_f32_16x16x32_bf16 v[144:147], v[56:59], v[228:231], v[144:147]\n"
"ds_read_b64_tr_b16 v[68:69], v10 offset:3072\n"
"v_mfma_f32_16x16x32_bf16 v[148:151], v[56:59], v[232:235], v[148:151]\n"
"ds_read_b64_tr_b16 v[70:71], v10 offset:11264\n"
"s_waitcnt lgkmcnt(4)\n"
"v_mfma_f32_16x16x32_bf16 v[132:135], v[60:63], v[236:239], v[132:135]\n"
"v_mfma_f32_16x16x32_bf16 v[136:139], v[60:63], v[240:243], v[136:139]\n"
"buffer_load_dwordx4 v6, s[56:59], s91 offen offset:3072 lds\n"
"v_mfma_f32_16x16x32_bf16 v[140:143], v[60:63], v[244:247], v[140:143]\n"
"s_add_u32 m0, s97, 0\n"
"v_mfma_f32_16x16x32_bf16 v[144:147], v[60:63], v[248:251], v[144:147]\n"
"s_add_u32 s90, s84, s93\n"
"s_mul_i32 s73, s72, 92160\n"
"v_mfma_f32_16x16x32_bf16 v[148:151], v[60:63], v[252:255], v[148:151]\n"
"s_waitcnt lgkmcnt(0) vmcnt(11)\n"
"s_barrier\n"
"v_mfma_f32_16x16x32_bf16 v[132:135], v[64:67], v[72:75], v[132:135]\n"
"ds_read_b128 v[56:59], v8 offset:16384\n"
"v_mfma_f32_16x16x32_bf16 v[136:139], v[64:67], v[76:79], v[136:139]\n"
"s_add_u32 s91, s85, s93\n"
"buffer_store_dwordx2 v[46:47], off, s[64:67], s74 sc1\n"
"v_mfma_f32_16x16x32_bf16 v[140:143], v[64:67], v[80:83], v[140:143]\n"
"v_mfma_f32_16x16x32_bf16 v[144:147], v[64:67], v[84:87], v[144:147]\n"
"buffer_load_dwordx4 v5, s[56:59], s90 offen lds\n"
"v_mfma_f32_16x16x32_bf16 v[148:151], v[64:67], v[88:91], v[148:151]\n"
"v_mfma_f32_16x16x32_bf16 v[132:135], v[68:71], v[92:95], v[132:135]\n"
"ds_read_b128 v[60:63], v8 offset:17408\n"
"s_add_u32 s73, s73, s98\n"
"v_mfma_f32_16x16x32_bf16 v[136:139], v[68:71], v[96:99], v[136:139]\n"
"v_mfma_f32_16x16x32_bf16 v[140:143], v[68:71], v[100:103], v[140:143]\n"
"v_mfma_f32_16x16x32_bf16 v[144:147], v[68:71], v[104:107], v[144:147]\n"
"buffer_load_dwordx4 v6, s[56:59], s90 offen offset:1024 lds\n"
"v_mfma_f32_16x16x32_bf16 v[148:151], v[68:71], v[108:111], v[148:151]\n"
"s_add_u32 s98, s98, 10240\n"
"s_waitcnt lgkmcnt(1)\n"
"v_mfma_f32_16x16x32_bf16 v[112:115], v[56:59], a[0:3], 0\n"
"ds_read_b128 v[64:67], v9 offset:16384\n"
"v_cvt_pk_bf16_f32 v132, v132, v133\n"
"v_mfma_f32_16x16x32_bf16 v[116:119], v[56:59], a[4:7], 0\n"
"v_cvt_pk_bf16_f32 v133, v134, v135\n"
"v_cvt_pk_bf16_f32 v134, v136, v137\n"
"v_mfma_f32_16x16x32_bf16 v[120:123], v[56:59], a[8:11], 0\n"
"buffer_load_dwordx4 v5, s[56:59], s91 offen offset:2048 lds\n"
"v_cvt_pk_bf16_f32 v135, v138, v139\n"
"v_mfma_f32_16x16x32_bf16 v[124:127], v[56:59], a[12:15], 0\n"
"ds_read_b128 v[68:71], v9 offset:17408\n"
"v_cvt_pk_bf16_f32 v136, v140, v141\n"
"v_mfma_f32_16x16x32_bf16 v[128:131], v[56:59], a[16:19], 0\n"
"v_cvt_pk_bf16_f32 v137, v142, v143\n"
"s_waitcnt lgkmcnt(2)\n"
"v_mfma_f32_16x16x32_bf16 v[112:115], v[60:63], a[20:23], v[112:115]\n"
"v_cvt_pk_bf16_f32 v138, v144, v145\n"
"v_cvt_pk_bf16_f32 v139, v146, v147\n"
"s_waitcnt vmcnt(14)\n"
"v_mfma_f32_16x16x32_bf16 v[26:29], v[50:53], v[38:41], v[26:29]\n"
"v_mfma_f32_16x16x32_bf16 v[116:119], v[60:63], a[24:27], v[116:119]\n"
"buffer_load_dwordx4 v6, s[56:59], s91 offen offset:3072 lds\n"
"v_cvt_pk_bf16_f32 v140, v148, v149\n"
"s_waitcnt vmcnt(12)\n"
"v_mfma_f32_16x16x32_bf16 v[30:33], v[50:53], v[40:43], v[30:33]\n"
"v_mfma_f32_16x16x32_bf16 v[120:123], v[60:63], a[28:31], v[120:123]\n"
"s_add_u32 m0, s97, 16384\n"
"v_cvt_pk_bf16_f32 v141, v150, v151\n"
"v_mfma_f32_16x16x32_bf16 v[14:17], v[52:55], v[34:37], v[14:17]\n"
"v_mfma_f32_16x16x32_bf16 v[124:127], v[60:63], a[32:35], v[124:127]\n"
"s_add_u32 s90, s86, s93\n"
"v_mfma_f32_16x16x32_bf16 v[18:21], v[50:53], v[34:37], v[18:21]\n"
"v_mfma_f32_16x16x32_bf16 v[128:131], v[60:63], a[36:39], v[128:131]\n"
"buffer_store_dwordx4 v[132:135], v48, s[64:67], s73 offen sc1\n"
"v_mfma_f32_16x16x32_bf16 v[22:25], v[52:55], v[38:41], v[22:25]\n"
"s_waitcnt lgkmcnt(0) vmcnt(11)\n"
"s_barrier\n"
"v_mfma_f32_16x16x32_bf16 v[112:115], v[64:67], a[40:43], v[112:115]\n"
"ds_read_b64_tr_b16 v[56:57], v10 offset:32768\n"
"v_mfma_f32_16x16x32_bf16 v[116:119], v[64:67], a[44:47], v[116:119]\n"
"ds_read_b64_tr_b16 v[58:59], v10 offset:40960\n"
"s_add_u32 s91, s87, s93\n"
"v_mfma_f32_16x16x32_bf16 v[120:123], v[64:67], a[48:51], v[120:123]\n"
"v_mfma_f32_16x16x32_bf16 v[124:127], v[64:67], a[52:55], v[124:127]\n"
"buffer_load_dwordx4 v5, s[56:59], s90 offen lds\n"
"v_mfma_f32_16x16x32_bf16 v[128:131], v[64:67], a[56:59], v[128:131]\n"
"v_mfma_f32_16x16x32_bf16 v[112:115], v[68:71], a[60:63], v[112:115]\n"
"ds_read_b64_tr_b16 v[60:61], v10 offset:33792\n"
"v_mfma_f32_16x16x32_bf16 v[116:119], v[68:71], a[64:67], v[116:119]\n"
"ds_read_b64_tr_b16 v[62:63], v10 offset:41984\n"
"v_mfma_f32_16x16x32_bf16 v[120:123], v[68:71], a[68:71], v[120:123]\n"
"v_mfma_f32_16x16x32_bf16 v[124:127], v[68:71], a[72:75], v[124:127]\n"
"buffer_load_dwordx4 v6, s[56:59], s90 offen offset:1024 lds\n"
"v_mfma_f32_16x16x32_bf16 v[128:131], v[68:71], a[76:79], v[128:131]\n"
"buffer_store_dwordx4 v[136:139], v48, s[64:67], s73 offen offset:1024 sc1\n"
"s_waitcnt lgkmcnt(2)\n"
"v_mfma_f32_16x16x32_bf16 v[112:115], v[56:59], a[80:83], v[112:115]\n"
"ds_read_b64_tr_b16 v[64:65], v10 offset:34816\n"
"v_mfma_f32_16x16x32_bf16 v[116:119], v[56:59], a[84:87], v[116:119]\n"
"ds_read_b64_tr_b16 v[66:67], v10 offset:43008\n"
"v_mfma_f32_16x16x32_bf16 v[120:123], v[56:59], a[88:91], v[120:123]\n"
"buffer_load_dwordx4 v5, s[56:59], s91 offen offset:2048 lds\n"
"v_mfma_f32_16x16x32_bf16 v[124:127], v[56:59], a[92:95], v[124:127]\n"
"ds_read_b64_tr_b16 v[68:69], v10 offset:35840\n"
"v_mfma_f32_16x16x32_bf16 v[128:131], v[56:59], a[96:99], v[128:131]\n"
"ds_read_b64_tr_b16 v[70:71], v10 offset:44032\n"
"s_waitcnt lgkmcnt(4)\n"
"v_mfma_f32_16x16x32_bf16 v[112:115], v[60:63], a[100:103], v[112:115]\n"
"v_mfma_f32_16x16x32_bf16 v[116:119], v[60:63], a[104:107], v[116:119]\n"
"buffer_load_dwordx4 v6, s[56:59], s91 offen offset:3072 lds\n"
"v_mfma_f32_16x16x32_bf16 v[120:123], v[60:63], a[108:111], v[120:123]\n"
"s_add_u32 m0, s97, 32768\n"
"s_add_u32 s93, s93, 128\n"
"v_mfma_f32_16x16x32_bf16 v[124:127], v[60:63], a[112:115], v[124:127]\n"
"s_cmp_eq_u32 s93, 1152\n"
"s_cselect_b32 s93, 0, s93\n"
"v_mfma_f32_16x16x32_bf16 v[128:131], v[60:63], a[116:119], v[128:131]\n"
"buffer_store_dwordx2 v[140:141], v49, s[64:67], s73 offen sc1\n"
"s_waitcnt lgkmcnt(0) vmcnt(12)\n"
"s_barrier\n"
"v_mfma_f32_16x16x32_bf16 v[112:115], v[64:67], a[120:123], v[112:115]\n"
"ds_read_b64_tr_b16 v[56:57], v10 offset:49152\n"
"s_lshl_b32 s74, s72, 11\n"
"v_mfma_f32_16x16x32_bf16 v[116:119], v[64:67], a[124:127], v[116:119]\n"
"ds_read_b64_tr_b16 v[58:59], v10 offset:57344\n"
"s_mul_i32 s90, s93, 576\n"
"v_mfma_f32_16x16x32_bf16 v[120:123], v[64:67], a[128:131], v[120:123]\n"
"s_add_u32 s90, s90, s92\n"
"v_cmp_ne_u64 vcc, s[88:89], v[44:45]\n"
"v_mfma_f32_16x16x32_bf16 v[124:127], v[64:67], a[132:135], v[124:127]\n"
"buffer_load_dwordx4 v3, s[40:43], s90 offen lds\n"
"s_cbranch_vccnz .Lfz_rf1\n"
".Lfz_cf1:\n"
"v_mfma_f32_16x16x32_bf16 v[128:131], v[64:67], a[136:139], v[128:131]\n"
"s_add_u32 s79, s75, 10240\n"
"v_mfma_f32_16x16x32_bf16 v[112:115], v[68:71], a[140:143], v[112:115]\n"
"ds_read_b64_tr_b16 v[60:61], v10 offset:50176\n"
"v_mfma_f32_16x16x32_bf16 v[116:119], v[68:71], a[144:147], v[116:119]\n"
"ds_read_b64_tr_b16 v[62:63], v10 offset:58368\n"
"v_mfma_f32_16x16x32_bf16 v[120:123], v[68:71], a[148:151], v[120:123]\n"
"v_mfma_f32_16x16x32_bf16 v[124:127], v[68:71], a[152:155], v[124:127]\n"
"buffer_load_dwordx4 v3, s[44:47], s90 offen offset:1024 lds\n"
"v_mfma_f32_16x16x32_bf16 v[128:131], v[68:71], a[156:159], v[128:131]\n"
"s_add_u32 s74, s74, s99\n"
"buffer_load_dwordx4 v[34:37], v48, s[64:67], s79 offen sc1\n"
"s_waitcnt lgkmcnt(2)\n"
"v_mfma_f32_16x16x32_bf16 v[112:115], v[56:59], a[160:163], v[112:115]\n"
"ds_read_b64_tr_b16 v[64:65], v10 offset:51200\n"
"v_mfma_f32_16x16x32_bf16 v[116:119], v[56:59], a[164:167], v[116:119]\n"
"ds_read_b64_tr_b16 v[66:67], v10 offset:59392\n"
"v_mfma_f32_16x16x32_bf16 v[120:123], v[56:59], a[168:171], v[120:123]\n"
"buffer_load_dwordx4 v4, s[40:43], s90 offen offset:2048 lds\n"
"v_mfma_f32_16x16x32_bf16 v[124:127], v[56:59], a[172:175], v[124:127]\n"
"ds_read_b64_tr_b16 v[68:69], v10 offset:52224\n"
"v_mfma_f32_16x16x32_bf16 v[128:131], v[56:59], a[176:179], v[128:131]\n"
"ds_read_b64_tr_b16 v[70:71], v10 offset:60416\n"
"s_waitcnt lgkmcnt(4)\n"
"v_mfma_f32_16x16x32_bf16 v[112:115], v[60:63], a[180:183], v[112:115]\n"
"v_mfma_f32_16x16x32_bf16 v[116:119], v[60:63], a[184:187], v[116:119]\n"
"buffer_load_dwordx4 v4, s[44:47], s90 offen offset:3072 lds\n"
"v_mfma_f32_16x16x32_bf16 v[120:123], v[60:63], a[188:191], v[120:123]\n"
"s_add_u32 m0, s97, 49152\n"
"v_mfma_f32_16x16x32_bf16 v[124:127], v[60:63], a[192:195], v[124:127]\n"
"s_add_u32 s90, s80, s93\n"
"v_mfma_f32_16x16x32_bf16 v[128:131], v[60:63], a[196:199], v[128:131]\n"
"buffer_load_dwordx4 v[38:41], v48, s[64:67], s79 offen offset:1024 sc1\n"
"s_waitcnt lgkmcnt(0) vmcnt(13)\n"
"s_barrier\n"
"v_mfma_f32_16x16x32_bf16 v[112:115], v[64:67], a[200:203], v[112:115]\n"
"ds_read_b64_tr_b16 v[56:57], v10 offset:0\n"
"s_add_u32 s99, s99, 256\n"
"v_mfma_f32_16x16x32_bf16 v[116:119], v[64:67], a[204:207], v[116:119]\n"
"ds_read_b64_tr_b16 v[58:59], v10 offset:8192\n"
"s_add_u32 s91, s81, s93\n"
"v_mfma_f32_16x16x32_bf16 v[120:123], v[64:67], a[208:211], v[120:123]\n"
"v_mfma_f32_16x16x32_bf16 v[124:127], v[64:67], a[212:215], v[124:127]\n"
"buffer_load_dwordx4 v5, s[56:59], s90 offen lds\n"
"v_mfma_f32_16x16x32_bf16 v[128:131], v[64:67], a[216:219], v[128:131]\n"
"v_mfma_f32_16x16x32_bf16 v[112:115], v[68:71], a[220:223], v[112:115]\n"
"ds_read_b64_tr_b16 v[60:61], v10 offset:1024\n"
"v_mfma_f32_16x16x32_bf16 v[116:119], v[68:71], a[224:227], v[116:119]\n"
"ds_read_b64_tr_b16 v[62:63], v10 offset:9216\n"
"v_mfma_f32_16x16x32_bf16 v[120:123], v[68:71], a[228:231], v[120:123]\n"
"v_mfma_f32_16x16x32_bf16 v[124:127], v[68:71], a[232:235], v[124:127]\n"
"buffer_load_dwordx4 v6, s[56:59], s90 offen offset:1024 lds\n"
"v_mfma_f32_16x16x32_bf16 v[128:131], v[68:71], a[236:239], v[128:131]\n"
"buffer_load_dwordx2 v[42:43], v49, s[64:67], s79 offen sc1\n"
"s_waitcnt lgkmcnt(2)\n"
"v_mfma_f32_16x16x32_bf16 v[112:115], v[56:59], a[240:243], v[112:115]\n"
"ds_read_b64_tr_b16 v[64:65], v10 offset:2048\n"
"v_mfma_f32_16x16x32_bf16 v[116:119], v[56:59], a[244:247], v[116:119]\n"
"ds_read_b64_tr_b16 v[66:67], v10 offset:10240\n"
"v_mfma_f32_16x16x32_bf16 v[120:123], v[56:59], a[248:251], v[120:123]\n"
"buffer_load_dwordx4 v5, s[56:59], s91 offen offset:2048 lds\n"
"v_mfma_f32_16x16x32_bf16 v[124:127], v[56:59], a[252:255], v[124:127]\n"
"ds_read_b64_tr_b16 v[68:69], v10 offset:3072\n"
"v_mfma_f32_16x16x32_bf16 v[128:131], v[56:59], v[152:155], v[128:131]\n"
"ds_read_b64_tr_b16 v[70:71], v10 offset:11264\n"
"s_waitcnt lgkmcnt(4)\n"
"v_mfma_f32_16x16x32_bf16 v[112:115], v[60:63], v[156:159], v[112:115]\n"
"v_mfma_f32_16x16x32_bf16 v[116:119], v[60:63], v[160:163], v[116:119]\n"
"buffer_load_dwordx4 v6, s[56:59], s91 offen offset:3072 lds\n"
"v_mfma_f32_16x16x32_bf16 v[120:123], v[60:63], v[164:167], v[120:123]\n"
"s_add_u32 m0, s97, 0\n"
"v_mfma_f32_16x16x32_bf16 v[124:127], v[60:63], v[168:171], v[124:127]\n"
"s_add_u32 s90, s82, s93\n"
"v_mfma_f32_16x16x32_bf16 v[128:131], v[60:63], v[172:175], v[128:131]\n"
"s_waitcnt lgkmcnt(0) vmcnt(12)\n"
"s_barrier\n"
"v_mfma_f32_16x16x32_bf16 v[112:115], v[64:67], v[176:179], v[112:115]\n"
"ds_read_b64_tr_b16 v[56:57], v10 offset:16384\n"
"v_mfma_f32_16x16x32_bf16 v[116:119], v[64:67], v[180:183], v[116:119]\n"
"ds_read_b64_tr_b16 v[58:59], v10 offset:24576\n"
"s_add_u32 s91, s83, s93\n"
"v_mfma_f32_16x16x32_bf16 v[120:123], v[64:67], v[184:187], v[120:123]\n"
"s_add_u32 s72, s72, 1\n"
"s_add_u32 s77, s76, 512\n"
"buffer_load_dwordx2 v[44:45], off, s[64:67], s77 sc1\n"
"v_mfma_f32_16x16x32_bf16 v[124:127], v[64:67], v[188:191], v[124:127]\n"
"buffer_load_dwordx4 v5, s[56:59], s90 offen lds\n"
"v_mfma_f32_16x16x32_bf16 v[128:131], v[64:67], v[192:195], v[128:131]\n"
"v_mfma_f32_16x16x32_bf16 v[112:115], v[68:71], v[196:199], v[112:115]\n"
"ds_read_b64_tr_b16 v[60:61], v10 offset:17408\n"
"v_mfma_f32_16x16x32_bf16 v[116:119], v[68:71], v[200:203], v[116:119]\n"
"ds_read_b64_tr_b16 v[62:63], v10 offset:25600\n"
"v_mfma_f32_16x16x32_bf16 v[120:123], v[68:71], v[204:207], v[120:123]\n"
"s_cmp_eq_u32 s72, 9\n"
"s_cselect_b32 s72, 0, s72\n"
"v_mfma_f32_16x16x32_bf16 v[124:127], v[68:71], v[208:211], v[124:127]\n"
"buffer_load_dwordx4 v6, s[56:59], s90 offen offset:1024 lds\n"
"v_mfma_f32_16x16x32_bf16 v[128:131], v[68:71], v[212:215], v[128:131]\n"
"s_waitcnt lgkmcnt(2)\n"
"v_mfma_f32_16x16x32_bf16 v[112:115], v[56:59], v[216:219], v[112:115]\n"
"ds_read_b64_tr_b16 v[64:65], v10 offset:18432\n"
"v_mfma_f32_16x16x32_bf16 v[116:119], v[56:59], v[220:223], v[116:119]\n"
"ds_read_b64_tr_b16 v[66:67], v10 offset:26624\n"
"v_mfma_f32_16x16x32_bf16 v[120:123], v[56:59], v[224:227], v[120:123]\n"
"buffer_load_dwordx4 v5, s[56:59], s91 offen offset:2048 lds\n"
"v_mfma_f32_16x16x32_bf16 v[124:127], v[56:59], v[228:231], v[124:127]\n"
"ds_read_b64_tr_b16 v[68:69], v10 offset:19456\n"
"v_mfma_f32_16x16x32_bf16 v[128:131], v[56:59], v[232:235], v[128:131]\n"
"ds_read_b64_tr_b16 v[70:71], v10 offset:27648\n"
"s_waitcnt lgkmcnt(4)\n"
"v_mfma_f32_16x16x32_bf16 v[112:115], v[60:63], v[236:239], v[112:115]\n"
"v_mfma_f32_16x16x32_bf16 v[116:119], v[60:63], v[240:243], v[116:119]\n"
"buffer_load_dwordx4 v6, s[56:59], s91 offen offset:3072 lds\n"
"v_mfma_f32_16x16x32_bf16 v[120:123], v[60:63], v[244:247], v[120:123]\n"
"s_add_u32 m0, s97, 16384\n"
"v_mfma_f32_16x16x32_bf16 v[124:127], v[60:63], v[248:251], v[124:127]\n"
"s_add_u32 s90, s84, s93\n"
"s_mul_i32 s73, s72, 92160\n"
"v_mfma_f32_16x16x32_bf16 v[128:131], v[60:63], v[252:255], v[128:131]\n"
"s_waitcnt lgkmcnt(0) vmcnt(11)\n"
"s_barrier\n"
"v_mfma_f32_16x16x32_bf16 v[112:115], v[64:67], v[72:75], v[112:115]\n"
"ds_read_b128 v[56:59], v8 offset:32768\n"
"v_mfma_f32_16x16x32_bf16 v[116:119], v[64:67], v[76:79], v[116:119]\n"
"s_add_u32 s91, s85, s93\n"
"buffer_store_dwordx2 v[46:47], off, s[64:67], s74 sc1\n"
"v_mfma_f32_16x16x32_bf16 v[120:123], v[64:67], v[80:83], v[120:123]\n"
"v_mfma_f32_16x16x32_bf16 v[124:127], v[64:67], v[84:87], v[124:127]\n"
"buffer_load_dwordx4 v5, s[56:59], s90 offen lds\n"
"v_mfma_f32_16x16x32_bf16 v[128:131], v[64:67], v[88:91], v[128:131]\n"
"v_mfma_f32_16x16x32_bf16 v[112:115], v[68:71], v[92:95], v[112:115]\n"
"ds_read_b128 v[60:63], v8 offset:33792\n"
"s_add_u32 s73, s73, s98\n"
"v_mfma_f32_16x16x32_bf16 v[116:119], v[68:71], v[96:99], v[116:119]\n"
"v_mfma_f32_16x16x32_bf16 v[120:123], v[68:71], v[100:103], v[120:123]\n"
"v_mfma_f32_16x16x32_bf16 v[124:127], v[68:71], v[104:107], v[124:127]\n"
"buffer_load_dwordx4 v6, s[56:59], s90 offen offset:1024 lds\n"
"v_mfma_f32_16x16x32_bf16 v[128:131], v[68:71], v[108:111], v[128:131]\n"
"s_add_u32 s98, s98, 10240\n"
"s_waitcnt lgkmcnt(1)\n"
"v_mfma_f32_16x16x32_bf16 v[132:135], v[56:59], a[0:3], 0\n"
"ds_read_b128 v[64:67], v9 offset:32768\n"
"v_cvt_pk_bf16_f32 v112, v112, v113\n"
"v_mfma_f32_16x16x32_bf16 v[136:139], v[56:59], a[4:7], 0\n"
"v_cvt_pk_bf16_f32 v113, v114, v115\n"
"v_cvt_pk_bf16_f32 v114, v116, v117\n"
"v_mfma_f32_16x16x32_bf16 v[140:143], v[56:59], a[8:11], 0\n"
"buffer_load_dwordx4 v5, s[56:59], s91 offen offset:2048 lds\n"
"v_cvt_pk_bf16_f32 v115, v118, v119\n"
"v_mfma_f32_16x16x32_bf16 v[144:147], v[56:59], a[12:15], 0\n"
"ds_read_b128 v[68:71], v9 offset:33792\n"
"v_cvt_pk_bf16_f32 v116, v120, v121\n"
"v_mfma_f32_16x16x32_bf16 v[148:151], v[56:59], a[16:19], 0\n"
"v_cvt_pk_bf16_f32 v117, v122, v123\n"
"s_waitcnt lgkmcnt(2)\n"
"v_mfma_f32_16x16x32_bf16 v[132:135], v[60:63], a[20:23], v[132:135]\n"
"v_cvt_pk_bf16_f32 v118, v124, v125\n"
"v_cvt_pk_bf16_f32 v119, v126, v127\n"
"s_waitcnt vmcnt(14)\n"
"v_mfma_f32_16x16x32_bf16 v[26:29], v[50:53], v[38:41], v[26:29]\n"
"v_mfma_f32_16x16x32_bf16 v[136:139], v[60:63], a[24:27], v[136:139]\n"
"buffer_load_dwordx4 v6, s[56:59], s91 offen offset:3072 lds\n"
"v_cvt_pk_bf16_f32 v120, v128, v129\n"
"s_waitcnt vmcnt(12)\n"
"v_mfma_f32_16x16x32_bf16 v[30:33], v[50:53], v[40:43], v[30:33]\n"
"v_mfma_f32_16x16x32_bf16 v[140:143], v[60:63], a[28:31], v[140:143]\n"
"s_add_u32 m0, s97, 32768\n"
"v_cvt_pk_bf16_f32 v121, v130, v131\n"
"v_mfma_f32_16x16x32_bf16 v[14:17], v[52:55], v[34:37], v[14:17]\n"
"v_mfma_f32_16x16x32_bf16 v[144:147], v[60:63], a[32:35], v[144:147]\n"
"s_add_u32 s90, s86, s93\n"
"v_mfma_f32_16x16x32_bf16 v[18:21], v[50:53], v[34:37], v[18:21]\n"
"v_mfma_f32_16x16x32_bf16 v[148:151], v[60:63], a[36:39], v[148:151]\n"
"buffer_store_dwordx4 v[112:115], v48, s[64:67], s73 offen sc1\n"
"v_mfma_f32_16x16x32_bf16 v[22:25], v[52:55], v[38:41], v[22:25]\n"
"s_waitcnt lgkmcnt(0) vmcnt(11)\n"
"s_barrier\n"
"v_mfma_f32_16x16x32_bf16 v[132:135], v[64:67], a[40:43], v[132:135]\n"
"ds_read_b64_tr_b16 v[56:57], v10 offset:49152\n"
"v_mfma_f32_16x16x32_bf16 v[136:139], v[64:67], a[44:47], v[136:139]\n"
"ds_read_b64_tr_b16 v[58:59], v10 offset:57344\n"
"s_add_u32 s91, s87, s93\n"
"v_mfma_f32_16x16x32_bf16 v[140:143], v[64:67], a[48:51], v[140:143]\n"
"v_mfma_f32_16x16x32_bf16 v[144:147], v[64:67], a[52:55], v[144:147]\n"
"buffer_load_dwordx4 v5, s[56:59], s90 offen lds\n"
"v_mfma_f32_16x16x32_bf16 v[148:151], v[64:67], a[56:59], v[148:151]\n"
"v_mfma_f32_16x16x32_bf16 v[132:135], v[68:71], a[60:63], v[132:135]\n"
"ds_read_b64_tr_b16 v[60:61], v10 offset:50176\n"
"v_mfma_f32_16x16x32_bf16 v[136:139], v[68:71], a[64:67], v[136:139]\n"
"ds_read_b64_tr_b16 v[62:63], v10 offset:58368\n"
"v_mfma_f32_16x16x32_bf16 v[140:143], v[68:71], a[68:71], v[140:143]\n"
"v_mfma_f32_16x16x32_bf16 v[144:147], v[68:71], a[72:75], v[144:147]\n"
"buffer_load_dwordx4 v6, s[56:59], s90 offen offset:1024 lds\n"
"v_mfma_f32_16x16x32_bf16 v[148:151], v[68:71], a[76:79], v[148:151]\n"
"buffer_store_dwordx4 v[116:119], v48, s[64:67], s73 offen offset:1024 sc1\n"
"s_waitcnt lgkmcnt(2)\n"
"v_mfma_f32_16x16x32_bf16 v[132:135], v[56:59], a[80:83], v[132:135]\n"
"ds_read_b64_tr_b16 v[64:65], v10 offset:51200\n"
"v_mfma_f32_16x16x32_bf16 v[136:139], v[56:59], a[84:87], v[136:139]\n"
"ds_read_b64_tr_b16 v[66:67], v10 offset:59392\n"
"v_mfma_f32_16x16x32_bf16 v[140:143], v[56:59], a[88:91], v[140:143]\n"
"buffer_load_dwordx4 v5, s[56:59], s91 offen offset:2048 lds\n"
"v_mfma_f32_16x16x32_bf16 v[144:147], v[56:59], a[92:95], v[144:147]\n"
"ds_read_b64_tr_b16 v[68:69], v10 offset:52224\n"
"v_mfma_f32_16x16x32_bf16 v[148:151], v[56:59], a[96:99], v[148:151]\n"
"ds_read_b64_tr_b16 v[70:71], v10 offset:60416\n"
"s_waitcnt lgkmcnt(4)\n"
"v_mfma_f32_16x16x32_bf16 v[132:135], v[60:63], a[100:103], v[132:135]\n"
"v_mfma_f32_16x16x32_bf16 v[136:139], v[60:63], a[104:107], v[136:139]\n"
"buffer_load_dwordx4 v6, s[56:59], s91 offen offset:3072 lds\n"
"v_mfma_f32_16x16x32_bf16 v[140:143], v[60:63], a[108:111], v[140:143]\n"
"s_add_u32 m0, s97, 49152\n"
"s_add_u32 s93, s93, 128\n"
"v_mfma_f32_16x16x32_bf16 v[144:147], v[60:63], a[112:115], v[144:147]\n"
"s_cmp_eq_u32 s93, 1152\n"
"s_cselect_b32 s93, 0, s93\n"
"v_mfma_f32_16x16x32_bf16 v[148:151], v[60:63], a[116:119], v[148:151]\n"
"buffer_store_dwordx2 v[120:121], v49, s[64:67], s73 offen sc1\n"
"s_waitcnt lgkmcnt(0) vmcnt(12)\n"
"s_barrier\n"
"v_mfma_f32_16x16x32_bf16 v[132:135], v[64:67], a[120:123], v[132:135]\n"
"ds_read_b64_tr_b16 v[56:57], v10 offset:0\n"
"s_lshl_b32 s74, s72, 11\n"
"v_mfma_f32_16x16x32_bf16 v[136:139], v[64:67], a[124:127], v[136:139]\n"
"ds_read_b64_tr_b16 v[58:59], v10 offset:8192\n"
"s_mul_i32 s90, s93, 576\n"
"v_mfma_f32_16x16x32_bf16 v[140:143], v[64:67], a[128:131], v[140:143]\n"
"s_add_u32 s90, s90, s92\n"
"v_cmp_ne_u64 vcc, s[88:89], v[44:45]\n"
"v_mfma_f32_16x16x32_bf16 v[144:147], v[64:67], a[132:135], v[144:147]\n"
"buffer_load_dwordx4 v3, s[40:43], s90 offen lds\n"
"s_cbranch_vccnz .Lfz_rf2\n"
".Lfz_cf2:\n"
"v_mfma_f32_16x16x32_bf16 v[148:151], v[64:67], a[136:139], v[148:151]\n"
"s_add_u32 s79, s75, 20480\n"
"v_mfma_f32_16x16x32_bf16 v[132:135], v[68:71], a[140:143], v[132:135]\n"
"ds_read_b64_tr_b16 v[60:61], v10 offset:1024\n"
"v_mfma_f32_16x16x32_bf16 v[136:139], v[68:71], a[144:147], v[136:139]\n"
"ds_read_b64_tr_b16 v[62:63], v10 offset:9216\n"
"v_mfma_f32_16x16x32_bf16 v[140:143], v[68:71], a[148:151], v[140:143]\n"
"v_mfma_f32_16x16x32_bf16 v[144:147], v[68:71], a[152:155], v[144:147]\n"
"buffer_load_dwordx4 v3, s[44:47], s90 offen offset:1024 lds\n"
"v_mfma_f32_16x16x32_bf16 v[148:151], v[68:71], a[156:159], v[148:151]\n"
"s_add_u32 s74, s74, s99\n"
"buffer_load_dwordx4 v[34:37], v48, s[64:67], s79 offen sc1\n"
"s_waitcnt lgkmcnt(2)\n"
"v_mfma_f32_16x16x32_bf16 v[132:135], v[56:59], a[160:163], v[132:135]\n"
"ds_read_b64_tr_b16 v[64:65], v10 offset:2048\n"
"v_mfma_f32_16x16x32_bf16 v[136:139], v[56:59], a[164:167], v[136:139]\n"
"ds_read_b64_tr_b16 v[66:67], v10 offset:10240\n"
"v_mfma_f32_16x16x32_bf16 v[140:143], v[56:59], a[168:171], v[140:143]\n"
"buffer_load_dwordx4 v4, s[40:43], s90 offen offset:2048 lds\n"
"v_mfma_f32_16x16x32_bf16 v[144:147], v[56:59], a[172:175], v[144:147]\n"
"ds_read_b64_tr_b16 v[68:69], v10 offset:3072\n"
"v_mfma_f32_16x16x32_bf16 v[148:151], v[56:59], a[176:179], v[148:151]\n"
"ds_read_b64_tr_b16 v[70:71], v10 offset:11264\n"
"s_waitcnt lgkmcnt(4)\n"
"v_mfma_f32_16x16x32_bf16 v[132:135], v[60:63], a[180:183], v[132:135]\n"
"v_mfma_f32_16x16x32_bf16 v[136:139], v[60:63], a[184:187], v[136:139]\n"
"buffer_load_dwordx4 v4, s[44:47], s90 offen offset:3072 lds\n"
"v_mfma_f32_16x16x32_bf16 v[140:143], v[60:63], a[188:191], v[140:143]\n"
"s_add_u32 m0, s97, 0\n"
"v_mfma_f32_16x16x32_bf16 v[144:147], v[60:63], a[192:195], v[144:147]\n"
"s_add_u32 s90, s80, s93\n"
"v_mfma_f32_16x16x32_bf16 v[148:151], v[60:63], a[196:199], v[148:151]\n"
"buffer_load_dwordx4 v[38:41], v48, s[64:67], s79 offen offset:1024 sc1\n"
"s_waitcnt lgkmcnt(0) vmcnt(13)\n"
"s_barrier\n"
"v_mfma_f32_16x16x32_bf16 v[132:135], v[64:67], a[200:203], v[132:135]\n"
"ds_read_b64_tr_b16 v[56:57], v10 offset:16384\n"
"s_add_u32 s99, s99, 256\n"
"v_mfma_f32_16x16x32_bf16 v[136:139], v[64:67], a[204:207], v[136:139]\n"
"ds_read_b64_tr_b16 v[58:59], v10 offset:24576\n"
"s_add_u32 s91, s81, s93\n"
"v_mfma_f32_16x16x32_bf16 v[140:143], v[64:67], a[208:211], v[140:143]\n"
"v_mfma_f32_16x16x32_bf16 v[144:147], v[64:67], a[212:215], v[144:147]\n"
"buffer_load_dwordx4 v5, s[56:59], s90 offen lds\n"
"v_mfma_f32_16x16x32_bf16 v[148:151], v[64:67], a[216:219], v[148:151]\n"
"v_mfma_f32_16x16x32_bf16 v[132:135], v[68:71], a[220:223], v[132:135]\n"
"ds_read_b64_tr_b16 v[60:61], v10 offset:17408\n"
"v_mfma_f32_16x16x32_bf16 v[136:139], v[68:71], a[224:227], v[136:139]\n"
"ds_read_b64_tr_b16 v[62:63], v10 offset:25600\n"
"v_mfma_f32_16x16x32_bf16 v[140:143], v[68:71], a[228:231], v[140:143]\n"
"v_mfma_f32_16x16x32_bf16 v[144:147], v[68:71], a[232:235], v[144:147]\n"
"buffer_load_dwordx4 v6, s[56:59], s90 offen offset:1024 lds\n"
"v_mfma_f32_16x16x32_bf16 v[148:151], v[68:71], a[236:239], v[148:151]\n"
"buffer_load_dwordx2 v[42:43], v49, s[64:67], s79 offen sc1\n"
"s_waitcnt lgkmcnt(2)\n"
"v_mfma_f32_16x16x32_bf16 v[132:135], v[56:59], a[240:243], v[132:135]\n"
"ds_read_b64_tr_b16 v[64:65], v10 offset:18432\n"
"v_mfma_f32_16x16x32_bf16 v[136:139], v[56:59], a[244:247], v[136:139]\n"
"ds_read_b64_tr_b16 v[66:67], v10 offset:26624\n"
"v_mfma_f32_16x16x32_bf16 v[140:143], v[56:59], a[248:251], v[140:143]\n"
"buffer_load_dwordx4 v5, s[56:59], s91 offen offset:2048 lds\n"
"v_mfma_f32_16x16x32_bf16 v[144:147], v[56:59], a[252:255], v[144:147]\n"
"ds_read_b64_tr_b16 v[68:69], v10 offset:19456\n"
"v_mfma_f32_16x16x32_bf16 v[148:151], v[56:59], v[152:155], v[148:151]\n"
"ds_read_b64_tr_b16 v[70:71], v10 offset:27648\n"
"s_waitcnt lgkmcnt(4)\n"
"v_mfma_f32_16x16x32_bf16 v[132:135], v[60:63], v[156:159], v[132:135]\n"
"v_mfma_f32_16x16x32_bf16 v[136:139], v[60:63], v[160:163], v[136:139]\n"
"buffer_load_dwordx4 v6, s[56:59], s91 offen offset:3072 lds\n"
"v_mfma_f32_16x16x32_bf16 v[140:143], v[60:63], v[164:167], v[140:143]\n"
"s_add_u32 m0, s97, 16384\n"
"v_mfma_f32_16x16x32_bf16 v[144:147], v[60:63], v[168:171], v[144:147]\n"
"s_add_u32 s90, s82, s93\n"
"v_mfma_f32_16x16x32_bf16 v[148:151], v[60:63], v[172:175], v[148:151]\n"
"s_waitcnt lgkmcnt(0) vmcnt(12)\n"
"s_barrier\n"
"v_mfma_f32_16x16x32_bf16 v[132:135], v[64:67], v[176:179], v[132:135]\n"
"ds_read_b64_tr_b16 v[56:57], v10 offset:32768\n"
"v_mfma_f32_16x16x32_bf16 v[136:139], v[64:67], v[180:183], v[136:139]\n"
"ds_read_b64_tr_b16 v[58:59], v10 offset:40960\n"
"s_add_u32 s91, s83, s93\n"
"v_mfma_f32_16x16x32_bf16 v[140:143], v[64:67], v[184:187], v[140:143]\n"
"s_add_u32 s72, s72, 1\n"
"s_add_u32 s77, s76, 768\n"
"buffer_load_dwordx2 v[44:45], off, s[64:67], s77 sc1\n"
"v_mfma_f32_16x16x32_bf16 v[144:147], v[64:67], v[188:191], v[144:147]\n"
"buffer_load_dwordx4 v5, s[56:59], s90 offen lds\n"
"v_mfma_f32_16x16x32_bf16 v[148:151], v[64:67], v[192:195], v[148:151]\n"
"v_mfma_f32_16x16x32_bf16 v[132:135], v[68:71], v[196:199], v[132:135]\n"
"ds_read_b64_tr_b16 v[60:61], v10 offset:33792\n"
"v_mfma_f32_16x16x32_bf16 v[136:139], v[68:71], v[200:203], v[136:139]\n"
"ds_read_b64_tr_b16 v[62:63], v10 offset:41984\n"
"v_mfma_f32_16x16x32_bf16 v[140:143], v[68:71], v[204:207], v[140:143]\n"
"s_cmp_eq_u32 s72, 9\n"
"s_cselect_b32 s72, 0, s72\n"
"v_mfma_f32_16x16x32_bf16 v[144:147], v[68:71], v[208:211], v[144:147]\n"
"buffer_load_dwordx4 v6, s[56:59], s90 offen offset:1024 lds\n"
"v_mfma_f32_16x16x32_bf16 v[148:151], v[68:71], v[212:215], v[148:151]\n"
"s_waitcnt lgkmcnt(2)\n"
"v_mfma_f32_16x16x32_bf16 v[132:135], v[56:59], v[216:219], v[132:135]\n"
"ds_read_b64_tr_b16 v[64:65], v10 offset:34816\n"
"v_mfma_f32_16x16x32_bf16 v[136:139], v[56:59], v[220:223], v[136:139]\n"
"ds_read_b64_tr_b16 v[66:67], v10 offset:43008\n"
"v_mfma_f32_16x16x32_bf16 v[140:143], v[56:59], v[224:227], v[140:143]\n"
"buffer_load_dwordx4 v5, s[56:59], s91 offen offset:2048 lds\n"
"v_mfma_f32_16x16x32_bf16 v[144:147], v[56:59], v[228:231], v[144:147]\n"
"ds_read_b64_tr_b16 v[68:69], v10 offset:35840\n"
"v_mfma_f32_16x16x32_bf16 v[148:151], v[56:59], v[232:235], v[148:151]\n"
"ds_read_b64_tr_b16 v[70:71], v10 offset:44032\n"
"s_waitcnt lgkmcnt(4)\n"
"v_mfma_f32_16x16x32_bf16 v[132:135], v[60:63], v[236:239], v[132:135]\n"
"v_mfma_f32_16x16x32_bf16 v[136:139], v[60:63], v[240:243], v[136:139]\n"
"buffer_load_dwordx4 v6, s[56:59], s91 offen offset:3072 lds\n"
"v_mfma_f32_16x16x32_bf16 v[140:143], v[60:63], v[244:247], v[140:143]\n"
"s_add_u32 m0, s97, 32768\n"
"v_mfma_f32_16x16x32_bf16 v[144:147], v[60:63], v[248:251], v[144:147]\n"
"s_add_u32 s90, s84, s93\n"
"s_mul_i32 s73, s72, 92160\n"
"v_mfma_f32_16x16x32_bf16 v[148:151], v[60:63], v[252:255], v[148:151]\n"
"s_waitcnt lgkmcnt(0) vmcnt(11)\n"
"s_barrier\n"
"v_mfma_f32_16x16x32_bf16 v[132:135], v[64:67], v[72:75], v[132:135]\n"
"ds_read_b128 v[56:59], v8 offset:49152\n"
"v_mfma_f32_16x16x32_bf16 v[136:139], v[64:67], v[76:79], v[136:139]\n"
"s_add_u32 s91, s85, s93\n"
"buffer_store_dwordx2 v[46:47], off, s[64:67], s74 sc1\n"
"v_mfma_f32_16x16x32_bf16 v[140:143], v[64:67], v[80:83], v[140:143]\n"
"v_mfma_f32_16x16x32_bf16 v[144:147], v[64:67], v[84:87], v[144:147]\n"
"buffer_load_dwordx4 v5, s[56:59], s90 offen lds\n"
"v_mfma_f32_16x16x32_bf16 v[148:151], v[64:67], v[88:91], v[148:151]\n"
"v_mfma_f32_16x16x32_bf16 v[132:135], v[68:71], v[92:95], v[132:135]\n"
"ds_read_b128 v[60:63], v8 offset:50176\n"
"s_add_u32 s73, s73, s98\n"
"v_mfma_f32_16x16x32_bf16 v[136:139], v[68:71], v[96:99], v[136:139]\n"
"v_mfma_f32_16x16x32_bf16 v[140:143], v[68:71], v[100:103], v[140:143]\n"
"v_mfma_f32_16x16x32_bf16 v[144:147], v[68:71], v[104:107], v[144:147]\n"
"buffer_load_dwordx4 v6, s[56:59], s90 offen offset:1024 lds\n"
"v_mfma_f32_16x16x32_bf16 v[148:151], v[68:71], v[108:111], v[148:151]\n"
"s_add_u32 s98, s98, 10240\n"
"s_waitcnt lgkmcnt(1)\n"
"v_mfma_f32_16x16x32_bf16 v[112:115], v[56:59], a[0:3], 0\n"
"ds_read_b128 v[64:67], v9 offset:49152\n"
"v_cvt_pk_bf16_f32 v132, v132, v133\n"
"v_mfma_f32_16x16x32_bf16 v[116:119], v[56:59], a[4:7], 0\n"
"v_cvt_pk_bf16_f32 v133, v134, v135\n"
"v_cvt_pk_bf16_f32 v134, v136, v137\n"
"v_mfma_f32_16x16x32_bf16 v[120:123], v[56:59], a[8:11], 0\n"
"buffer_load_dwordx4 v5, s[56:59], s91 offen offset:2048 lds\n"
"v_cvt_pk_bf16_f32 v135, v138, v139\n"
"v_mfma_f32_16x16x32_bf16 v[124:127], v[56:59], a[12:15], 0\n"
"ds_read_b128 v[68:71], v9 offset:50176\n"
"v_cvt_pk_bf16_f32 v136, v140, v141\n"
"v_mfma_f32_16x16x32_bf16 v[128:131], v[56:59], a[16:19], 0\n"
"v_cvt_pk_bf16_f32 v137, v142, v143\n"
"s_waitcnt lgkmcnt(2)\n"
"v_mfma_f32_16x16x32_bf16 v[112:115], v[60:63], a[20:23], v[112:115]\n"
"v_cvt_pk_bf16_f32 v138, v144, v145\n"
"v_cvt_pk_bf16_f32 v139, v146, v147\n"
"s_waitcnt vmcnt(14)\n"
"v_mfma_f32_16x16x32_bf16 v[26:29], v[50:53], v[38:41], v[26:29]\n"
"v_mfma_f32_16x16x32_bf16 v[116:119], v[60:63], a[24:27], v[116:119]\n"
"buffer_load_dwordx4 v6, s[56:59], s91 offen offset:3072 lds\n"
"v_cvt_pk_bf16_f32 v140, v148, v149\n"
"s_waitcnt vmcnt(12)\n"
"v_mfma_f32_16x16x32_bf16 v[30:33], v[50:53], v[40:43], v[30:33]\n"
"v_mfma_f32_16x16x32_bf16 v[120:123], v[60:63], a[28:31], v[120:123]\n"
"s_add_u32 m0, s97, 49152\n"
"v_cvt_pk_bf16_f32 v141, v150, v151\n"
"v_mfma_f32_16x16x32_bf16 v[14:17], v[52:55], v[34:37], v[14:17]\n"
"v_mfma_f32_16x16x32_bf16 v[124:127], v[60:63], a[32:35], v[124:127]\n"
"s_add_u32 s90, s86, s93\n"
"v_mfma_f32_16x16x32_bf16 v[18:21], v[50:53], v[34:37], v[18:21]\n"
"v_mfma_f32_16x16x32_bf16 v[128:131], v[60:63], a[36:39], v[128:131]\n"
"buffer_store_dwordx4 v[132:135], v48, s[64:67], s73 offen sc1\n"
"v_mfma_f32_16x16x32_bf16 v[22:25], v[52:55], v[38:41], v[22:25]\n"
"s_waitcnt lgkmcnt(0) vmcnt(11)\n"
"s_barrier\n"
"v_mfma_f32_16x16x32_bf16 v[112:115], v[64:67], a[40:43], v[112:115]\n"
"ds_read_b64_tr_b16 v[56:57], v10 offset:0\n"
"v_mfma_f32_16x16x32_bf16 v[116:119], v[64:67], a[44:47], v[116:119]\n"
"ds_read_b64_tr_b16 v[58:59], v10 offset:8192\n"
"s_add_u32 s91, s87, s93\n"
"v_mfma_f32_16x16x32_bf16 v[120:123], v[64:67], a[48:51], v[120:123]\n"
"v_mfma_f32_16x16x32_bf16 v[124:127], v[64:67], a[52:55], v[124:127]\n"
"buffer_load_dwordx4 v5, s[56:59], s90 offen lds\n"
"v_mfma_f32_16x16x32_bf16 v[128:131], v[64:67], a[56:59], v[128:131]\n"
"v_mfma_f32_16x16x32_bf16 v[112:115], v[68:71], a[60:63], v[112:115]\n"
"ds_read_b64_tr_b16 v[60:61], v10 offset:1024\n"
"v_mfma_f32_16x16x32_bf16 v[116:119], v[68:71], a[64:67], v[116:119]\n"
"ds_read_b64_tr_b16 v[62:63], v10 offset:9216\n"
"v_mfma_f32_16x16x32_bf16 v[120:123], v[68:71], a[68:71], v[120:123]\n"
"v_mfma_f32_16x16x32_bf16 v[124:127], v[68:71], a[72:75], v[124:127]\n"
"buffer_load_dwordx4 v6, s[56:59], s90 offen offset:1024 lds\n"
"v_mfma_f32_16x16x32_bf16 v[128:131], v[68:71], a[76:79], v[128:131]\n"
"buffer_store_dwordx4 v[136:139], v48, s[64:67], s73 offen offset:1024 sc1\n"
"s_waitcnt lgkmcnt(2)\n"
"v_mfma_f32_16x16x32_bf16 v[112:115], v[56:59], a[80:83], v[112:115]\n"
"ds_read_b64_tr_b16 v[64:65], v10 offset:2048\n"
"v_mfma_f32_16x16x32_bf16 v[116:119], v[56:59], a[84:87], v[116:119]\n"
"ds_read_b64_tr_b16 v[66:67], v10 offset:10240\n"
"v_mfma_f32_16x16x32_bf16 v[120:123], v[56:59], a[88:91], v[120:123]\n"
"buffer_load_dwordx4 v5, s[56:59], s91 offen offset:2048 lds\n"
"v_mfma_f32_16x16x32_bf16 v[124:127], v[56:59], a[92:95], v[124:127]\n"
"ds_read_b64_tr_b16 v[68:69], v10 offset:3072\n"
"v_mfma_f32_16x16x32_bf16 v[128:131], v[56:59], a[96:99], v[128:131]\n"
"ds_read_b64_tr_b16 v[70:71], v10 offset:11264\n"
"s_waitcnt lgkmcnt(4)\n"
"v_mfma_f32_16x16x32_bf16 v[112:115], v[60:63], a[100:103], v[112:115]\n"
"v_mfma_f32_16x16x32_bf16 v[116:119], v[60:63], a[104:107], v[116:119]\n"
"buffer_load_dwordx4 v6, s[56:59], s91 offen offset:3072 lds\n"
"v_mfma_f32_16x16x32_bf16 v[120:123], v[60:63], a[108:111], v[120:123]\n"
"s_add_u32 m0, s97, 0\n"
"s_add_u32 s93, s93, 128\n"
"v_mfma_f32_16x16x32_bf16 v[124:127], v[60:63], a[112:115], v[124:127]\n"
"s_cmp_eq_u32 s93, 1152\n"
"s_cselect_b32 s93, 0, s93\n"
"v_mfma_f32_16x16x32_bf16 v[128:131], v[60:63], a[116:119], v[128:131]\n"
"buffer_store_dwordx2 v[140:141], v49, s[64:67], s73 offen sc1\n"
"s_waitcnt lgkmcnt(0) vmcnt(12)\n"
"s_barrier\n"
"v_mfma_f32_16x16x32_bf16 v[112:115], v[64:67], a[120:123], v[112:115]\n"
"ds_read_b64_tr_b16 v[56:57], v10 offset:16384\n"
"s_lshl_b32 s74, s72, 11\n"
"v_mfma_f32_16x16x32_bf16 v[116:119], v[64:67], a[124:127], v[116:119]\n"
"ds_read_b64_tr_b16 v[58:59], v10 offset:24576\n"
"s_mul_i32 s90, s93, 576\n"
"v_mfma_f32_16x16x32_bf16 v[120:123], v[64:67], a[128:131], v[120:123]\n"
"s_add_u32 s90, s90, s92\n"
"v_cmp_ne_u64 vcc, s[88:89], v[44:45]\n"
"v_mfma_f32_16x16x32_bf16 v[124:127], v[64:67], a[132:135], v[124:127]\n"
"buffer_load_dwordx4 v3, s[40:43], s90 offen lds\n"
"s_cbranch_vccnz .Lfz_rf3\n"
".Lfz_cf3:\n"
"v_mfma_f32_16x16x32_bf16 v[128:131], v[64:67], a[136:139], v[128:131]\n"
"s_add_u32 s79, s75, 30720\n"
"v_mfma_f32_16x16x32_bf16 v[112:115], v[68:71], a[140:143], v[112:115]\n"
"ds_read_b64_tr_b16 v[60:61], v10 offset:17408\n"
"v_mfma_f32_16x16x32_bf16 v[116:119], v[68:71], a[144:147], v[116:119]\n"
"ds_read_b64_tr_b16 v[62:63], v10 offset:25600\n"
"v_mfma_f32_16x16x32_bf16 v[120:123], v[68:71], a[148:151], v[120:123]\n"
"v_mfma_f32_16x16x32_bf16 v[124:127], v[68:71], a[152:155], v[124:127]\n"
"buffer_load_dwordx4 v3, s[44:47], s90 offen offset:1024 lds\n"
"v_mfma_f32_16x16x32_bf16 v[128:131], v[68:71], a[156:159], v[128:131]\n"
"s_add_u32 s74, s74, s99\n"
"buffer_load_dwordx4 v[34:37], v48, s[64:67], s79 offen sc1\n"
"s_waitcnt lgkmcnt(2)\n"
"v_mfma_f32_16x16x32_bf16 v[112:115], v[56:59], a[160:163], v[112:115]\n"
"ds_read_b64_tr_b16 v[64:65], v10 offset:18432\n"
"v_mfma_f32_16x16x32_bf16 v[116:119], v[56:59], a[164:167], v[116:119]\n"
"ds_read_b64_tr_b16 v[66:67], v10 offset:26624\n"
"v_mfma_f32_16x16x32_bf16 v[120:123], v[56:59], a[168:171], v[120:123]\n"
"buffer_load_dwordx4 v4, s[40:43], s90 offen offset:2048 lds\n"
"v_mfma_f32_16x16x32_bf16 v[124:127], v[56:59], a[172:175], v[124:127]\n"
"ds_read_b64_tr_b16 v[68:69], v10 offset:19456\n"
"v_mfma_f32_16x16x32_bf16 v[128:131], v[56:59], a[176:179], v[128:131]\n"
"ds_read_b64_tr_b16 v[70:71], v10 offset:27648\n"
"s_waitcnt lgkmcnt(4)\n"
"v_mfma_f32_16x16x32_bf16 v[112:115], v[60:63], a[180:183], v[112:115]\n"
"v_mfma_f32_16x16x32_bf16 v[116:119], v[60:63], a[184:187], v[116:119]\n"
"buffer_load_dwordx4 v4, s[44:47], s90 offen offset:3072 lds\n"
"v_mfma_f32_16x16x32_bf16 v[120:123], v[60:63], a[188:191], v[120:123]\n"
"s_add_u32 m0, s97, 16384\n"
"v_mfma_f32_16x16x32_bf16 v[124:127], v[60:63], a[192:195], v[124:127]\n"
"s_add_u32 s90, s80, s93\n"
"v_mfma_f32_16x16x32_bf16 v[128:131], v[60:63], a[196:199], v[128:131]\n"
"buffer_load_dwordx4 v[38:41], v48, s[64:67], s79 offen offset:1024 sc1\n"
"s_waitcnt lgkmcnt(0) vmcnt(13)\n"
"s_barrier\n"
"v_mfma_f32_16x16x32_bf16 v[112:115], v[64:67], a[200:203], v[112:115]\n"
"ds_read_b64_tr_b16 v[56:57], v10 offset:32768\n"
"s_add_u32 s99, s99, 256\n"
"v_mfma_f32_16x16x32_bf16 v[116:119], v[64:67], a[204:207], v[116:119]\n"
"ds_read_b64_tr_b16 v[58:59], v10 offset:40960\n"
"s_add_u32 s91, s81, s93\n"
"v_mfma_f32_16x16x32_bf16 v[120:123], v[64:67], a[208:211], v[120:123]\n"
"v_mfma_f32_16x16x32_bf16 v[124:127], v[64:67], a[212:215], v[124:127]\n"
"buffer_load_dwordx4 v5, s[56:59], s90 offen lds\n"
"v_mfma_f32_16x16x32_bf16 v[128:131], v[64:67], a[216:219], v[128:131]\n"
"v_mfma_f32_16x16x32_bf16 v[112:115], v[68:71], a[220:223], v[112:115]\n"
"ds_read_b64_tr_b16 v[60:61], v10 offset:33792\n"
"v_mfma_f32_16x16x32_bf16 v[116:119], v[68:71], a[224:227], v[116:119]\n"
"ds_read_b64_tr_b16 v[62:63], v10 offset:41984\n"
"v_mfma_f32_16x16x32_bf16 v[120:123], v[68:71], a[228:231], v[120:123]\n"
"v_mfma_f32_16x16x32_bf16 v[124:127], v[68:71], a[232:235], v[124:127]\n"
"buffer_load_dwordx4 v6, s[56:59], s90 offen offset:1024 lds\n"
"v_mfma_f32_16x16x32_bf16 v[128:131], v[68:71], a[236:239], v[128:131]\n"
"buffer_load_dwordx2 v[42:43], v49, s[64:67], s79 offen sc1\n"
"s_waitcnt lgkmcnt(2)\n"
"v_mfma_f32_16x16x32_bf16 v[112:115], v[56:59], a[240:243], v[112:115]\n"
"ds_read_b64_tr_b16 v[64:65], v10 offset:34816\n"
"v_mfma_f32_16x16x32_bf16 v[116:119], v[56:59], a[244:247], v[116:119]\n"
"ds_read_b64_tr_b16 v[66:67], v10 offset:43008\n"
"v_mfma_f32_16x16x32_bf16 v[120:123], v[56:59], a[248:251], v[120:123]\n"
"buffer_load_dwordx4 v5, s[56:59], s91 offen offset:2048 lds\n"
"v_mfma_f32_16x16x32_bf16 v[124:127], v[56:59], a[252:255], v[124:127]\n"
"ds_read_b64_tr_b16 v[68:69], v10 offset:35840\n"
"v_mfma_f32_16x16x32_bf16 v[128:131], v[56:59], v[152:155], v[128:131]\n"
"ds_read_b64_tr_b16 v[70:71], v10 offset:44032\n"
"s_waitcnt lgkmcnt(4)\n"
"v_mfma_f32_16x16x32_bf16 v[112:115], v[60:63], v[156:159], v[112:115]\n"
"v_mfma_f32_16x16x32_bf16 v[116:119], v[60:63], v[160:163], v[116:119]\n"
"buffer_load_dwordx4 v6, s[56:59], s91 offen offset:3072 lds\n"
"v_mfma_f32_16x16x32_bf16 v[120:123], v[60:63], v[164:167], v[120:123]\n"
"s_add_u32 m0, s97, 32768\n"
"v_mfma_f32_16x16x32_bf16 v[124:127], v[60:63], v[168:171], v[124:127]\n"
"s_add_u32 s90, s82, s93\n"
"v_mfma_f32_16x16x32_bf16 v[128:131], v[60:63], v[172:175], v[128:131]\n"
"s_waitcnt lgkmcnt(0) vmcnt(12)\n"
"s_barrier\n"
"v_mfma_f32_16x16x32_bf16 v[112:115], v[64:67], v[176:179], v[112:115]\n"
"ds_read_b64_tr_b16 v[56:57], v10 offset:49152\n"
"v_mfma_f32_16x16x32_bf16 v[116:119], v[64:67], v[180:183], v[116:119]\n"
"ds_read_b64_tr_b16 v[58:59], v10 offset:57344\n"
"s_add_u32 s91, s83, s93\n"
"v_mfma_f32_16x16x32_bf16 v[120:123], v[64:67], v[184:187], v[120:123]\n"
"s_add_u32 s72, s72, 1\n"
"s_add_u32 s77, s76, 1024\n"
"buffer_load_dwordx2 v[44:45], off, s[64:67], s77 sc1\n"
"v_mfma_f32_16x16x32_bf16 v[124:127], v[64:67], v[188:191], v[124:127]\n"
"buffer_load_dwordx4 v5, s[56:59], s90 offen lds\n"
"v_mfma_f32_16x16x32_bf16 v[128:131], v[64:67], v[192:195], v[128:131]\n"
"v_mfma_f32_16x16x32_bf16 v[112:115], v[68:71], v[196:199], v[112:115]\n"
"ds_read_b64_tr_b16 v[60:61], v10 offset:50176\n"
"v_mfma_f32_16x16x32_bf16 v[116:119], v[68:71], v[200:203], v[116:119]\n"
"ds_read_b64_tr_b16 v[62:63], v10 offset:58368\n"
"v_mfma_f32_16x16x32_bf16 v[120:123], v[68:71], v[204:207], v[120:123]\n"
"s_cmp_eq_u32 s72, 9\n"
"s_cselect_b32 s72, 0, s72\n"
"v_mfma_f32_16x16x32_bf16 v[124:127], v[68:71], v[208:211], v[124:127]\n"
"buffer_load_dwordx4 v6, s[56:59], s90 offen offset:1024 lds\n"
"v_mfma_f32_16x16x32_bf16 v[128:131], v[68:71], v[212:215], v[128:131]\n"
"s_waitcnt lgkmcnt(2)\n"
"v_mfma_f32_16x16x32_bf16 v[112:115], v[56:59], v[216:219], v[112:115]\n"
"ds_read_b64_tr_b16 v[64:65], v10 offset:51200\n"
"v_mfma_f32_16x16x32_bf16 v[116:119], v[56:59], v[220:223], v[116:119]\n"
"ds_read_b64_tr_b16 v[66:67], v10 offset:59392\n"
"v_mfma_f32_16x16x32_bf16 v[120:123], v[56:59], v[224:227], v[120:123]\n"
"buffer_load_dwordx4 v5, s[56:59], s91 offen offset:2048 lds\n"
"v_mfma_f32_16x16x32_bf16 v[124:127], v[56:59], v[228:231], v[124:127]\n"
"ds_read_b64_tr_b16 v[68:69], v10 offset:52224\n"
"v_mfma_f32_16x16x32_bf16 v[128:131], v[56:59], v[232:235], v[128:131]\n"
"ds_read_b64_tr_b16 v[70:71], v10 offset:60416\n"
"s_waitcnt lgkmcnt(4)\n"
"v_mfma_f32_16x16x32_bf16 v[112:115], v[60:63], v[236:239], v[112:115]\n"
"v_mfma_f32_16x16x32_bf16 v[116:119], v[60:63], v[240:243], v[116:119]\n"
"buffer_load_dwordx4 v6, s[56:59], s91 offen offset:3072 lds\n"
"v_mfma_f32_16x16x32_bf16 v[120:123], v[60:63], v[244:247], v[120:123]\n"
"s_add_u32 m0, s97, 49152\n"
"v_mfma_f32_16x16x32_bf16 v[124:127], v[60:63], v[248:251], v[124:127]\n"
"s_add_u32 s90, s84, s93\n"
"s_mul_i32 s73, s72, 92160\n"
"v_mfma_f32_16x16x32_bf16 v[128:131], v[60:63], v[252:255], v[128:131]\n"
"s_waitcnt lgkmcnt(0) vmcnt(11)\n"
"s_barrier\n"
"v_mfma_f32_16x16x32_bf16 v[112:115], v[64:67], v[72:75], v[112:115]\n"
"ds_read_b128 v[56:59], v8 offset:0\n"
"v_mfma_f32_16x16x32_bf16 v[116:119], v[64:67], v[76:79], v[116:119]\n"
"s_add_u32 s91, s85, s93\n"
"buffer_store_dwordx2 v[46:47], off, s[64:67], s74 sc1\n"
"v_mfma_f32_16x16x32_bf16 v[120:123], v[64:67], v[80:83], v[120:123]\n"
"v_mfma_f32_16x16x32_bf16 v[124:127], v[64:67], v[84:87], v[124:127]\n"
"buffer_load_dwordx4 v5, s[56:59], s90 offen lds\n"
"v_mfma_f32_16x16x32_bf16 v[128:131], v[64:67], v[88:91], v[128:131]\n"
"v_mfma_f32_16x16x32_bf16 v[112:115], v[68:71], v[92:95], v[112:115]\n"
"ds_read_b128 v[60:63], v8 offset:1024\n"
"s_add_u32 s73, s73, s98\n"
"v_mfma_f32_16x16x32_bf16 v[116:119], v[68:71], v[96:99], v[116:119]\n"
"v_mfma_f32_16x16x32_bf16 v[120:123], v[68:71], v[100:103], v[120:123]\n"
"v_mfma_f32_16x16x32_bf16 v[124:127], v[68:71], v[104:107], v[124:127]\n"
"buffer_load_dwordx4 v6, s[56:59], s90 offen offset:1024 lds\n"
"v_mfma_f32_16x16x32_bf16 v[128:131], v[68:71], v[108:111], v[128:131]\n"
"s_add_u32 s98, s98, 10240\n"
"s_waitcnt lgkmcnt(1)\n"
"v_mfma_f32_16x16x32_bf16 v[132:135], v[56:59], a[0:3], 0\n"
"ds_read_b128 v[64:67], v9 offset:0\n"
"v_cvt_pk_bf16_f32 v112, v112, v113\n"
"v_mfma_f32_16x16x32_bf16 v[136:139], v[56:59], a[4:7], 0\n"
"v_cvt_pk_bf16_f32 v113, v114, v115\n"
"v_cvt_pk_bf16_f32 v114, v116, v117\n"
"v_mfma_f32_16x16x32_bf16 v[140:143], v[56:59], a[8:11], 0\n"
"buffer_load_dwordx4 v5, s[56:59], s91 offen offset:2048 lds\n"
"v_cvt_pk_bf16_f32 v115, v118, v119\n"
"v_mfma_f32_16x16x32_bf16 v[144:147], v[56:59], a[12:15], 0\n"
"ds_read_b128 v[68:71], v9 offset:1024\n"
"v_cvt_pk_bf16_f32 v116, v120, v121\n"
"v_mfma_f32_16x16x32_bf16 v[148:151], v[56:59], a[16:19], 0\n"
"v_cvt_pk_bf16_f32 v117, v122, v123\n"
"s_waitcnt lgkmcnt(2)\n"
"v_mfma_f32_16x16x32_bf16 v[132:135], v[60:63], a[20:23], v[132:135]\n"
"v_cvt_pk_bf16_f32 v118, v124, v125\n"
"v_cvt_pk_bf16_f32 v119, v126, v127\n"
"s_waitcnt vmcnt(14)\n"
"v_mfma_f32_16x16x32_bf16 v[26:29], v[50:53], v[38:41], v[26:29]\n"
"v_mfma_f32_16x16x32_bf16 v[136:139], v[60:63], a[24:27], v[136:139]\n"
"buffer_load_dwordx4 v6, s[56:59], s91 offen offset:3072 lds\n"
"v_cvt_pk_bf16_f32 v120, v128, v129\n"
"s_waitcnt vmcnt(12)\n"
"v_mfma_f32_16x16x32_bf16 v[30:33], v[50:53], v[40:43], v[30:33]\n"
"v_mfma_f32_16x16x32_bf16 v[140:143], v[60:63], a[28:31], v[140:143]\n"
"s_add_u32 m0, s97, 0\n"
"v_cvt_pk_bf16_f32 v121, v130, v131\n"
"v_mfma_f32_16x16x32_bf16 v[14:17], v[52:55], v[34:37], v[14:17]\n"
"v_mfma_f32_16x16x32_bf16 v[144:147], v[60:63], a[32:35], v[144:147]\n"
"s_add_u32 s90, s86, s93\n"
"v_mfma_f32_16x16x32_bf16 v[18:21], v[50:53], v[34:37], v[18:21]\n"
"v_mfma_f32_16x16x32_bf16 v[148:151], v[60:63], a[36:39], v[148:151]\n"
"buffer_store_dwordx4 v[112:115], v48, s[64:67], s73 offen sc1\n"
"v_mfma_f32_16x16x32_bf16 v[22:25], v[52:55], v[38:41], v[22:25]\n"
"s_waitcnt lgkmcnt(0) vmcnt(11)\n"
"s_barrier\n"
"v_mfma_f32_16x16x32_bf16 v[132:135], v[64:67], a[40:43], v[132:135]\n"
"ds_read_b64_tr_b16 v[56:57], v10 offset:16384\n"
"v_mfma_f32_16x16x32_bf16 v[136:139], v[64:67], a[44:47], v[136:139]\n"
"ds_read_b64_tr_b16 v[58:59], v10 offset:24576\n"
"s_add_u32 s91, s87, s93\n"
"v_mfma_f32_16x16x32_bf16 v[140:143], v[64:67], a[48:51], v[140:143]\n"
"v_mfma_f32_16x16x32_bf16 v[144:147], v[64:67], a[52:55], v[144:147]\n"
"buffer_load_dwordx4 v5, s[56:59], s90 offen lds\n"
"v_mfma_f32_16x16x32_bf16 v[148:151], v[64:67], a[56:59], v[148:151]\n"
"v_mfma_f32_16x16x32_bf16 v[132:135], v[68:71], a[60:63], v[132:135]\n"
"ds_read_b64_tr_b16 v[60:61], v10 offset:17408\n"
"v_mfma_f32_16x16x32_bf16 v[136:139], v[68:71], a[64:67], v[136:139]\n"
"ds_read_b64_tr_b16 v[62:63], v10 offset:25600\n"
"v_mfma_f32_16x16x32_bf16 v[140:143], v[68:71], a[68:71], v[140:143]\n"
"v_mfma_f32_16x16x32_bf16 v[144:147], v[68:71], a[72:75], v[144:147]\n"
"buffer_load_dwordx4 v6, s[56:59], s90 offen offset:1024 lds\n"
"v_mfma_f32_16x16x32_bf16 v[148:151], v[68:71], a[76:79], v[148:151]\n"
"buffer_store_dwordx4 v[116:119], v48, s[64:67], s73 offen offset:1024 sc1\n"
"s_waitcnt lgkmcnt(2)\n"
"v_mfma_f32_16x16x32_bf16 v[132:135], v[56:59], a[80:83], v[132:135]\n"
"ds_read_b64_tr_b16 v[64:65], v10 offset:18432\n"
"v_mfma_f32_16x16x32_bf16 v[136:139], v[56:59], a[84:87], v[136:139]\n"
"ds_read_b64_tr_b16 v[66:67], v10 offset:26624\n"
"v_mfma_f32_16x16x32_bf16 v[140:143], v[56:59], a[88:91], v[140:143]\n"
"buffer_load_dwordx4 v5, s[56:59], s91 offen offset:2048 lds\n"
"v_mfma_f32_16x16x32_bf16 v[144:147], v[56:59], a[92:95], v[144:147]\n"
"ds_read_b64_tr_b16 v[68:69], v10 offset:19456\n"
"v_mfma_f32_16x16x32_bf16 v[148:151], v[56:59], a[96:99], v[148:151]\n"
"ds_read_b64_tr_b16 v[70:71], v10 offset:27648\n"
"s_waitcnt lgkmcnt(4)\n"
"v_mfma_f32_16x16x32_bf16 v[132:135], v[60:63], a[100:103], v[132:135]\n"
"v_mfma_f32_16x16x32_bf16 v[136:139], v[60:63], a[104:107], v[136:139]\n"
"buffer_load_dwordx4 v6, s[56:59], s91 offen offset:3072 lds\n"
"v_mfma_f32_16x16x32_bf16 v[140:143], v[60:63], a[108:111], v[140:143]\n"
"s_add_u32 m0, s97, 16384\n"
"s_add_u32 s93, s93, 128\n"
"v_mfma_f32_16x16x32_bf16 v[144:147], v[60:63], a[112:115], v[144:147]\n"
"s_cmp_eq_u32 s93, 1152\n"
"s_cselect_b32 s93, 0, s93\n"
"v_mfma_f32_16x16x32_bf16 v[148:151], v[60:63], a[116:119], v[148:151]\n"
"buffer_store_dwordx2 v[120:121], v49, s[64:67], s73 offen sc1\n"
"s_waitcnt lgkmcnt(0) vmcnt(12)\n"
"s_barrier\n"
"v_mfma_f32_16x16x32_bf16 v[132:135], v[64:67], a[120:123], v[132:135]\n"
"ds_read_b64_tr_b16 v[56:57], v10 offset:32768\n"
"s_lshl_b32 s74, s72, 11\n"
"v_mfma_f32_16x16x32_bf16 v[136:139], v[64:67], a[124:127], v[136:139]\n"
"ds_read_b64_tr_b16 v[58:59], v10 offset:40960\n"
"s_mul_i32 s90, s93, 576\n"
"v_mfma_f32_16x16x32_bf16 v[140:143], v[64:67], a[128:131], v[140:143]\n"
"s_add_u32 s90, s90, s92\n"
"v_cmp_ne_u64 vcc, s[88:89], v[44:45]\n"
"v_mfma_f32_16x16x32_bf16 v[144:147], v[64:67], a[132:135], v[144:147]\n"
"buffer_load_dwordx4 v3, s[40:43], s90 offen lds\n"
"s_cbranch_vccnz .Lfz_rf4\n"
".Lfz_cf4:\n"
"v_mfma_f32_16x16x32_bf16 v[148:151], v[64:67], a[136:139], v[148:151]\n"
"s_add_u32 s79, s75, 40960\n"
"v_mfma_f32_16x16x32_bf16 v[132:135], v[68:71], a[140:143], v[132:135]\n"
"ds_read_b64_tr_b16 v[60:61], v10 offset:33792\n"
"v_mfma_f32_16x16x32_bf16 v[136:139], v[68:71], a[144:147], v[136:139]\n"
"ds_read_b64_tr_b16 v[62:63], v10 offset:41984\n"
"v_mfma_f32_16x16x32_bf16 v[140:143], v[68:71], a[148:151], v[140:143]\n"
"v_mfma_f32_16x16x32_bf16 v[144:147], v[68:71], a[152:155], v[144:147]\n"
"buffer_load_dwordx4 v3, s[44:47], s90 offen offset:1024 lds\n"
"v_mfma_f32_16x16x32_bf16 v[148:151], v[68:71], a[156:159], v[148:151]\n"
"s_add_u32 s74, s74, s99\n"
"buffer_load_dwordx4 v[34:37], v48, s[64:67], s79 offen sc1\n"
"s_waitcnt lgkmcnt(2)\n"
"v_mfma_f32_16x16x32_bf16 v[132:135], v[56:59], a[160:163], v[132:135]\n"
"ds_read_b64_tr_b16 v[64:65], v10 offset:34816\n"
"v_mfma_f32_16x16x32_bf16 v[136:139], v[56:59], a[164:167], v[136:139]\n"
"ds_read_b64_tr_b16 v[66:67], v10 offset:43008\n"
"v_mfma_f32_16x16x32_bf16 v[140:143], v[56:59], a[168:171], v[140:143]\n"
"buffer_load_dwordx4 v4, s[40:43], s90 offen offset:2048 lds\n"
"v_mfma_f32_16x16x32_bf16 v[144:147], v[56:59], a[172:175], v[144:147]\n"
"ds_read_b64_tr_b16 v[68:69], v10 offset:35840\n"
"v_mfma_f32_16x16x32_bf16 v[148:151], v[56:59], a[176:179], v[148:151]\n"
"ds_read_b64_tr_b16 v[70:71], v10 offset:44032\n"
"s_waitcnt lgkmcnt(4)\n"
"v_mfma_f32_16x16x32_bf16 v[132:135], v[60:63], a[180:183], v[132:135]\n"
"v_mfma_f32_16x16x32_bf16 v[136:139], v[60:63], a[184:187], v[136:139]\n"
"buffer_load_dwordx4 v4, s[44:47], s90 offen offset:3072 lds\n"
"v_mfma_f32_16x16x32_bf16 v[140:143], v[60:63], a[188:191], v[140:143]\n"
"s_add_u32 m0, s97, 32768\n"
"v_mfma_f32_16x16x32_bf16 v[144:147], v[60:63], a[192:195], v[144:147]\n"
"s_add_u32 s90, s80, s93\n"
"v_mfma_f32_16x16x32_bf16 v[148:151], v[60:63], a[196:199], v[148:151]\n"
"buffer_load_dwordx4 v[38:41], v48, s[64:67], s79 offen offset:1024 sc1\n"
"s_waitcnt lgkmcnt(0) vmcnt(13)\n"
"s_barrier\n"
"v_mfma_f32_16x16x32_bf16 v[132:135], v[64:67], a[200:203], v[132:135]\n"
"ds_read_b64_tr_b16 v[56:57], v10 offset:49152\n"
"s_add_u32 s99, s99, 256\n"
"v_mfma_f32_16x16x32_bf16 v[136:139], v[64:67], a[204:207], v[136:139]\n"
"ds_read_b64_tr_b16 v[58:59], v10 offset:57344\n"
"s_add_u32 s91, s81, s93\n"
"v_mfma_f32_16x16x32_bf16 v[140:143], v[64:67], a[208:211], v[140:143]\n"
"v_mfma_f32_16x16x32_bf16 v[144:147], v[64:67], a[212:215], v[144:147]\n"
"buffer_load_dwordx4 v5, s[56:59], s90 offen lds\n"
"v_mfma_f32_16x16x32_bf16 v[148:151], v[64:67], a[216:219], v[148:151]\n"
"v_mfma_f32_16x16x32_bf16 v[132:135], v[68:71], a[220:223], v[132:135]\n"
"ds_read_b64_tr_b16 v[60:61], v10 offset:50176\n"
"v_mfma_f32_16x16x32_bf16 v[136:139], v[68:71], a[224:227], v[136:139]\n"
"ds_read_b64_tr_b16 v[62:63], v10 offset:58368\n"
"v_mfma_f32_16x16x32_bf16 v[140:143], v[68:71], a[228:231], v[140:143]\n"
"v_mfma_f32_16x16x32_bf16 v[144:147], v[68:71], a[232:235], v[144:147]\n"
"buffer_load_dwordx4 v6, s[56:59], s90 offen offset:1024 lds\n"
"v_mfma_f32_16x16x32_bf16 v[148:151], v[68:71], a[236:239], v[148:151]\n"
"buffer_load_dwordx2 v[42:43], v49, s[64:67], s79 offen sc1\n"
"s_waitcnt lgkmcnt(2)\n"
"v_mfma_f32_16x16x32_bf16 v[132:135], v[56:59], a[240:243], v[132:135]\n"
"ds_read_b64_tr_b16 v[64:65], v10 offset:51200\n"
"v_mfma_f32_16x16x32_bf16 v[136:139], v[56:59], a[244:247], v[136:139]\n"
"ds_read_b64_tr_b16 v[66:67], v10 offset:59392\n"
"v_mfma_f32_16x16x32_bf16 v[140:143], v[56:59], a[248:251], v[140:143]\n"
"buffer_load_dwordx4 v5, s[56:59], s91 offen offset:2048 lds\n"
"v_mfma_f32_16x16x32_bf16 v[144:147], v[56:59], a[252:255], v[144:147]\n"
"ds_read_b64_tr_b16 v[68:69], v10 offset:52224\n"
"v_mfma_f32_16x16x32_bf16 v[148:151], v[56:59], v[152:155], v[148:151]\n"
"ds_read_b64_tr_b16 v[70:71], v10 offset:60416\n"
"s_waitcnt lgkmcnt(4)\n"
"v_mfma_f32_16x16x32_bf16 v[132:135], v[60:63], v[156:159], v[132:135]\n"
"v_mfma_f32_16x16x32_bf16 v[136:139], v[60:63], v[160:163], v[136:139]\n"
"buffer_load_dwordx4 v6, s[56:59], s91 offen offset:3072 lds\n"
"v_mfma_f32_16x16x32_bf16 v[140:143], v[60:63], v[164:167], v[140:143]\n"
"s_add_u32 m0, s97, 49152\n"
"v_mfma_f32_16x16x32_bf16 v[144:147], v[60:63], v[168:171], v[144:147]\n"
"s_add_u32 s90, s82, s93\n"
"v_mfma_f32_16x16x32_bf16 v[148:151], v[60:63], v[172:175], v[148:151]\n"
"s_waitcnt lgkmcnt(0) vmcnt(12)\n"
"s_barrier\n"
"v_mfma_f32_16x16x32_bf16 v[132:135], v[64:67], v[176:179], v[132:135]\n"
"ds_read_b64_tr_b16 v[56:57], v10 offset:0\n"
"v_mfma_f32_16x16x32_bf16 v[136:139], v[64:67], v[180:183], v[136:139]\n"
"ds_read_b64_tr_b16 v[58:59], v10 offset:8192\n"
"s_add_u32 s91, s83, s93\n"
"v_mfma_f32_16x16x32_bf16 v[140:143], v[64:67], v[184:187], v[140:143]\n"
"s_add_u32 s72, s72, 1\n"
"s_add_u32 s77, s76, 1280\n"
"buffer_load_dwordx2 v[44:45], off, s[64:67], s77 sc1\n"
"v_mfma_f32_16x16x32_bf16 v[144:147], v[64:67], v[188:191], v[144:147]\n"
"buffer_load_dwordx4 v5, s[56:59], s90 offen lds\n"
"v_mfma_f32_16x16x32_bf16 v[148:151], v[64:67], v[192:195], v[148:151]\n"
"v_mfma_f32_16x16x32_bf16 v[132:135], v[68:71], v[196:199], v[132:135]\n"
"ds_read_b64_tr_b16 v[60:61], v10 offset:1024\n"
"v_mfma_f32_16x16x32_bf16 v[136:139], v[68:71], v[200:203], v[136:139]\n"
"ds_read_b64_tr_b16 v[62:63], v10 offset:9216\n"
"v_mfma_f32_16x16x32_bf16 v[140:143], v[68:71], v[204:207], v[140:143]\n"
"s_cmp_eq_u32 s72, 9\n"
"s_cselect_b32 s72, 0, s72\n"
"v_mfma_f32_16x16x32_bf16 v[144:147], v[68:71], v[208:211], v[144:147]\n"
"buffer_load_dwordx4 v6, s[56:59], s90 offen offset:1024 lds\n"
"v_mfma_f32_16x16x32_bf16 v[148:151], v[68:71], v[212:215], v[148:151]\n"
"s_waitcnt lgkmcnt(2)\n"
"v_mfma_f32_16x16x32_bf16 v[132:135], v[56:59], v[216:219], v[132:135]\n"
"ds_read_b64_tr_b16 v[64:65], v10 offset:2048\n"
"v_mfma_f32_16x16x32_bf16 v[136:139], v[56:59], v[220:223], v[136:139]\n"
"ds_read_b64_tr_b16 v[66:67], v10 offset:10240\n"
"v_mfma_f32_16x16x32_bf16 v[140:143], v[56:59], v[224:227], v[140:143]\n"
"buffer_load_dwordx4 v5, s[56:59], s91 offen offset:2048 lds\n"
"v_mfma_f32_16x16x32_bf16 v[144:147], v[56:59], v[228:231], v[144:147]\n"
"ds_read_b64_tr_b16 v[68:69], v10 offset:3072\n"
"v_mfma_f32_16x16x32_bf16 v[148:151], v[56:59], v[232:235], v[148:151]\n"
"ds_read_b64_tr_b16 v[70:71], v10 offset:11264\n"
"s_waitcnt lgkmcnt(4)\n"
"v_mfma_f32_16x16x32_bf16 v[132:135], v[60:63], v[236:239], v[132:135]\n"
"v_mfma_f32_16x16x32_bf16 v[136:139], v[60:63], v[240:243], v[136:139]\n"
"buffer_load_dwordx4 v6, s[56:59], s91 offen offset:3072 lds\n"
"v_mfma_f32_16x16x32_bf16 v[140:143], v[60:63], v[244:247], v[140:143]\n"
"s_add_u32 m0, s97, 0\n"
"v_mfma_f32_16x16x32_bf16 v[144:147], v[60:63], v[248:251], v[144:147]\n"
"s_add_u32 s90, s84, s93\n"
"s_mul_i32 s73, s72, 92160\n"
"v_mfma_f32_16x16x32_bf16 v[148:151], v[60:63], v[252:255], v[148:151]\n"
"s_waitcnt lgkmcnt(0) vmcnt(11)\n"
"s_barrier\n"
"v_mfma_f32_16x16x32_bf16 v[132:135], v[64:67], v[72:75], v[132:135]\n"
"ds_read_b128 v[56:59], v8 offset:16384\n"
"v_mfma_f32_16x16x32_bf16 v[136:139], v[64:67], v[76:79], v[136:139]\n"
"s_add_u32 s91, s85, s93\n"
"buffer_store_dwordx2 v[46:47], off, s[64:67], s74 sc1\n"
"v_mfma_f32_16x16x32_bf16 v[140:143], v[64:67], v[80:83], v[140:143]\n"
"v_mfma_f32_16x16x32_bf16 v[144:147], v[64:67], v[84:87], v[144:147]\n"
"buffer_load_dwordx4 v5, s[56:59], s90 offen lds\n"
"v_mfma_f32_16x16x32_bf16 v[148:151], v[64:67], v[88:91], v[148:151]\n"
"v_mfma_f32_16x16x32_bf16 v[132:135], v[68:71], v[92:95], v[132:135]\n"
"ds_read_b128 v[60:63], v8 offset:17408\n"
"s_add_u32 s73, s73, s98\n"
"v_mfma_f32_16x16x32_bf16 v[136:139], v[68:71], v[96:99], v[136:139]\n"
"v_mfma_f32_16x16x32_bf16 v[140:143], v[68:71], v[100:103], v[140:143]\n"
"v_mfma_f32_16x16x32_bf16 v[144:147], v[68:71], v[104:107], v[144:147]\n"
"buffer_load_dwordx4 v6, s[56:59], s90 offen offset:1024 lds\n"
"v_mfma_f32_16x16x32_bf16 v[148:151], v[68:71], v[108:111], v[148:151]\n"
"s_add_u32 s98, s98, 10240\n"
"s_waitcnt lgkmcnt(1)\n"
"v_mfma_f32_16x16x32_bf16 v[112:115], v[56:59], a[0:3], v[14:17]\n"
"ds_read_b128 v[64:67], v9 offset:16384\n"
"v_cvt_pk_bf16_f32 v132, v132, v133\n"
"v_mfma_f32_16x16x32_bf16 v[116:119], v[56:59], a[4:7], v[18:21]\n"
"v_cvt_pk_bf16_f32 v133, v134, v135\n"
"v_cvt_pk_bf16_f32 v134, v136, v137\n"
"v_mfma_f32_16x16x32_bf16 v[120:123], v[56:59], a[8:11], v[22:25]\n"
"buffer_load_dwordx4 v5, s[56:59], s91 offen offset:2048 lds\n"
"v_cvt_pk_bf16_f32 v135, v138, v139\n"
"v_mfma_f32_16x16x32_bf16 v[124:127], v[56:59], a[12:15], v[26:29]\n"
"ds_read_b128 v[68:71], v9 offset:17408\n"
"v_cvt_pk_bf16_f32 v136, v140, v141\n"
"v_mfma_f32_16x16x32_bf16 v[128:131], v[56:59], a[16:19], v[30:33]\n"
"v_cvt_pk_bf16_f32 v137, v142, v143\n"
"s_waitcnt lgkmcnt(2)\n"
"v_mfma_f32_16x16x32_bf16 v[112:115], v[60:63], a[20:23], v[112:115]\n"
"v_cvt_pk_bf16_f32 v138, v144, v145\n"
"v_cvt_pk_bf16_f32 v139, v146, v147\n"
"s_waitcnt vmcnt(14)\n"
"v_mfma_f32_16x16x32_bf16 v[124:127], v[50:53], v[38:41], v[124:127]\n"
"v_mfma_f32_16x16x32_bf16 v[116:119], v[60:63], a[24:27], v[116:119]\n"
"buffer_load_dwordx4 v6, s[56:59], s91 offen offset:3072 lds\n"
"v_cvt_pk_bf16_f32 v140, v148, v149\n"
"s_waitcnt vmcnt(12)\n"
"v_mfma_f32_16x16x32_bf16 v[128:131], v[50:53], v[40:43], v[128:131]\n"
"v_mfma_f32_16x16x32_bf16 v[120:123], v[60:63], a[28:31], v[120:123]\n"
"s_add_u32 m0, s97, 16384\n"
"v_cvt_pk_bf16_f32 v141, v150, v151\n"
"buffer_store_dwordx4 v[132:135], v48, s[64:67], s73 offen sc1\n"
"v_mfma_f32_16x16x32_bf16 v[112:115], v[52:55], v[34:37], v[112:115]\n"
"v_mfma_f32_16x16x32_bf16 v[124:127], v[60:63], a[32:35], v[124:127]\n"
"s_add_u32 s90, s86, s93\n"
"buffer_store_dwordx4 v[136:139], v48, s[64:67], s73 offen offset:1024 sc1\n"
"v_mfma_f32_16x16x32_bf16 v[116:119], v[50:53], v[34:37], v[116:119]\n"
"v_mfma_f32_16x16x32_bf16 v[128:131], v[60:63], a[36:39], v[128:131]\n"
"buffer_store_dwordx2 v[140:141], v49, s[64:67], s73 offen sc1\n"
"v_mfma_f32_16x16x32_bf16 v[120:123], v[52:55], v[38:41], v[120:123]\n"
"s_waitcnt lgkmcnt(0) vmcnt(13)\n"
"s_barrier\n"
"v_mfma_f32_16x16x32_bf16 v[112:115], v[64:67], a[40:43], v[112:115]\n"
"ds_read_b64_tr_b16 v[56:57], v10 offset:32768\n"
"v_mfma_f32_16x16x32_bf16 v[116:119], v[64:67], a[44:47], v[116:119]\n"
"ds_read_b64_tr_b16 v[58:59], v10 offset:40960\n"
"s_add_u32 s91, s87, s93\n"
"v_mfma_f32_16x16x32_bf16 v[120:123], v[64:67], a[48:51], v[120:123]\n"
"v_mfma_f32_16x16x32_bf16 v[124:127], v[64:67], a[52:55], v[124:127]\n"
"buffer_load_dwordx4 v5, s[56:59], s90 offen lds\n"
"v_mfma_f32_16x16x32_bf16 v[128:131], v[64:67], a[56:59], v[128:131]\n"
"v_mfma_f32_16x16x32_bf16 v[112:115], v[68:71], a[60:63], v[112:115]\n"
"ds_read_b64_tr_b16 v[60:61], v10 offset:33792\n"
"v_mfma_f32_16x16x32_bf16 v[116:119], v[68:71], a[64:67], v[116:119]\n"
"ds_read_b64_tr_b16 v[62:63], v10 offset:41984\n"
"v_mfma_f32_16x16x32_bf16 v[120:123], v[68:71], a[68:71], v[120:123]\n"
"v_mfma_f32_16x16x32_bf16 v[124:127], v[68:71], a[72:75], v[124:127]\n"
"buffer_load_dwordx4 v6, s[56:59], s90 offen offset:1024 lds\n"
"v_mfma_f32_16x16x32_bf16 v[128:131], v[68:71], a[76:79], v[128:131]\n"
"s_waitcnt lgkmcnt(2)\n"
"v_mfma_f32_16x16x32_bf16 v[112:115], v[56:59], a[80:83], v[112:115]\n"
"ds_read_b64_tr_b16 v[64:65], v10 offset:34816\n"
"v_mfma_f32_16x16x32_bf16 v[116:119], v[56:59], a[84:87], v[116:119]\n"
"ds_read_b64_tr_b16 v[66:67], v10 offset:43008\n"
"v_mfma_f32_16x16x32_bf16 v[120:123], v[56:59], a[88:91], v[120:123]\n"
"buffer_load_dwordx4 v5, s[56:59], s91 offen offset:2048 lds\n"
"v_mfma_f32_16x16x32_bf16 v[124:127], v[56:59], a[92:95], v[124:127]\n"
"ds_read_b64_tr_b16 v[68:69], v10 offset:35840\n"
"v_mfma_f32_16x16x32_bf16 v[128:131], v[56:59], a[96:99], v[128:131]\n"
"ds_read_b64_tr_b16 v[70:71], v10 offset:44032\n"
"s_waitcnt lgkmcnt(4)\n"
"v_mfma_f32_16x16x32_bf16 v[112:115], v[60:63], a[100:103], v[112:115]\n"
"v_mfma_f32_16x16x32_bf16 v[116:119], v[60:63], a[104:107], v[116:119]\n"
"buffer_load_dwordx4 v6, s[56:59], s91 offen offset:3072 lds\n"
"v_mfma_f32_16x16x32_bf16 v[120:123], v[60:63], a[108:111], v[120:123]\n"
"v_mfma_f32_16x16x32_bf16 v[124:127], v[60:63], a[112:115], v[124:127]\n"
"v_mfma_f32_16x16x32_bf16 v[128:131], v[60:63], a[116:119], v[128:131]\n"
"s_waitcnt lgkmcnt(0) vmcnt(12)\n"
"s_barrier\n"
"v_mfma_f32_16x16x32_bf16 v[112:115], v[64:67], a[120:123], v[112:115]\n"
"ds_read_b64_tr_b16 v[56:57], v10 offset:49152\n"
"s_lshl_b32 s74, s72, 11\n"
"v_mfma_f32_16x16x32_bf16 v[116:119], v[64:67], a[124:127], v[116:119]\n"
"ds_read_b64_tr_b16 v[58:59], v10 offset:57344\n"
"v_cmp_ne_u64 vcc, s[88:89], v[44:45]\n"
"v_mfma_f32_16x16x32_bf16 v[120:123], v[64:67], a[128:131], v[120:123]\n"
"s_cbranch_vccnz .Lfz_rf5\n"
".Lfz_cf5:\n"
"s_add_u32 s79, s75, 51200\n"
"s_add_u32 s77, s76, 1536\n"
"buffer_load_dwordx2 v[44:45], off, s[64:67], s77 sc1\n"
"v_mfma_f32_16x16x32_bf16 v[124:127], v[64:67], a[132:135], v[124:127]\n"
"buffer_load_dwordx4 v[34:37], v48, s[64:67], s79 offen sc1\n"
"buffer_load_dwordx4 v[38:41], v48, s[64:67], s79 offen offset:1024 sc1\n"
"v_mfma_f32_16x16x32_bf16 v[128:131], v[64:67], a[136:139], v[128:131]\n"
"buffer_load_dwordx2 v[42:43], v49, s[64:67], s79 offen sc1\n"
"v_mfma_f32_16x16x32_bf16 v[112:115], v[68:71], a[140:143], v[112:115]\n"
"ds_read_b64_tr_b16 v[60:61], v10 offset:50176\n"
"v_mfma_f32_16x16x32_bf16 v[116:119], v[68:71], a[144:147], v[116:119]\n"
"ds_read_b64_tr_b16 v[62:63], v10 offset:58368\n"
"v_mfma_f32_16x16x32_bf16 v[120:123], v[68:71], a[148:151], v[120:123]\n"
"v_mfma_f32_16x16x32_bf16 v[124:127], v[68:71], a[152:155], v[124:127]\n"
"v_mfma_f32_16x16x32_bf16 v[128:131], v[68:71], a[156:159], v[128:131]\n"
"s_add_u32 s74, s74, s99\n"
"s_waitcnt lgkmcnt(2)\n"
"v_mfma_f32_16x16x32_bf16 v[112:115], v[56:59], a[160:163], v[112:115]\n"
"ds_read_b64_tr_b16 v[64:65], v10 offset:51200\n"
"v_mfma_f32_16x16x32_bf16 v[116:119], v[56:59], a[164:167], v[116:119]\n"
"ds_read_b64_tr_b16 v[66:67], v10 offset:59392\n"
"v_mfma_f32_16x16x32_bf16 v[120:123], v[56:59], a[168:171], v[120:123]\n"
"v_mfma_f32_16x16x32_bf16 v[124:127], v[56:59], a[172:175], v[124:127]\n"
"ds_read_b64_tr_b16 v[68:69], v10 offset:52224\n"
"v_mfma_f32_16x16x32_bf16 v[128:131], v[56:59], a[176:179], v[128:131]\n"
"ds_read_b64_tr_b16 v[70:71], v10 offset:60416\n"
"s_waitcnt lgkmcnt(4)\n"
"v_mfma_f32_16x16x32_bf16 v[112:115], v[60:63], a[180:183], v[112:115]\n"
"v_mfma_f32_16x16x32_bf16 v[116:119], v[60:63], a[184:187], v[116:119]\n"
"v_mfma_f32_16x16x32_bf16 v[120:123], v[60:63], a[188:191], v[120:123]\n"
"v_mfma_f32_16x16x32_bf16 v[124:127], v[60:63], a[192:195], v[124:127]\n"
"s_add_u32 s99, s99, 256\n"
"v_mfma_f32_16x16x32_bf16 v[128:131], v[60:63], a[196:199], v[128:131]\n"
"s_waitcnt lgkmcnt(0) vmcnt(11)\n"
"s_barrier\n"
"v_mfma_f32_16x16x32_bf16 v[112:115], v[64:67], a[200:203], v[112:115]\n"
"ds_read_b64_tr_b16 v[56:57], v10 offset:0\n"
"v_mfma_f32_16x16x32_bf16 v[116:119], v[64:67], a[204:207], v[116:119]\n"
"ds_read_b64_tr_b16 v[58:59], v10 offset:8192\n"
"v_mfma_f32_16x16x32_bf16 v[120:123], v[64:67], a[208:211], v[120:123]\n"
"v_mfma_f32_16x16x32_bf16 v[124:127], v[64:67], a[212:215], v[124:127]\n"
"v_mfma_f32_16x16x32_bf16 v[128:131], v[64:67], a[216:219], v[128:131]\n"
"v_mfma_f32_16x16x32_bf16 v[112:115], v[68:71], a[220:223], v[112:115]\n"
"ds_read_b64_tr_b16 v[60:61], v10 offset:1024\n"
"v_mfma_f32_16x16x32_bf16 v[116:119], v[68:71], a[224:227], v[116:119]\n"
"ds_read_b64_tr_b16 v[62:63], v10 offset:9216\n"
"v_mfma_f32_16x16x32_bf16 v[120:123], v[68:71], a[228:231], v[120:123]\n"
"v_mfma_f32_16x16x32_bf16 v[124:127], v[68:71], a[232:235], v[124:127]\n"
"v_mfma_f32_16x16x32_bf16 v[128:131], v[68:71], a[236:239], v[128:131]\n"
"s_waitcnt lgkmcnt(2)\n"
"v_mfma_f32_16x16x32_bf16 v[112:115], v[56:59], a[240:243], v[112:115]\n"
"ds_read_b64_tr_b16 v[64:65], v10 offset:2048\n"
"v_mfma_f32_16x16x32_bf16 v[116:119], v[56:59], a[244:247], v[116:119]\n"
"ds_read_b64_tr_b16 v[66:67], v10 offset:10240\n"
"s_waitcnt vmcnt(8)\n"
"buffer_store_dwordx2 v[46:47], off, s[64:67], s74 sc1\n"
"v_mfma_f32_16x16x32_bf16 v[120:123], v[56:59], a[248:251], v[120:123]\n"
"v_mfma_f32_16x16x32_bf16 v[124:127], v[56:59], a[252:255], v[124:127]\n"
"ds_read_b64_tr_b16 v[68:69], v10 offset:3072\n"
"v_mfma_f32_16x16x32_bf16 v[128:131], v[56:59], v[152:155], v[128:131]\n"
"ds_read_b64_tr_b16 v[70:71], v10 offset:11264\n"
"s_waitcnt lgkmcnt(4)\n"
"v_mfma_f32_16x16x32_bf16 v[112:115], v[60:63], v[156:159], v[112:115]\n"
"v_mfma_f32_16x16x32_bf16 v[116:119], v[60:63], v[160:163], v[116:119]\n"
"s_waitcnt vmcnt(4)\n"
"v_cmp_ne_u64 vcc, s[88:89], v[44:45]\n"
"s_cbranch_vccnz .Lfz_rf6\n"
".Lfz_cf6:\n"
"v_mfma_f32_16x16x32_bf16 v[120:123], v[60:63], v[164:167], v[120:123]\n"
"s_add_u32 s79, s75, 61440\n"
"buffer_load_dwordx4 v[142:145], v48, s[64:67], s79 offen sc1\n"
"v_mfma_f32_16x16x32_bf16 v[124:127], v[60:63], v[168:171], v[124:127]\n"
"buffer_load_dwordx4 v[146:149], v48, s[64:67], s79 offen offset:1024 sc1\n"
"buffer_load_dwordx2 v[150:151], v49, s[64:67], s79 offen sc1\n"
"v_mfma_f32_16x16x32_bf16 v[128:131], v[60:63], v[172:175], v[128:131]\n"
"s_waitcnt lgkmcnt(0)\n"
"s_barrier\n"
"v_mfma_f32_16x16x32_bf16 v[112:115], v[64:67], v[176:179], v[112:115]\n"
"ds_read_b64_tr_b16 v[56:57], v10 offset:16384\n"
"v_mfma_f32_16x16x32_bf16 v[116:119], v[64:67], v[180:183], v[116:119]\n"
"ds_read_b64_tr_b16 v[58:59], v10 offset:24576\n"
"v_mfma_f32_16x16x32_bf16 v[120:123], v[64:67], v[184:187], v[120:123]\n"
"v_mfma_f32_16x16x32_bf16 v[124:127], v[64:67], v[188:191], v[124:127]\n"
"v_mfma_f32_16x16x32_bf16 v[128:131], v[64:67], v[192:195], v[128:131]\n"
"v_mfma_f32_16x16x32_bf16 v[112:115], v[68:71], v[196:199], v[112:115]\n"
"ds_read_b64_tr_b16 v[60:61], v10 offset:17408\n"
"v_mfma_f32_16x16x32_bf16 v[116:119], v[68:71], v[200:203], v[116:119]\n"
"ds_read_b64_tr_b16 v[62:63], v10 offset:25600\n"
"v_mfma_f32_16x16x32_bf16 v[120:123], v[68:71], v[204:207], v[120:123]\n"
"v_mfma_f32_16x16x32_bf16 v[124:127], v[68:71], v[208:211], v[124:127]\n"
"v_mfma_f32_16x16x32_bf16 v[128:131], v[68:71], v[212:215], v[128:131]\n"
"s_waitcnt lgkmcnt(2)\n"
"v_mfma_f32_16x16x32_bf16 v[112:115], v[56:59], v[216:219], v[112:115]\n"
"ds_read_b64_tr_b16 v[64:65], v10 offset:18432\n"
"v_mfma_f32_16x16x32_bf16 v[116:119], v[56:59], v[220:223], v[116:119]\n"
"ds_read_b64_tr_b16 v[66:67], v10 offset:26624\n"
"v_mfma_f32_16x16x32_bf16 v[120:123], v[56:59], v[224:227], v[120:123]\n"
"v_mfma_f32_16x16x32_bf16 v[124:127], v[56:59], v[228:231], v[124:127]\n"
"ds_read_b64_tr_b16 v[68:69], v10 offset:19456\n"
"v_mfma_f32_16x16x32_bf16 v[128:131], v[56:59], v[232:235], v[128:131]\n"
"ds_read_b64_tr_b16 v[70:71], v10 offset:27648\n"
"s_waitcnt lgkmcnt(4)\n"
"v_mfma_f32_16x16x32_bf16 v[112:115], v[60:63], v[236:239], v[112:115]\n"
"v_mfma_f32_16x16x32_bf16 v[116:119], v[60:63], v[240:243], v[116:119]\n"
"v_mfma_f32_16x16x32_bf16 v[120:123], v[60:63], v[244:247], v[120:123]\n"
"v_mfma_f32_16x16x32_bf16 v[124:127], v[60:63], v[248:251], v[124:127]\n"
"v_mfma_f32_16x16x32_bf16 v[128:131], v[60:63], v[252:255], v[128:131]\n"
"s_waitcnt lgkmcnt(0)\n"
"s_barrier\n"
"v_mfma_f32_16x16x32_bf16 v[112:115], v[64:67], v[72:75], v[112:115]\n"
"v_mfma_f32_16x16x32_bf16 v[116:119], v[64:67], v[76:79], v[116:119]\n"
"v_mfma_f32_16x16x32_bf16 v[120:123], v[64:67], v[80:83], v[120:123]\n"
"s_waitcnt vmcnt(6)\n"
"v_mfma_f32_16x16x32_bf16 v[112:115], v[52:55], v[34:37], v[112:115]\n"
"v_mfma_f32_16x16x32_bf16 v[124:127], v[64:67], v[84:87], v[124:127]\n"
"v_mfma_f32_16x16x32_bf16 v[116:119], v[50:53], v[34:37], v[116:119]\n"
"v_mfma_f32_16x16x32_bf16 v[128:131], v[64:67], v[88:91], v[128:131]\n"
"s_waitcnt vmcnt(5)\n"
"v_mfma_f32_16x16x32_bf16 v[120:123], v[52:55], v[38:41], v[120:123]\n"
"v_mfma_f32_16x16x32_bf16 v[112:115], v[68:71], v[92:95], v[112:115]\n"
"v_mfma_f32_16x16x32_bf16 v[124:127], v[50:53], v[38:41], v[124:127]\n"
"v_mfma_f32_16x16x32_bf16 v[116:119], v[68:71], v[96:99], v[116:119]\n"
"s_waitcnt vmcnt(4)\n"
"v_mfma_f32_16x16x32_bf16 v[128:131], v[50:53], v[40:43], v[128:131]\n"
"v_mfma_f32_16x16x32_bf16 v[120:123], v[68:71], v[100:103], v[120:123]\n"
"v_mfma_f32_16x16x32_bf16 v[124:127], v[68:71], v[104:107], v[124:127]\n"
"s_add_u32 s78, s76, 1792\n"
"buffer_load_dwordx2 v[12:13], off, s[64:67], s78 sc1\n"
"v_mfma_f32_16x16x32_bf16 v[128:131], v[68:71], v[108:111], v[128:131]\n"
"s_waitcnt vmcnt(0)\n"
"v_cmp_ne_u64 vcc, s[88:89], v[12:13]\n"
"s_cbranch_vccnz .Lfz_rf7\n"
".Lfz_cf7:\n"
"s_add_u32 s79, s75, 71680\n"
"buffer_load_dwordx4 v[132:135], v48, s[64:67], s79 offen sc1\n"
"buffer_load_dwordx4 v[136:139], v48, s[64:67], s79 offen offset:1024 sc1\n"
"buffer_load_dwordx2 v[140:141], v49, s[64:67], s79 offen sc1\n"
"s_mov_b32 s98, 0x3ef5fdf0\n"
"s_mov_b32 s99, 0x3ef5fdf0\n"
"v_mfma_f32_16x16x32_bf16 v[112:115], v[52:55], v[142:145], v[112:115]\n"
"v_mfma_f32_16x16x32_bf16 v[116:119], v[50:53], v[142:145], v[116:119]\n"
"v_mfma_f32_16x16x32_bf16 v[120:123], v[52:55], v[146:149], v[120:123]\n"
"v_mfma_f32_16x16x32_bf16 v[124:127], v[50:53], v[146:149], v[124:127]\n"
"v_mfma_f32_16x16x32_bf16 v[128:131], v[50:53], v[148:151], v[128:131]\n"
"s_waitcnt vmcnt(2)\n"
"v_mfma_f32_16x16x32_bf16 v[112:115], v[52:55], v[132:135], v[112:115]\n"
"v_mfma_f32_16x16x32_bf16 v[116:119], v[50:53], v[132:135], v[116:119]\n"
"s_waitcnt vmcnt(1)\n"
"v_mfma_f32_16x16x32_bf16 v[120:123], v[52:55], v[136:139], v[120:123]\n"
"v_mfma_f32_16x16x32_bf16 v[124:127], v[50:53], v[136:139], v[124:127]\n"
"s_waitcnt vmcnt(0)\n"
"v_mfma_f32_16x16x32_bf16 v[128:131], v[50:53], v[138:141], v[128:131]\n"
"s_nop 1\n"
"v_pk_mul_f32 v[112:113], v[112:113], s[98:99]\n"
"v_pk_mul_f32 v[114:115], v[114:115], s[98:99]\n"
"ds_write_b128 v1, v[112:115] offset:0\n"
"ds_read_b128 v[14:17], v2 offset:0\n"
"v_pk_mul_f32 v[116:117], v[116:117], s[98:99]\n"
"v_pk_mul_f32 v[118:119], v[118:119], s[98:99]\n"
"ds_write_b128 v1, v[116:119] offset:1024\n"
"ds_read_b128 v[18:21], v2 offset:1024\n"
"v_pk_mul_f32 v[120:121], v[120:121], s[98:99]\n"
"v_pk_mul_f32 v[122:123], v[122:123], s[98:99]\n"
"ds_write_b128 v1, v[120:123] offset:2048\n"
"ds_read_b128 v[22:25], v2 offset:2048\n"
"v_pk_mul_f32 v[124:125], v[124:125], s[98:99]\n"
"v_pk_mul_f32 v[126:127], v[126:127], s[98:99]\n"
"ds_write_b128 v1, v[124:127] offset:3072\n"
"ds_read_b128 v[26:29], v2 offset:3072\n"
"v_pk_mul_f32 v[128:129], v[128:129], s[98:99]\n"
"v_pk_mul_f32 v[130:131], v[130:131], s[98:99]\n"
"ds_write_b128 v1, v[128:131] offset:4096\n"
"ds_read_b128 v[30:33], v2 offset:4096\n"
"s_waitcnt lgkmcnt(8)\n"
"buffer_store_dwordx4 v[14:17], v11, s[68:71], 0 offen\n"
"v_add_u32 v11, 9216, v11\n"
"s_waitcnt lgkmcnt(6)\n"
"buffer_store_dwordx4 v[18:21], v11, s[68:71], 0 offen\n"
"v_add_u32 v11, 9216, v11\n"
"s_waitcnt lgkmcnt(4)\n"
"buffer_store_dwordx4 v[22:25], v11, s[68:71], 0 offen\n"
"v_add_u32 v11, 9216, v11\n"
"s_waitcnt lgkmcnt(2)\n"
"buffer_store_dwordx4 v[26:29], v11, s[68:71], 0 offen\n"
"v_add_u32 v11, 9216, v11\n"
"s_waitcnt lgkmcnt(0)\n"
"buffer_store_dwordx4 v[30:33], v11, s[68:71], 0 offen\n"
"s_cmp_lg_u32 %[ti], 25\n"
"s_cbranch_scc1 .Lfz_end\n"
"s_cmp_lg_u32 %[sl], 0\n"
"s_cbranch_scc1 .Lfz_end\n"
"s_cmp_lg_u32 s95, 0\n"
"s_cbranch_scc1 .Lfz_end\n"
"s_add_u32 s96, s96, 1\n"
"v_mov_b32 v14, s96\n"
"buffer_store_dword v14, off, s[64:67], 0 sc1\n"
"s_branch .Lfz_end\n"
".Lfz_rg:\n"
"s_cmp_eq_u32 s94, 0\n"
"s_cbranch_scc1 .Lfz_cg\n"
"s_sub_u32 s94, s94, 1\n"
"s_sleep 1\n"
"v_subrev_u32 v26, 0, v54\n"
"v_max_i32 v26, v26, v19\n"
"buffer_load_dwordx4 v[26:29], v26, s[64:67], 0 offen sc1\n"
"v_subrev_u32 v30, 256, v54\n"
"v_max_i32 v30, v30, v19\n"
"buffer_load_dwordx4 v[30:33], v30, s[64:67], 0 offen sc1\n"
"v_subrev_u32 v34, 512, v54\n"
"v_max_i32 v34, v34, v19\n"
"buffer_load_dwordx4 v[34:37], v34, s[64:67], 0 offen sc1\n"
"v_subrev_u32 v38, 768, v54\n"
"v_max_i32 v38, v38, v19\n"
"buffer_load_dwordx4 v[38:41], v38, s[64:67], 0 offen sc1\n"
"v_subrev_u32 v42, 1024, v54\n"
"v_max_i32 v42, v42, v19\n"
"buffer_load_dwordx4 v[42:45], v42, s[64:67], 0 offen sc1\n"
"v_subrev_u32 v46, 1280, v54\n"
"v_max_i32 v46, v46, v19\n"
"buffer_load_dwordx4 v[46:49], v46, s[64:67], 0 offen sc1\n"
"v_subrev_u32 v50, 1536, v54\n"
"v_max_i32 v50, v50, v19\n"
"buffer_load_dwordx4 v[50:53], v50, s[64:67], 0 offen sc1\n"
"s_waitcnt vmcnt(6)\n"
"v_bitop3_b32 v27, v27, v29, s88 bitop3:0x7e\n"
"s_waitcnt vmcnt(5)\n"
"v_bitop3_b32 v31, v31, v33, s88 bitop3:0x7e\n"
"s_waitcnt vmcnt(4)\n"
"v_bitop3_b32 v35, v35, v37, s88 bitop3:0x7e\n"
"s_waitcnt vmcnt(3)\n"
"v_bitop3_b32 v39, v39, v41, s88 bitop3:0x7e\n"
"s_waitcnt vmcnt(2)\n"
"v_bitop3_b32 v43, v43, v45, s88 bitop3:0x7e\n"
"s_waitcnt vmcnt(1)\n"
"v_bitop3_b32 v47, v47, v49, s88 bitop3:0x7e\n"
"s_waitcnt vmcnt(0)\n"
"v_bitop3_b32 v51, v51, v53, s88 bitop3:0x7e\n"
"v_or3_b32 v27, v27, v31, v35\n"
"v_or3_b32 v39, v39, v43, v47\n"
"v_or3_b32 v27, v27, v39, v51\n"
"v_cmp_ne_u32 vcc, 0, v27\n"
"s_cbranch_vccnz .Lfz_rg\n"
"s_branch .Lfz_cg\n"
".Lfz_rf0:\n"
"s_cmp_eq_u32 s94, 0\n"
"s_cbranch_scc1 .Lfz_cf0\n"
"s_sub_u32 s94, s94, 1\n"
"s_sleep 1\n"
"buffer_load_dwordx2 v[44:45], off, s[64:67], s77 sc1\n"
"s_waitcnt vmcnt(0)\n"
"v_cmp_ne_u64 vcc, s[88:89], v[44:45]\n"
"s_cbranch_vccnz .Lfz_rf0\n"
"s_branch .Lfz_cf0\n"
".Lfz_rf1:\n"
"s_cmp_eq_u32 s94, 0\n"
"s_cbranch_scc1 .Lfz_cf1\n"
"s_sub_u32 s94, s94, 1\n"
"s_sleep 1\n"
"buffer_load_dwordx2 v[44:45], off, s[64:67], s77 sc1\n"
"s_waitcnt vmcnt(0)\n"
"v_cmp_ne_u64 vcc, s[88:89], v[44:45]\n"
"s_cbranch_vccnz .Lfz_rf1\n"
"s_branch .Lfz_cf1\n"
".Lfz_rf2:\n"
"s_cmp_eq_u32 s94, 0\n"
"s_cbranch_scc1 .Lfz_cf2\n"
"s_sub_u32 s94, s94, 1\n"
"s_sleep 1\n"
"buffer_load_dwordx2 v[44:45], off, s[64:67], s77 sc1\n"
"s_waitcnt vmcnt(0)\n"
"v_cmp_ne_u64 vcc, s[88:89], v[44:45]\n"
"s_cbranch_vccnz .Lfz_rf2\n"
"s_branch .Lfz_cf2\n"
".Lfz_rf3:\n"
"s_cmp_eq_u32 s94, 0\n"
"s_cbranch_scc1 .Lfz_cf3\n"
"s_sub_u32 s94, s94, 1\n"
"s_sleep 1\n"
"buffer_load_dwordx2 v[44:45], off, s[64:67], s77 sc1\n"
"s_waitcnt vmcnt(0)\n"
"v_cmp_ne_u64 vcc, s[88:89], v[44:45]\n"
"s_cbranch_vccnz .Lfz_rf3\n"
"s_branch .Lfz_cf3\n"
".Lfz_rf4:\n"
"s_cmp_eq_u32 s94, 0\n"
"s_cbranch_scc1 .Lfz_cf4\n"
"s_sub_u32 s94, s94, 1\n"
"s_sleep 1\n"
"buffer_load_dwordx2 v[44:45], off, s[64:67], s77 sc1\n"
"s_waitcnt vmcnt(0)\n"
"v_cmp_ne_u64 vcc, s[88:89], v[44:45]\n"
"s_cbranch_vccnz .Lfz_rf4\n"
"s_branch .Lfz_cf4\n"
".Lfz_rf5:\n"
"s_cmp_eq_u32 s94, 0\n"
"s_cbranch_scc1 .Lfz_cf5\n"
"s_sub_u32 s94, s94, 1\n"
"s_sleep 1\n"
"buffer_load_dwordx2 v[44:45], off, s[64:67], s77 sc1\n"
"s_waitcnt vmcnt(0)\n"
"v_cmp_ne_u64 vcc, s[88:89], v[44:45]\n"
"s_cbranch_vccnz .Lfz_rf5\n"
"s_branch .Lfz_cf5\n"
".Lfz_rf6:\n"
"s_cmp_eq_u32 s94, 0\n"
"s_cbranch_scc1 .Lfz_cf6\n"
"s_sub_u32 s94, s94, 1\n"
"s_sleep 1\n"
"buffer_load_dwordx2 v[44:45], off, s[64:67], s77 sc1\n"
"s_waitcnt vmcnt(0)\n"
"v_cmp_ne_u64 vcc, s[88:89], v[44:45]\n"
"s_cbranch_vccnz .Lfz_rf6\n"
"s_branch .Lfz_cf6\n"
".Lfz_rf7:\n"
"s_cmp_eq_u32 s94, 0\n"
"s_cbranch_scc1 .Lfz_cf7\n"
"s_sub_u32 s94, s94, 1\n"
"s_sleep 1\n"
"buffer_load_dwordx2 v[12:13], off, s[64:67], s78 sc1\n"
"s_waitcnt vmcnt(0)\n"
"v_cmp_ne_u64 vcc, s[88:89], v[12:13]\n"
"s_cbranch_vccnz .Lfz_rf7\n"
"s_branch .Lfz_cf7\n"
".Lfz_end:\n"
      : [tid] "+v"(tid)
      : [x] "s"(x), [lre] "s"(lre), [lim] "s"(lim), [bre] "s"(bre), [bim] "s"(bim), [cre] "s"(cre), [cim] "s"(cim), [ldt] "s"(ldt),
        [dsk] "s"(dsk), [w1] "s"(w1), [w3] "s"(w3), [w2] "s"(w2), [out] "s"(out), [ws] "s"(ws), [sl] "s"(sl), [ti] "s"(ti), [cs] "s"(cs),
        [lds] "s"((unsigned)(size_t)lds)
      : "v1","v2","v3","v4","v5","v6","v7","v8","v9","v10","v11","v12","v13","v14","v15","v16","v17","v18","v19","v20","v21","v22","v23","v24","v25","v26","v27","v28","v29","v30","v31","v32","v33","v34","v35","v36","v37","v38","v39","v40","v41","v42","v43","v44","v45","v46","v47","v48","v49","v50","v51","v52","v53","v54","v55","v56","v57","v58","v59","v60","v61","v62","v63","v64","v65","v66","v67","v68","v69","v70","v71","v72","v73","v74","v75","v76","v77","v78","v79","v80","v81","v82","v83","v84","v85","v86","v87","v88","v89","v90","v91","v92","v93","v94","v95","v96","v97","v98","v99","v100","v101","v102","v103","v104","v105","v106","v107","v108","v109","v110","v111","v112","v113","v114","v115","v116","v117","v118","v119","v120","v121","v122","v123","v124","v125","v126","v127","v128","v129","v130","v131","v132","v133","v134","v135","v136","v137","v138","v139","v140","v141","v142","v143","v144","v145","v146","v147","v148","v149","v150","v151","v152","v153","v154","v155","v156","v157","v158","v159","v160","v161","v162","v163","v164","v165","v166","v167","v168","v169","v170","v171","v172","v173","v174","v175","v176","v177","v178","v179","v180","v181","v182","v183","v184","v185","v186","v187","v188","v189","v190","v191","v192","v193","v194","v195","v196","v197","v198","v199","v200","v201","v202","v203","v204","v205","v206","v207","v208","v209","v210","v211","v212","v213","v214","v215","v216","v217","v218","v219","v220","v221","v222","v223","v224","v225","v226","v227","v228","v229","v230","v231","v232","v233","v234","v235","v236","v237","v238","v239","v240","v241","v242","v243","v244","v245","v246","v247","v248","v249","v250","v251","v252","v253","v254","v255","a0","a1","a2","a3","a4","a5","a6","a7","a8","a9","a10","a11","a12","a13","a14","a15","a16","a17","a18","a19","a20","a21","a22","a23","a24","a25","a26","a27","a28","a29","a30","a31","a32","a33","a34","a35","a36","a37","a38","a39","a40","a41","a42","a43","a44","a45","a46","a47","a48","a49","a50","a51","a52","a53","a54","a55","a56","a57","a58","a59","a60","a61","a62","a63","a64","a65","a66","a67","a68","a69","a70","a71","a72","a73","a74","a75","a76","a77","a78","a79","a80","a81","a82","a83","a84","a85","a86","a87","a88","a89","a90","a91","a92","a93","a94","a95","a96","a97","a98","a99","a100","a101","a102","a103","a104","a105","a106","a107","a108","a109","a110","a111","a112","a113","a114","a115","a116","a117","a118","a119","a120","a121","a122","a123","a124","a125","a126","a127","a128","a129","a130","a131","a132","a133","a134","a135","a136","a137","a138","a139","a140","a141","a142","a143","a144","a145","a146","a147","a148","a149","a150","a151","a152","a153","a154","a155","a156","a157","a158","a159","a160","a161","a162","a163","a164","a165","a166","a167","a168","a169","a170","a171","a172","a173","a174","a175","a176","a177","a178","a179","a180","a181","a182","a183","a184","a185","a186","a187","a188","a189","a190","a191","a192","a193","a194","a195","a196","a197","a198","a199","a200","a201","a202","a203","a204","a205","a206","a207","a208","a209","a210","a211","a212","a213","a214","a215","a216","a217","a218","a219","a220","a221","a222","a223","a224","a225","a226","a227","a228","a229","a230","a231","a232","a233","a234","a235","a236","a237","a238","a239","a240","a241","a242","a243","a244","a245","a246","a247","a248","a249","a250","a251","a252","a253","a254","a255","s32","s33","s34","s35","s36","s37","s38","s39","s40","s41","s42","s43","s44","s45","s46","s47","s48","s49","s50","s51","s52","s53","s54","s55","s56","s57","s58","s59","s60","s61","s62","s63","s64","s65","s66","s67","s68","s69","s70","s71","s72","s73","s74","s75","s76","s77","s78","s79","s80","s81","s82","s83","s84","s85","s86","s87","s88","s89","s90","s91","s92","s93","s94","s95","s96","s97","s98","s99", "vcc", "scc", "memory");
}

extern "C" void kernel_launch(void* const* d_in, const int* in_sizes, int n_in, void* d_out, int out_size,
                              void* d_ws, size_t ws_size, hipStream_t stream) {
  fused_kernel<<<256, 256, 0, stream>>>((const u16*)d_in[0], (const u16*)d_in[1], (const u16*)d_in[2], (const u16*)d_in[3],
                                        (const u16*)d_in[4], (const u16*)d_in[5], (const u16*)d_in[6], (const u16*)d_in[7],
                                        (const u16*)d_in[8], (const u16*)d_in[10], (const u16*)d_in[11], (const u16*)d_in[12],
                                        (float*)d_out, (char*)d_ws);
}
```
